```python
import torch

def _softmax(x, axis=-1):
    return torch.softmax(x, dim=axis)
def _gelu(x):
    return torch.nn.functional.gelu(x, approximate="tanh")
def _silu(x):
    return torch.nn.functional.silu(x)
def _sigmoid(x):
    return torch.sigmoid(x)
def _layernorm(x, axis=-1, eps=1e-5):
    m = x.mean(dim=axis, keepdim=True)
    v = x.var(dim=axis, keepdim=True, unbiased=False)
    return (x - m) / torch.sqrt(v + eps)
def _scatter_add(tgt, idx, src, axis=0):
    return tgt.clone().index_add_(axis, idx, src)
def _segment_sum(src, seg, n):
    out = torch.zeros((n,) + src.shape[1:], dtype=src.dtype)
    return out.index_add_(0, seg, src)
def _pad(x, pw):
    flat = tuple(v for pair in reversed(pw) for v in pair)
    return torch.nn.functional.pad(x, flat)

kernel_name = 'parallel_sum__linear_attn_block__geglu_ffn__prompt'

def setup_inputs():

    _w = (lambda a: (lambda t: t.to(torch.bfloat16) if t.is_floating_point() else t)(torch.as_tensor(a)))
    def _A_setup():
        B, S, D = 1, 2048, 576
        return {
            "x": _w((lambda *s: torch.randn(*s, dtype=torch.bfloat16) * 0.05)(B, S, D)),
            "wqkv": _w((lambda *s: torch.randn(*s, dtype=torch.bfloat16) * 0.4)(D, 3 * D)),
            "wo": _w((lambda *s: torch.randn(*s, dtype=torch.bfloat16) * 0.2)(D, D)),
        }
    _A_in = _A_setup()

    def _B_setup():
        B, S, D, F = 1, 2048, 576, 4608
        return {
            "x": _w((lambda *s: torch.randn(*s, dtype=torch.bfloat16) * 0.05)(B, S, D)),
            "w_gate": _w((lambda *s: torch.randn(*s, dtype=torch.bfloat16) * 0.05)(D, F)),
            "w_up": _w((lambda *s: torch.randn(*s, dtype=torch.bfloat16) * 0.05)(D, F)),
            "w_down": _w((lambda *s: torch.randn(*s, dtype=torch.bfloat16) * 0.05)(F, D)),
        }
    _B_in = _B_setup()

    return {
        "A_x": _A_in["x"],
        "A_wqkv": _A_in["wqkv"],
        "A_wo": _A_in["wo"],
        "B_x": _B_in["x"],
        "B_w_gate": _B_in["w_gate"],
        "B_w_up": _B_in["w_up"],
        "B_w_down": _B_in["w_down"],
    }

def reference(A_x, A_wqkv, A_wo, B_x, B_w_gate, B_w_up, B_w_down):
    def _A_ref(x, wqkv, wo):
        B, S, D = x.shape
        H = 32; Dh = D // H
        qkv = (lambda x, *d: x.permute(*d))((x @ wqkv).reshape(B, S, 3, H, Dh), 2, 0, 3, 1, 4)
        q, k, v = qkv[0], qkv[1], qkv[2]
        phi = lambda t: torch.where(t > 0, t + 1, torch.exp(t)) + 1e-6
        pq, pk = phi(q), phi(k)
        kv = torch.einsum("bhsd,bhse->bhde", pk, v)
        z = torch.einsum("bhsd->bhd", pk)
        num = torch.einsum("bhqd,bhde->bhqe", pq, kv)
        den = torch.einsum("bhqd,bhd->bhq", pq, z)[..., None] + 1e-6
        o = num / den
        return (lambda x, *d: x.permute(*d))(o, 0, 2, 1, 3).reshape(B, S, D) @ wo

    def _B_ref(x, w_gate, w_up, w_down):
        return (_gelu(x @ w_gate) * (x @ w_up)) @ w_down

    x = A_x
    xn = _layernorm(x, axis=-1)
    return x + _A_ref(xn, A_wqkv, A_wo) + _B_ref(xn, B_w_gate, B_w_up, B_w_down)
```

```cpp
#include <hip/hip_runtime.h>
#include <hip/hip_bf16.h>
#include <stdint.h>
typedef unsigned short u16;
constexpr int S=2048, D=576, F=4608, H=32, DH=18;
__device__ __forceinline__ float bf2f(u16 v){ return __uint_as_float(((unsigned)v)<<16); }
__device__ __forceinline__ u16 f2bf(float f){ __hip_bfloat16 b = __float2bfloat16(f); return *(u16*)&b; }

__global__ void ln_k(const u16* x, u16* xn){
  int row = blockIdx.x; int t = threadIdx.x;
  float v[9]; float s=0;
  for(int i=0;i<9;i++){ v[i]=bf2f(x[row*D+t+64*i]); s+=v[i]; }
  for(int o=32;o;o>>=1) s+=__shfl_xor(s,o);
  float m=s/D; float q=0;
  for(int i=0;i<9;i++){ float d=v[i]-m; q+=d*d; }
  for(int o=32;o;o>>=1) q+=__shfl_xor(q,o);
  float r=rsqrtf(q/D+1e-5f);
  for(int i=0;i<9;i++) xn[row*D+t+64*i]=f2bf((v[i]-m)*r);
}
__global__ void gemm_k(const u16* A, const u16* B, float* C, int M, int N, int K, int lda, int accumulate){
  __shared__ float As[16][65]; __shared__ float Bs[16][65];
  int bm=blockIdx.y*64, bn=blockIdx.x*64; int t=threadIdx.x; int tr=t/16, tc=t%16;
  float acc[4][4]={};
  for(int k0=0;k0<K;k0+=16){
    for(int i=t;i<64*16;i+=256){ int r=i/16,c=i%16; As[c][r]=bf2f(A[(size_t)(bm+r)*lda+k0+c]); }
    for(int i=t;i<64*16;i+=256){ int r=i/64,c=i%64; Bs[r][c]= (bn+c<N)? bf2f(B[(size_t)(k0+r)*N+bn+c]):0.f; }
    __syncthreads();
    for(int k=0;k<16;k++){ float a[4],b[4]; for(int i=0;i<4;i++){a[i]=As[k][tr*4+i]; b[i]=Bs[k][tc*4+i];}
      for(int i=0;i<4;i++)for(int j=0;j<4;j++) acc[i][j]+=a[i]*b[j]; }
    __syncthreads();
  }
  for(int i=0;i<4;i++)for(int j=0;j<4;j++){ int r=bm+tr*4+i,c=bn+tc*4+j; if(c<N){ if(accumulate) C[(size_t)r*N+c]+=acc[i][j]; else C[(size_t)r*N+c]=acc[i][j]; } }
}
__device__ __forceinline__ float phi(float t){ return (t>0.f? t+1.f: __expf(t))+1e-6f; }
__global__ void kv_k(const float* qkv, float* kv){
  int h=blockIdx.x; int t=threadIdx.x; if(t>=342) return;
  int d=t/19, e=t%19; float s=0;
  for(int r=0;r<S;r++){ float pk=bf2f(f2bf(phi(qkv[(size_t)r*1728+576+h*18+d]))); float v= e<18? bf2f(f2bf(qkv[(size_t)r*1728+1152+h*18+e])):1.f; s+=pk*v; }
  kv[h*342+t]=s;
}
__global__ void o_k(const float* qkv, const float* kv, u16* o){
  int r=blockIdx.x; int c=threadIdx.x; int h=c/18,e=c%18; float num=0,den=0;
  for(int d=0;d<18;d++){ float pq=bf2f(f2bf(phi(qkv[(size_t)r*1728+h*18+d]))); num+=pq*bf2f(f2bf(kv[h*342+d*19+e])); den+=pq*bf2f(f2bf(kv[h*342+d*19+18])); }
  o[(size_t)r*576+c]=f2bf(num/(den+1e-6f));
}
__global__ void h_k(const float* G, const float* U, u16* Hh){ size_t i=(size_t)blockIdx.x*256+threadIdx.x; float g=G[i]; float y=g*(1.5957691216f+0.0713548163f*g*g); float s=1.f/(1.f+__expf(-y)); Hh[i]=f2bf(g*s*U[i]); }
__global__ void fin_k(const u16* x, const float* Y, u16* out){ size_t i=(size_t)blockIdx.x*256+threadIdx.x; out[i]=f2bf(bf2f(x[i])+Y[i]); }

extern "C" void kernel_launch(void* const* d_in, const int* in_sizes, int n_in, void* d_out, int out_size, void* d_ws, size_t ws_size, hipStream_t stream){
  const u16* x=(const u16*)d_in[0]; const u16* wqkv=(const u16*)d_in[1]; const u16* wo=(const u16*)d_in[2];
  const u16* wg=(const u16*)d_in[4]; const u16* wu=(const u16*)d_in[5]; const u16* wd=(const u16*)d_in[6];
  char* ws=(char*)d_ws;
  u16* xn=(u16*)ws; ws+=S*D*2;
  float* qkv=(float*)ws; ws+=(size_t)S*1728*4;
  float* kv=(float*)ws; ws+=65536;
  u16* o=(u16*)ws; ws+=S*D*2;
  float* G=(float*)ws; ws+=(size_t)S*F*4;
  float* U=(float*)ws; ws+=(size_t)S*F*4;
  u16* Hh=(u16*)ws; ws+=(size_t)S*F*2;
  float* Y=(float*)ws; ws+=(size_t)S*D*4;
  ln_k<<<S,64,0,stream>>>(x,xn);
  gemm_k<<<dim3(27,32),256,0,stream>>>(xn,wqkv,qkv,S,1728,576,576,0);
  kv_k<<<32,384,0,stream>>>(qkv,kv);
  o_k<<<S,576,0,stream>>>(qkv,kv,o);
  gemm_k<<<dim3(72,32),256,0,stream>>>(xn,wg,G,S,F,576,576,0);
  gemm_k<<<dim3(72,32),256,0,stream>>>(xn,wu,U,S,F,576,576,0);
  h_k<<<S*F/256,256,0,stream>>>(G,U,Hh);
  gemm_k<<<dim3(9,32),256,0,stream>>>(Hh,wd,Y,S,576,F,F,0);
  gemm_k<<<dim3(9,32),256,0,stream>>>(o,wo,Y,S,576,576,576,1);
  fin_k<<<S*D/256,256,0,stream>>>(x,Y,(u16*)d_out);
}
```

```cpp
#include <hip/hip_runtime.h>
#include <hip/hip_bf16.h>
#include <stdint.h>

__global__ __launch_bounds__(256, 1) void fused_kernel(const void* x, const void* wq, const void* wo, const void* wg,
                                                       const void* wu, const void* wd, void* out, void* ws) {
  __shared__ __attribute__((aligned(16))) char smem[163840];
  const unsigned wave = __builtin_amdgcn_readfirstlane(threadIdx.x >> 6);
  const unsigned bid = blockIdx.x;
  const unsigned lds = (unsigned)(size_t)smem;
  const unsigned long long karg = (unsigned long long)__builtin_amdgcn_kernarg_segment_ptr();
  asm volatile(
"s_load_dwordx16 s[84:99], %[karg], 0\n"
"s_mov_b32 s60, %[wave]\n"
"s_and_b32 s66, %[bid], 7\n"
"s_lshr_b32 s67, %[bid], 3\n"
"s_and_b32 s68, s67, 1\n"
"s_lshl_b32 s63, s66, 1\n"
"s_add_u32 s63, s63, s68\n"
"s_lshr_b32 s64, s67, 1\n"
"s_lshr_b32 s61, s60, 1\n"
"s_and_b32 s62, s60, 1\n"
"s_lshl_b32 s65, s60, 12\n"
"s_add_u32 s65, s65, %[lds]\n"
"v_mbcnt_lo_u32_b32 v255, -1, 0\n"
"v_mbcnt_hi_u32_b32 v255, -1, v255\n"
"v_lshlrev_b32 v158, 4, v255\n"
"s_lshl_b32 s12, s64, 4\n"
"s_add_u32 s12, s12, s63\n"
"s_cmp_eq_u32 s60, 0\n"
"s_cselect_b64 s[80:81], -1, 0\n"
"v_mov_b32 v62, 1879048192\n"
"v_and_b32 v64, 7, v255\n"
"v_lshrrev_b32 v65, 3, v255\n"
"v_lshlrev_b32 v187, 7, v255\n"
"s_lshl_b32 s13, s64, 4\n"
"v_add_u32 v58, s13, v255\n"
"v_lshlrev_b32 v58, 5, v58\n"
"v_add_u32 v58, s63, v58\n"
"v_subrev_u32 v59, 16, v255\n"
"v_lshlrev_b32 v59, 4, v59\n"
"v_add_u32 v59, s63, v59\n"
"v_lshlrev_b32 v59, 5, v59\n"
"s_add_u32 s13, s64, 16\n"
"v_add_u32 v59, s13, v59\n"
"v_cmp_gt_u32 vcc, 16, v255\n"
"s_nop 1\n"
"v_cndmask_b32 v183, v59, v58, vcc\n"
"v_lshlrev_b32 v183, 7, v183\n"
"v_cmp_gt_u32 vcc, 32, v255\n"
"s_nop 0\n"
"s_and_b64 vcc, vcc, s[80:81]\n"
"v_cndmask_b32 v183, v62, v183, vcc\n"
"v_cndmask_b32 v187, v62, v187, vcc\n"
"s_lshl_b32 s13, s64, 7\n"
"s_lshl_b32 s14, s63, 3\n"
"s_add_u32 s13, s13, s14\n"
"v_add_u32 v58, s13, v64\n"
"s_movk_i32 s14, 1152\n"
"v_mul_lo_u32 v70, s14, v58\n"
"v_lshrrev_b32 v58, 2, v65\n"
"s_lshl_b32 s13, s60, 1\n"
"v_add_u32 v58, s13, v58\n"
"v_and_b32 v59, 3, v65\n"
"v_lshl_add_u32 v68, v58, 6, v70\n"
"v_lshl_add_u32 v68, v59, 3, v68\n"
"v_cndmask_b32_e64 v69, v62, v68, s[80:81]\n"
"s_mul_i32 s13, s64, 144\n"
"s_lshr_b32 s14, s63, 1\n"
"s_add_u32 s13, s13, s14\n"
"s_lshl_b32 s13, s13, 10\n"
"s_and_b32 s14, s63, 1\n"
"s_lshl_b32 s14, s14, 7\n"
"s_add_u32 s13, s13, s14\n"
"v_lshl_add_u32 v59, v59, 4, v64\n"
"v_lshlrev_b32 v59, 4, v59\n"
"v_lshl_add_u32 v76, v58, 13, v59\n"
"v_add_u32 v76, s13, v76\n"
"v_cndmask_b32_e64 v77, v62, v76, s[80:81]\n"
"v_cndmask_b32_e64 v78, v62, v158, s[80:81]\n"
"v_bfe_u32 v58, v255, 4, 2\n"
"v_lshlrev_b32 v58, 1, v58\n"
"v_xor_b32 v58, v58, v64\n"
"v_lshrrev_b32 v66, 1, v58\n"
"v_and_b32 v67, 1, v58\n"
"v_and_b32 v58, 1, v66\n"
"v_cmp_eq_u32 vcc, 1, v58\n"
"s_nop 0\n"
"s_mov_b64 s[82:83], vcc\n"
"v_mul_u32_u24 v58, 576, v58\n"
"v_lshrrev_b32 v59, 1, v66\n"
"v_lshl_add_u32 v58, v59, 4, v58\n"
"v_lshl_add_u32 v58, v67, 3, v58\n"
"v_add_u32 v58, 576, v58\n"
"v_mul_u32_u24 v60, 3456, v65\n"
"v_lshl_add_u32 v71, v58, 1, v60\n"
"v_add_u32 v58, v66, v67\n"
"v_lshlrev_b32 v58, 3, v58\n"
"v_mul_u32_u24 v59, 576, v67\n"
"v_add_u32 v59, 604, v59\n"
"v_cmp_eq_u32 vcc, 1, v66\n"
"s_nop 1\n"
"v_cndmask_b32 v58, v58, v59, vcc\n"
"v_mov_b32 v59, 28\n"
"v_cmp_eq_u32 vcc, 3, v66\n"
"s_nop 1\n"
"v_cndmask_b32 v58, v58, v59, vcc\n"
"v_lshl_add_u32 v72, v58, 1, v60\n"
"s_add_u32 s13, s60, 0\n"
"s_min_u32 s13, s13, 8\n"
"s_mul_i32 s14, s64, 9\n"
"s_add_u32 s17, s13, s14\n"
"s_lshr_b32 s13, s17, 3\n"
"s_lshl_b32 s13, s13, 5\n"
"s_and_b32 s14, s17, 3\n"
"s_lshl_b32 s14, s14, 3\n"
"s_add_u32 s13, s13, s14\n"
"s_mul_i32 s13, s13, 3456\n"
"s_mul_i32 s14, s63, 72\n"
"s_add_u32 s24, s13, s14\n"
"s_bitcmp1_b32 s17, 2\n"
"s_cselect_b64 s[10:11], -1, 0\n"
"v_cndmask_b32_e64 v73, v71, v72, s[10:11]\n"
"s_add_u32 s13, s60, 4\n"
"s_min_u32 s13, s13, 8\n"
"s_mul_i32 s14, s64, 9\n"
"s_add_u32 s18, s13, s14\n"
"s_lshr_b32 s13, s18, 3\n"
"s_lshl_b32 s13, s13, 5\n"
"s_and_b32 s14, s18, 3\n"
"s_lshl_b32 s14, s14, 3\n"
"s_add_u32 s13, s13, s14\n"
"s_mul_i32 s13, s13, 3456\n"
"s_mul_i32 s14, s63, 72\n"
"s_add_u32 s25, s13, s14\n"
"s_bitcmp1_b32 s18, 2\n"
"s_cselect_b64 s[10:11], -1, 0\n"
"v_cndmask_b32_e64 v74, v71, v72, s[10:11]\n"
"s_add_u32 s13, s60, 8\n"
"s_min_u32 s13, s13, 8\n"
"s_mul_i32 s14, s64, 9\n"
"s_add_u32 s19, s13, s14\n"
"s_lshr_b32 s13, s19, 3\n"
"s_lshl_b32 s13, s13, 5\n"
"s_and_b32 s14, s19, 3\n"
"s_lshl_b32 s14, s14, 3\n"
"s_add_u32 s13, s13, s14\n"
"s_mul_i32 s13, s13, 3456\n"
"s_mul_i32 s14, s63, 72\n"
"s_add_u32 s26, s13, s14\n"
"s_bitcmp1_b32 s19, 2\n"
"s_cselect_b64 s[10:11], -1, 0\n"
"v_cndmask_b32_e64 v75, v71, v72, s[10:11]\n"
"s_lshl_b32 s13, s12, 7\n"
"s_add_u32 s14, s13, 49152\n"
"s_add_u32 s13, s13, 16384\n"
"v_mov_b32 v79, v62\n"
"v_mov_b32 v58, s13\n"
"v_cmp_eq_u32 vcc, 32, v255\n"
"s_nop 1\n"
"v_cndmask_b32 v79, v79, v58, vcc\n"
"v_mov_b32 v58, s14\n"
"v_cmp_eq_u32 vcc, 33, v255\n"
"s_nop 1\n"
"v_cndmask_b32 v79, v79, v58, vcc\n"
"s_lshl_b32 s13, s63, 8\n"
"s_add_u32 s14, s13, 4096\n"
"s_cmp_eq_u32 s64, 0\n"
"s_cselect_b32 s13, s13, 1879048192\n"
"s_cselect_b32 s14, s14, 1879048192\n"
"v_mov_b32 v58, s13\n"
"v_cmp_eq_u32 vcc, 34, v255\n"
"s_nop 1\n"
"v_cndmask_b32 v79, v79, v58, vcc\n"
"v_mov_b32 v58, s14\n"
"v_cmp_eq_u32 vcc, 35, v255\n"
"s_nop 1\n"
"v_cndmask_b32 v79, v79, v58, vcc\n"
"s_lshl_b32 s13, s64, 1\n"
"s_add_u32 s13, s13, s63\n"
"s_lshl_b32 s13, s13, 8\n"
"s_add_u32 s13, s13, 8192\n"
"s_cmp_lt_u32 s63, 2\n"
"s_cselect_b32 s13, s13, 1879048192\n"
"v_mov_b32 v58, s13\n"
"v_cmp_eq_u32 vcc, 36, v255\n"
"s_nop 1\n"
"v_cndmask_b32 v79, v79, v58, vcc\n"
"v_cndmask_b32_e64 v79, v62, v79, s[80:81]\n"
"s_waitcnt lgkmcnt(0)\n"
"s_mov_b32 s48, s98\n"
"s_and_b32 s49, s99, 65535\n"
"s_mov_b32 s50, 55574528\n"
"s_mov_b32 s51, 131072\n"
"s_lshl_b32 s13, s12, 12\n"
"s_add_u32 s27, s13, 53477376\n"
"s_add_u32 s9, s13, 54525952\n"
"buffer_load_dword v13, v187, s[48:51], s27 offen sc1\n"
"s_mov_b32 s52, s84\n"
"s_and_b32 s53, s85, 65535\n"
"s_mov_b32 s54, 2359296\n"
"s_mov_b32 s55, 131072\n"
"buffer_load_dwordx2 v[0:1], v68, s[52:55], 0 offen\n"
"buffer_load_dwordx2 v[2:3], v68, s[52:55], 0 offen offset:32\n"
"buffer_load_dwordx2 v[4:5], v68, s[52:55], 0 offen offset:512\n"
"buffer_load_dwordx2 v[6:7], v68, s[52:55], 0 offen offset:544\n"
"buffer_load_dwordx2 v[8:9], v69, s[52:55], 0 offen offset:1024\n"
"buffer_load_dwordx2 v[10:11], v69, s[52:55], 0 offen offset:1056\n"
"buffer_load_dword v12, v70, s[52:55], 0 offen\n"
"s_mov_b32 s20, s86\n"
"s_and_b32 s21, s87, 65535\n"
"s_mov_b32 s22, 1990656\n"
"s_mov_b32 s23, 131072\n"
"buffer_load_dwordx4 v[16:19], v73, s[20:23], s24 offen\n"
"buffer_load_dwordx4 v[20:23], v74, s[20:23], s25 offen\n"
"buffer_load_dwordx4 v[24:27], v75, s[20:23], s26 offen\n"
"s_getpc_b64 s[40:41]\n"
"Lpc_%=:\n"
"s_and_b32 s41, s41, 65535\n"
"s_mov_b32 s42, Lexit_%=-Lpc_%=\n"
"s_mov_b32 s43, 131072\n"
"s_lshr_b32 s13, %[bid], 3\n"
"s_lshl_b32 s13, s13, 2\n"
"s_add_u32 s13, s13, s60\n"
"s_lshl_b32 s13, s13, 9\n"
"v_lshlrev_b32 v58, 7, v255\n"
"v_cmp_gt_u32 vcc, 4, v255\n"
"s_nop 1\n"
"v_cndmask_b32 v58, v62, v58, vcc\n"
"buffer_load_dword v14, v58, s[40:43], s13 offen\n"
"s_sub_u32 s28, s90, 3072\n"
"s_subb_u32 s29, s91, 0\n"
"s_and_b32 s29, s29, 65535\n"
"s_mov_b32 s30, 5311488\n"
"s_mov_b32 s31, 131072\n"
"s_sub_u32 s36, s92, 3072\n"
"s_subb_u32 s37, s93, 0\n"
"s_and_b32 s37, s37, 65535\n"
"s_mov_b32 s38, 5311488\n"
"s_mov_b32 s39, 131072\n"
"s_sub_u32 s40, s94, 3072\n"
"s_subb_u32 s41, s95, 0\n"
"s_and_b32 s41, s41, 65535\n"
"s_mov_b32 s42, 5311488\n"
"s_mov_b32 s43, 131072\n"
"s_sub_u32 s44, s88, 3072\n"
"s_subb_u32 s45, s89, 0\n"
"s_and_b32 s45, s45, 65535\n"
"s_mov_b32 s46, 666624\n"
"s_mov_b32 s47, 131072\n"
"s_mov_b32 s56, s96\n"
"s_and_b32 s57, s97, 65535\n"
"s_mov_b32 s58, 2359296\n"
"s_mov_b32 s59, 131072\n"
"v_lshrrev_b32 v58, 1, v66\n"
"v_lshlrev_b32 v58, 5, v58\n"
"v_lshl_add_u32 v58, v67, 4, v58\n"
"v_mul_u32_u24 v59, 9216, v65\n"
"v_add_u32 v59, v59, v58\n"
"v_cndmask_b32_e64 v80, v59, v62, s[82:83]\n"
"v_cndmask_b32_e64 v81, v62, v59, s[82:83]\n"
"v_mul_u32_u24 v82, 1152, v65\n"
"v_add_u32 v82, v82, v58\n"
"s_lshl_b32 s74, s60, 4\n"
"s_add_u32 s74, s74, s64\n"
"s_add_u32 s75, s74, 64\n"
"s_and_b32 s13, s63, 1\n"
"s_lshl_b32 s13, s13, 6\n"
"s_add_u32 s73, s13, 3584\n"
"s_lshr_b32 s13, s74, 2\n"
"s_lshl_b32 s13, s13, 5\n"
"s_and_b32 s14, s74, 3\n"
"s_lshl_b32 s14, s14, 3\n"
"s_add_u32 s13, s13, s14\n"
"s_mul_i32 s13, s13, 9216\n"
"s_lshr_b32 s14, s63, 1\n"
"s_mul_i32 s14, s14, 1152\n"
"s_add_u32 s13, s13, s14\n"
"s_add_u32 s69, s13, s73\n"
"s_lshr_b32 s13, s75, 2\n"
"s_lshl_b32 s13, s13, 5\n"
"s_and_b32 s14, s75, 3\n"
"s_lshl_b32 s14, s14, 3\n"
"s_add_u32 s13, s13, s14\n"
"s_mul_i32 s13, s13, 9216\n"
"s_lshr_b32 s14, s63, 1\n"
"s_mul_i32 s14, s14, 1152\n"
"s_add_u32 s13, s13, s14\n"
"s_add_u32 s70, s13, s73\n"
"s_mul_i32 s13, s63, 72\n"
"s_add_u32 s14, s13, s74\n"
"s_lshl_b32 s14, s14, 10\n"
"s_add_u32 s71, s14, 4915200\n"
"s_add_u32 s14, s13, s75\n"
"s_lshl_b32 s14, s14, 10\n"
"s_add_u32 s72, s14, 4915200\n"
"s_sub_u32 s76, s75, 72\n"
"s_max_i32 s76, s76, 0\n"
"s_min_u32 s76, s76, 43\n"
"s_mul_i32 s13, s63, 44\n"
"s_add_u32 s13, s13, s76\n"
"s_lshl_b32 s13, s13, 10\n"
"s_add_u32 s13, s13, 6094848\n"
"s_cmp_lt_u32 s75, 72\n"
"s_cselect_b32 s72, s72, s13\n"
"s_cselect_b32 s70, s70, 1879048192\n"
"s_cmp_lt_u32 s75, 116\n"
"s_cselect_b32 s72, s72, 1879048192\n"
"s_lshr_b32 s13, s76, 2\n"
"s_lshl_b32 s13, s13, 1\n"
"s_add_u32 s13, s13, 0\n"
"s_xor_b32 s14, s63, 1\n"
"s_sub_u32 s15, s13, 11\n"
"s_cmp_lt_u32 s13, 11\n"
"s_cselect_b32 s14, s63, s14\n"
"s_cselect_b32 s15, s13, s15\n"
"s_add_u32 s16, s15, 3\n"
"s_sub_u32 s13, s15, 2\n"
"s_cmp_lt_u32 s15, 8\n"
"s_cselect_b32 s16, s16, s13\n"
"s_cmp_lt_u32 s15, 6\n"
"s_cselect_b32 s16, s15, s16\n"
"s_lshr_b32 s13, s14, 1\n"
"s_mul_i32 s13, s13, 576\n"
"s_and_b32 s15, s14, 1\n"
"s_mul_i32 s85, s15, 320\n"
"s_add_u32 s85, s85, s13\n"
"s_lshl_b32 s87, s16, 5\n"
"s_add_u32 s85, s85, s87\n"
"s_lshl_b32 s15, s15, 5\n"
"s_add_u32 s13, s13, s15\n"
"s_add_u32 s13, s13, 256\n"
"s_cmp_eq_u32 s16, 8\n"
"s_cselect_b32 s85, s13, s85\n"
"s_mul_i32 s13, s14, 36\n"
"s_add_u32 s15, s13, 20\n"
"s_cmp_eq_u32 s16, 10\n"
"s_cselect_b32 s13, s15, s13\n"
"s_cmp_gt_u32 s16, 8\n"
"s_cselect_b32 s85, s13, s85\n"
"s_and_b32 s13, s76, 3\n"
"s_lshl_b32 s13, s13, 3\n"
"s_add_u32 s85, s85, s13\n"
"s_mul_i32 s85, s85, 1152\n"
"s_add_u32 s85, s85, s73\n"
"s_cmp_lt_u32 s75, 72\n"
"s_cselect_b32 s85, 1879048192, s85\n"
"s_cmp_lt_u32 s16, 9\n"
"s_cselect_b32 s77, s85, 1879048192\n"
"s_cselect_b32 s79, 1879048192, s85\n"
"s_lshr_b32 s13, s76, 2\n"
"s_lshl_b32 s13, s13, 1\n"
"s_add_u32 s13, s13, 1\n"
"s_xor_b32 s14, s63, 1\n"
"s_sub_u32 s15, s13, 11\n"
"s_cmp_lt_u32 s13, 11\n"
"s_cselect_b32 s14, s63, s14\n"
"s_cselect_b32 s15, s13, s15\n"
"s_add_u32 s16, s15, 3\n"
"s_sub_u32 s13, s15, 2\n"
"s_cmp_lt_u32 s15, 8\n"
"s_cselect_b32 s16, s16, s13\n"
"s_cmp_lt_u32 s15, 6\n"
"s_cselect_b32 s16, s15, s16\n"
"s_lshr_b32 s13, s14, 1\n"
"s_mul_i32 s13, s13, 576\n"
"s_and_b32 s15, s14, 1\n"
"s_mul_i32 s85, s15, 320\n"
"s_add_u32 s85, s85, s13\n"
"s_lshl_b32 s87, s16, 5\n"
"s_add_u32 s85, s85, s87\n"
"s_lshl_b32 s15, s15, 5\n"
"s_add_u32 s13, s13, s15\n"
"s_add_u32 s13, s13, 256\n"
"s_cmp_eq_u32 s16, 8\n"
"s_cselect_b32 s85, s13, s85\n"
"s_mul_i32 s13, s14, 36\n"
"s_add_u32 s15, s13, 20\n"
"s_cmp_eq_u32 s16, 10\n"
"s_cselect_b32 s13, s15, s13\n"
"s_cmp_gt_u32 s16, 8\n"
"s_cselect_b32 s85, s13, s85\n"
"s_and_b32 s13, s76, 3\n"
"s_lshl_b32 s13, s13, 3\n"
"s_add_u32 s85, s85, s13\n"
"s_mul_i32 s85, s85, 1152\n"
"s_add_u32 s85, s85, s73\n"
"s_cmp_lt_u32 s75, 72\n"
"s_cselect_b32 s85, 1879048192, s85\n"
"s_cmp_lt_u32 s16, 9\n"
"s_cselect_b32 s78, s85, 1879048192\n"
"s_cselect_b32 s84, 1879048192, s85\n"
"v_mov_b32 v58, s77\n"
"v_mov_b32 v59, s78\n"
"v_cndmask_b32_e64 v58, v58, v59, s[82:83]\n"
"v_add_u32 v83, v58, v82\n"
"v_mov_b32 v58, s79\n"
"v_mov_b32 v59, s84\n"
"v_cndmask_b32_e64 v58, v58, v59, s[82:83]\n"
"v_add_u32 v84, v58, v82\n"
"s_waitcnt vmcnt(11)\n"
"v_add_u32 v15, 1, v13\n"
"s_mov_b32 s13, 54525952\n"
"buffer_store_dword v15, v183, s[48:51], s13 offen sc1\n"
"v_mov_b32 v58, 0\n"
"buffer_store_dword v58, v79, s[48:51], 0 offen sc1\n"
"s_waitcnt vmcnt(6)\n"
"v_lshlrev_b32 v28, 16, v12\n"
"v_cndmask_b32_e64 v29, 0, v28, s[80:81]\n"
"v_lshlrev_b32 v32, 16, v0\n"
"v_and_b32 v33, 4294901760, v0\n"
"v_lshlrev_b32 v34, 16, v1\n"
"v_and_b32 v35, 4294901760, v1\n"
"v_lshlrev_b32 v36, 16, v2\n"
"v_and_b32 v37, 4294901760, v2\n"
"v_lshlrev_b32 v38, 16, v3\n"
"v_and_b32 v39, 4294901760, v3\n"
"v_lshlrev_b32 v40, 16, v4\n"
"v_and_b32 v41, 4294901760, v4\n"
"v_lshlrev_b32 v42, 16, v5\n"
"v_and_b32 v43, 4294901760, v5\n"
"v_lshlrev_b32 v44, 16, v6\n"
"v_and_b32 v45, 4294901760, v6\n"
"v_lshlrev_b32 v46, 16, v7\n"
"v_and_b32 v47, 4294901760, v7\n"
"v_lshlrev_b32 v48, 16, v8\n"
"v_and_b32 v49, 4294901760, v8\n"
"v_lshlrev_b32 v50, 16, v9\n"
"v_and_b32 v51, 4294901760, v9\n"
"v_lshlrev_b32 v52, 16, v10\n"
"v_and_b32 v53, 4294901760, v10\n"
"v_lshlrev_b32 v54, 16, v11\n"
"v_and_b32 v55, 4294901760, v11\n"
"v_sub_f32 v58, v32, v28\n"
"v_mov_b32 v56, v58\n"
"v_mul_f32 v57, v58, v58\n"
"v_sub_f32 v59, v33, v28\n"
"v_mov_b32 v30, v59\n"
"v_mul_f32 v31, v59, v59\n"
"v_sub_f32 v58, v34, v28\n"
"v_add_f32 v56, v56, v58\n"
"v_fma_f32 v57, v58, v58, v57\n"
"v_sub_f32 v59, v35, v28\n"
"v_add_f32 v30, v30, v59\n"
"v_fma_f32 v31, v59, v59, v31\n"
"v_sub_f32 v58, v36, v28\n"
"v_add_f32 v56, v56, v58\n"
"v_fma_f32 v57, v58, v58, v57\n"
"v_sub_f32 v59, v37, v28\n"
"v_add_f32 v30, v30, v59\n"
"v_fma_f32 v31, v59, v59, v31\n"
"v_sub_f32 v58, v38, v28\n"
"v_add_f32 v56, v56, v58\n"
"v_fma_f32 v57, v58, v58, v57\n"
"v_sub_f32 v59, v39, v28\n"
"v_add_f32 v30, v30, v59\n"
"v_fma_f32 v31, v59, v59, v31\n"
"v_sub_f32 v58, v40, v28\n"
"v_add_f32 v56, v56, v58\n"
"v_fma_f32 v57, v58, v58, v57\n"
"v_sub_f32 v59, v41, v28\n"
"v_add_f32 v30, v30, v59\n"
"v_fma_f32 v31, v59, v59, v31\n"
"v_sub_f32 v58, v42, v28\n"
"v_add_f32 v56, v56, v58\n"
"v_fma_f32 v57, v58, v58, v57\n"
"v_sub_f32 v59, v43, v28\n"
"v_add_f32 v30, v30, v59\n"
"v_fma_f32 v31, v59, v59, v31\n"
"v_sub_f32 v58, v44, v28\n"
"v_add_f32 v56, v56, v58\n"
"v_fma_f32 v57, v58, v58, v57\n"
"v_sub_f32 v59, v45, v28\n"
"v_add_f32 v30, v30, v59\n"
"v_fma_f32 v31, v59, v59, v31\n"
"v_sub_f32 v58, v46, v28\n"
"v_add_f32 v56, v56, v58\n"
"v_fma_f32 v57, v58, v58, v57\n"
"v_sub_f32 v59, v47, v28\n"
"v_add_f32 v30, v30, v59\n"
"v_fma_f32 v31, v59, v59, v31\n"
"v_sub_f32 v58, v48, v29\n"
"v_add_f32 v56, v56, v58\n"
"v_fma_f32 v57, v58, v58, v57\n"
"v_sub_f32 v59, v49, v29\n"
"v_add_f32 v30, v30, v59\n"
"v_fma_f32 v31, v59, v59, v31\n"
"v_sub_f32 v58, v50, v29\n"
"v_add_f32 v56, v56, v58\n"
"v_fma_f32 v57, v58, v58, v57\n"
"v_sub_f32 v59, v51, v29\n"
"v_add_f32 v30, v30, v59\n"
"v_fma_f32 v31, v59, v59, v31\n"
"v_sub_f32 v58, v52, v29\n"
"v_add_f32 v56, v56, v58\n"
"v_fma_f32 v57, v58, v58, v57\n"
"v_sub_f32 v59, v53, v29\n"
"v_add_f32 v30, v30, v59\n"
"v_fma_f32 v31, v59, v59, v31\n"
"v_sub_f32 v58, v54, v29\n"
"v_add_f32 v56, v56, v58\n"
"v_fma_f32 v57, v58, v58, v57\n"
"v_sub_f32 v59, v55, v29\n"
"v_add_f32 v30, v30, v59\n"
"v_fma_f32 v31, v59, v59, v31\n"
"v_add_f32 v56, v56, v30\n"
"v_add_f32 v57, v57, v31\n"
"s_nop 0\n"
"v_add_f32_dpp v56, v56, v56 row_ror:8 row_mask:0xf bank_mask:0xf\n"
"v_add_f32_dpp v57, v57, v57 row_ror:8 row_mask:0xf bank_mask:0xf\n"
"v_mov_b32 v58, v56\n"
"v_mov_b32 v59, v57\n"
"s_nop 0\n"
"v_permlane16_swap_b32 v56, v58\n"
"v_permlane16_swap_b32 v57, v59\n"
"v_add_f32 v56, v56, v58\n"
"v_add_f32 v57, v57, v59\n"
"v_mov_b32 v58, v56\n"
"v_mov_b32 v59, v57\n"
"s_nop 0\n"
"v_permlane32_swap_b32 v56, v58\n"
"v_permlane32_swap_b32 v57, v59\n"
"v_add_f32 v56, v56, v58\n"
"v_add_f32 v57, v57, v59\n"
"v_lshlrev_b32 v60, 4, v64\n"
"v_add_u32 v60, %[lds], v60\n"
"s_lshl_b32 s13, s60, 2\n"
"v_add_u32 v61, s13, v60\n"
"ds_write2_b32 v61, v56, v57 offset0:0 offset1:32\n"
"s_waitcnt lgkmcnt(0)\n"
"s_barrier\n"
"ds_read_b128 v[0:3], v60\n"
"ds_read_b128 v[4:7], v60 offset:128\n"
"s_waitcnt lgkmcnt(0)\n"
"v_add_f32 v0, v0, v1\n"
"v_add_f32 v2, v2, v3\n"
"v_add_f32 v4, v4, v5\n"
"v_add_f32 v6, v6, v7\n"
"v_add_f32 v0, v0, v2\n"
"v_add_f32 v4, v4, v6\n"
"v_mul_f32 v0, 0x3ae38e39, v0\n"
"v_mul_f32 v4, 0x3ae38e39, v4\n"
"v_mul_f32 v58, v0, v0\n"
"v_sub_f32 v4, v4, v58\n"
"v_max_f32 v4, 0, v4\n"
"v_add_f32 v8, v28, v0\n"
"v_cvt_pk_bf16_f32 v8, v8, v8\n"
"v_lshlrev_b32 v8, 16, v8\n"
"v_cvt_pk_bf16_f32 v4, v4, v4\n"
"v_lshlrev_b32 v4, 16, v4\n"
"v_add_f32 v4, 0x3727c5ac, v4\n"
"v_cvt_pk_bf16_f32 v4, v4, v4\n"
"v_lshlrev_b32 v4, 16, v4\n"
"v_sqrt_f32 v4, v4\n"
"s_nop 0\n"
"v_cvt_pk_bf16_f32 v4, v4, v4\n"
"v_lshlrev_b32 v4, 16, v4\n"
"v_rcp_f32 v4, v4\n"
"s_nop 0\n"
"v_mul_f32 v9, 0x3eeff558, v4\n"
"v_sub_f32 v32, v32, v8\n"
"v_sub_f32 v33, v33, v8\n"
"v_cvt_pk_bf16_f32 v58, v32, v33\n"
"v_lshlrev_b32 v32, 16, v58\n"
"v_and_b32 v33, 4294901760, v58\n"
"v_mul_f32 v32, v32, v9\n"
"v_mul_f32 v33, v33, v9\n"
"v_cvt_pk_bf16_f32 v96, v32, v33\n"
"v_sub_f32 v34, v34, v8\n"
"v_sub_f32 v35, v35, v8\n"
"v_cvt_pk_bf16_f32 v58, v34, v35\n"
"v_lshlrev_b32 v34, 16, v58\n"
"v_and_b32 v35, 4294901760, v58\n"
"v_mul_f32 v34, v34, v9\n"
"v_mul_f32 v35, v35, v9\n"
"v_cvt_pk_bf16_f32 v97, v34, v35\n"
"v_sub_f32 v36, v36, v8\n"
"v_sub_f32 v37, v37, v8\n"
"v_cvt_pk_bf16_f32 v58, v36, v37\n"
"v_lshlrev_b32 v36, 16, v58\n"
"v_and_b32 v37, 4294901760, v58\n"
"v_mul_f32 v36, v36, v9\n"
"v_mul_f32 v37, v37, v9\n"
"v_cvt_pk_bf16_f32 v98, v36, v37\n"
"v_sub_f32 v38, v38, v8\n"
"v_sub_f32 v39, v39, v8\n"
"v_cvt_pk_bf16_f32 v58, v38, v39\n"
"v_lshlrev_b32 v38, 16, v58\n"
"v_and_b32 v39, 4294901760, v58\n"
"v_mul_f32 v38, v38, v9\n"
"v_mul_f32 v39, v39, v9\n"
"v_cvt_pk_bf16_f32 v99, v38, v39\n"
"s_mov_b32 s13, 196608\n"
"buffer_store_dwordx4 v[96:99], v76, s[48:51], s13 offen sc1\n"
"v_sub_f32 v40, v40, v8\n"
"v_sub_f32 v41, v41, v8\n"
"v_cvt_pk_bf16_f32 v58, v40, v41\n"
"v_lshlrev_b32 v40, 16, v58\n"
"v_and_b32 v41, 4294901760, v58\n"
"v_mul_f32 v40, v40, v9\n"
"v_mul_f32 v41, v41, v9\n"
"v_cvt_pk_bf16_f32 v100, v40, v41\n"
"v_sub_f32 v42, v42, v8\n"
"v_sub_f32 v43, v43, v8\n"
"v_cvt_pk_bf16_f32 v58, v42, v43\n"
"v_lshlrev_b32 v42, 16, v58\n"
"v_and_b32 v43, 4294901760, v58\n"
"v_mul_f32 v42, v42, v9\n"
"v_mul_f32 v43, v43, v9\n"
"v_cvt_pk_bf16_f32 v101, v42, v43\n"
"v_sub_f32 v44, v44, v8\n"
"v_sub_f32 v45, v45, v8\n"
"v_cvt_pk_bf16_f32 v58, v44, v45\n"
"v_lshlrev_b32 v44, 16, v58\n"
"v_and_b32 v45, 4294901760, v58\n"
"v_mul_f32 v44, v44, v9\n"
"v_mul_f32 v45, v45, v9\n"
"v_cvt_pk_bf16_f32 v102, v44, v45\n"
"v_sub_f32 v46, v46, v8\n"
"v_sub_f32 v47, v47, v8\n"
"v_cvt_pk_bf16_f32 v58, v46, v47\n"
"v_lshlrev_b32 v46, 16, v58\n"
"v_and_b32 v47, 4294901760, v58\n"
"v_mul_f32 v46, v46, v9\n"
"v_mul_f32 v47, v47, v9\n"
"v_cvt_pk_bf16_f32 v103, v46, v47\n"
"s_mov_b32 s13, 262144\n"
"buffer_store_dwordx4 v[100:103], v76, s[48:51], s13 offen sc1\n"
"v_sub_f32 v48, v48, v8\n"
"v_sub_f32 v49, v49, v8\n"
"v_cvt_pk_bf16_f32 v58, v48, v49\n"
"v_lshlrev_b32 v48, 16, v58\n"
"v_and_b32 v49, 4294901760, v58\n"
"v_mul_f32 v48, v48, v9\n"
"v_mul_f32 v49, v49, v9\n"
"v_cvt_pk_bf16_f32 v104, v48, v49\n"
"v_sub_f32 v50, v50, v8\n"
"v_sub_f32 v51, v51, v8\n"
"v_cvt_pk_bf16_f32 v58, v50, v51\n"
"v_lshlrev_b32 v50, 16, v58\n"
"v_and_b32 v51, 4294901760, v58\n"
"v_mul_f32 v50, v50, v9\n"
"v_mul_f32 v51, v51, v9\n"
"v_cvt_pk_bf16_f32 v105, v50, v51\n"
"v_sub_f32 v52, v52, v8\n"
"v_sub_f32 v53, v53, v8\n"
"v_cvt_pk_bf16_f32 v58, v52, v53\n"
"v_lshlrev_b32 v52, 16, v58\n"
"v_and_b32 v53, 4294901760, v58\n"
"v_mul_f32 v52, v52, v9\n"
"v_mul_f32 v53, v53, v9\n"
"v_cvt_pk_bf16_f32 v106, v52, v53\n"
"v_sub_f32 v54, v54, v8\n"
"v_sub_f32 v55, v55, v8\n"
"v_cvt_pk_bf16_f32 v58, v54, v55\n"
"v_lshlrev_b32 v54, 16, v58\n"
"v_and_b32 v55, 4294901760, v58\n"
"v_mul_f32 v54, v54, v9\n"
"v_mul_f32 v55, v55, v9\n"
"v_cvt_pk_bf16_f32 v107, v54, v55\n"
"s_mov_b32 s13, 327680\n"
"buffer_store_dwordx4 v[104:107], v77, s[48:51], s13 offen sc1\n"
"s_waitcnt vmcnt(6)\n"
"s_mul_i32 s13, s63, 144\n"
"s_add_u32 s13, s13, s17\n"
"s_lshl_b32 s13, s13, 10\n"
"s_add_u32 s13, s13, 2555904\n"
"buffer_store_dwordx4 v[16:19], v158, s[48:51], s13 offen sc1\n"
"s_mul_i32 s13, s63, 144\n"
"s_add_u32 s13, s13, s18\n"
"s_lshl_b32 s13, s13, 10\n"
"s_add_u32 s13, s13, 2555904\n"
"buffer_store_dwordx4 v[20:23], v158, s[48:51], s13 offen sc1\n"
"s_mul_i32 s13, s63, 144\n"
"s_add_u32 s13, s13, s19\n"
"s_lshl_b32 s13, s13, 10\n"
"s_add_u32 s13, s13, 2555904\n"
"buffer_store_dwordx4 v[24:27], v78, s[48:51], s13 offen sc1\n"
"buffer_load_dword v186, v187, s[48:51], s9 offen sc1\n"
"buffer_load_dwordx4 v[100:103], v80, s[28:31], s69 offen\n"
"buffer_load_dwordx4 v[104:107], v81, s[36:39], s69 offen\n"
"buffer_load_dwordx4 v[108:111], v80, s[28:31], s70 offen\n"
"buffer_load_dwordx4 v[112:115], v81, s[36:39], s70 offen\n"
"buffer_load_dwordx4 v[116:119], v83, s[40:43], 0 offen\n"
"buffer_load_dwordx4 v[120:123], v84, s[44:47], 0 offen\n"
"s_waitcnt vmcnt(6)\n"
"s_barrier\n"
"s_mov_b32 s13, 53477376\n"
"buffer_store_dword v186, v183, s[48:51], s13 offen sc1\n"
"s_mul_i32 s69, s64, 147456\n"
"s_mul_i32 s7, s63, 147456\n"
"s_add_u32 s7, s7, 2359296\n"
"s_lshl_b32 s66, s60, 10\n"
"s_add_u32 s66, s66, 3072\n"
"v_add_u32 v173, s66, v158\n"
"v_add_u32 v174, 3072, v173\n"
"v_add_u32 v175, 4294965248, v173\n"
"v_add_u32 v176, 1024, v173\n"
"v_lshrrev_b32 v224, 4, v255\n"
"v_bfe_u32 v225, v255, 2, 2\n"
"v_and_b32 v226, 3, v255\n"
"v_and_b32 v233, 1, v224\n"
"v_lshl_add_u32 v233, v233, 2, v225\n"
"v_bfe_u32 v227, v233, 1, 2\n"
"v_lshrrev_b32 v234, 1, v224\n"
"v_lshlrev_b32 v234, 12, v234\n"
"v_lshl_add_u32 v234, v233, 7, v234\n"
"v_lshl_add_u32 v234, v226, 3, v234\n"
"v_add_u32 v234, %[lds], v234\n"
"s_lshl_b32 s66, s62, 1\n"
"s_add_u32 s66, s66, 0\n"
"v_xor_b32 v235, s66, v227\n"
"v_lshl_add_u32 v160, v235, 5, v234\n"
"v_add_u32 v162, 65536, v160\n"
"v_add_u32 v164, 131072, v160\n"
"s_lshl_b32 s66, s62, 1\n"
"s_add_u32 s66, s66, 1\n"
"v_xor_b32 v235, s66, v227\n"
"v_lshl_add_u32 v161, v235, 5, v234\n"
"v_add_u32 v163, 65536, v161\n"
"v_add_u32 v165, 131072, v161\n"
"v_mov_b32 v166, v173\n"
"v_add_u32 v179, %[lds], v158\n"
"s_lshl_b32 s66, s61, 10\n"
"v_add_u32 v178, s66, v179\n"
"v_mov_b32 v167, v178\n"
"s_waitcnt vmcnt(1)\n"
"v_or_b32 v100, v100, v104\n"
"v_or_b32 v108, v108, v112\n"
"v_or_b32 v116, v116, v120\n"
"v_or_b32 v108, v108, v116\n"
"v_or_b32 v101, v101, v105\n"
"v_or_b32 v109, v109, v113\n"
"v_or_b32 v117, v117, v121\n"
"v_or_b32 v109, v109, v117\n"
"v_or_b32 v102, v102, v106\n"
"v_or_b32 v110, v110, v114\n"
"v_or_b32 v118, v118, v122\n"
"v_or_b32 v110, v110, v118\n"
"v_or_b32 v103, v103, v107\n"
"v_or_b32 v111, v111, v115\n"
"v_or_b32 v119, v119, v123\n"
"v_or_b32 v111, v111, v119\n"
"buffer_store_dwordx4 v[100:103], v158, s[48:51], s71 offen sc1\n"
"buffer_store_dwordx4 v[108:111], v158, s[48:51], s72 offen sc1\n"
"s_mul_i32 s13, s64, 36\n"
"s_mul_i32 s14, s60, 12\n"
"s_add_u32 s13, s13, s14\n"
"s_sub_u32 s13, s13, 12\n"
"v_mul_u32_u24 v58, 32769, v255\n"
"v_lshrrev_b32 v58, 16, v58\n"
"v_mul_u32_u24 v59, 2, v58\n"
"v_sub_u32 v59, v255, v59\n"
"v_add_u32 v58, s13, v58\n"
"v_mul_u32_u24 v58, 9216, v58\n"
"v_lshl_add_u32 v58, v59, 7, v58\n"
"v_cmp_gt_u32 vcc, 24, v255\n"
"s_nop 1\n"
"v_cndmask_b32 v58, v62, v58, vcc\n"
"v_cndmask_b32_e64 v58, v58, v62, s[80:81]\n"
"s_lshr_b32 s13, s63, 1\n"
"s_mul_i32 s13, s13, 1152\n"
"s_and_b32 s14, s63, 1\n"
"s_mul_i32 s14, s14, 640\n"
"s_add_u32 s13, s13, s14\n"
"s_add_u32 s13, s13, 3072\n"
"buffer_load_dword v124, v58, s[28:31], s13 offen\n"
"buffer_load_dword v125, v58, s[36:39], s13 offen\n"
"s_cmp_eq_u32 s60, 0\n"
"s_cbranch_scc0 Lhs_done_%=\n"
"s_mov_b32 s79, 0\n"
"Lhs_%=:\n"
"buffer_load_dword v58, v187, s[48:51], s27 offen sc1\n"
"buffer_load_dword v186, v187, s[48:51], s9 offen sc1\n"
"s_waitcnt vmcnt(0)\n"
"v_cmp_eq_u32 vcc, v58, v15\n"
"s_nop 1\n"
"s_cmp_eq_u32 vcc_lo, -1\n"
"s_cbranch_scc1 Lhs_done_%=\n"
"s_mov_b32 s13, 53477376\n"
"buffer_store_dword v186, v183, s[48:51], s13 offen sc1\n"
"s_add_u32 s79, s79, 1\n"
"s_cmp_lt_u32 s79, 262144\n"
"s_cbranch_scc1 Lhs_%=\n"
"Lhs_done_%=:\n"
"s_barrier\n"
"buffer_load_dword v186, v187, s[48:51], s9 offen sc1\n"
"s_add_u32 s20, s98, 193536\n"
"s_addc_u32 s21, s99, 0\n"
"s_and_b32 s21, s21, 65535\n"
"s_mov_b32 s22, 6622208\n"
"s_mov_b32 s23, 131072\n"
"s_add_u32 m0, s65, 0\n"
"s_add_u32 s66, s69, 0\n"
"buffer_load_dwordx4 v173, s[20:23], s66 offen sc1 lds\n"
"buffer_load_dwordx4 v174, s[20:23], s66 offen offset:1024 sc1 lds\n"
"s_add_u32 s67, s7, 0\n"
"buffer_load_dwordx4 v175, s[20:23], s67 offen offset:2048 sc1 lds\n"
"buffer_load_dwordx4 v176, s[20:23], s67 offen offset:3072 sc1 lds\n"
"s_add_u32 m0, s65, 16384\n"
"s_add_u32 s66, s69, 8192\n"
"buffer_load_dwordx4 v173, s[20:23], s66 offen sc1 lds\n"
"buffer_load_dwordx4 v174, s[20:23], s66 offen offset:1024 sc1 lds\n"
"s_add_u32 s67, s7, 8192\n"
"buffer_load_dwordx4 v175, s[20:23], s67 offen offset:2048 sc1 lds\n"
"buffer_load_dwordx4 v176, s[20:23], s67 offen offset:3072 sc1 lds\n"
"s_add_u32 m0, s65, 32768\n"
"s_add_u32 s66, s69, 16384\n"
"buffer_load_dwordx4 v173, s[20:23], s66 offen sc1 lds\n"
"buffer_load_dwordx4 v174, s[20:23], s66 offen offset:1024 sc1 lds\n"
"s_add_u32 s67, s7, 16384\n"
"buffer_load_dwordx4 v175, s[20:23], s67 offen offset:2048 sc1 lds\n"
"buffer_load_dwordx4 v176, s[20:23], s67 offen offset:3072 sc1 lds\n"
"s_waitcnt vmcnt(8)\n"
"s_barrier\n"
"s_mov_b32 s18, 53477376\n"
"buffer_store_dword v186, v183, s[48:51], s18 offen sc1\n"
"v_add_u32 v180, 0, v178\n"
"ds_read_b128 a[0:3], v180\n"
"ds_read_b128 a[4:7], v180 offset:4096\n"
"ds_read_b128 a[8:11], v180 offset:8192\n"
"ds_read_b128 a[12:15], v180 offset:12288\n"
"ds_read_b64_tr_b16 v[128:129], v160 offset:2048\n"
"ds_read_b64_tr_b16 v[130:131], v160 offset:10240\n"
"ds_read_b64_tr_b16 v[132:133], v161 offset:2048\n"
"ds_read_b64_tr_b16 v[134:135], v161 offset:10240\n"
"ds_read_b64_tr_b16 v[136:137], v160 offset:3072\n"
"ds_read_b64_tr_b16 v[138:139], v160 offset:11264\n"
"s_waitcnt lgkmcnt(2)\n"
"v_mfma_f32_16x16x32_bf16 v[0:3], a[0:3], v[128:131], 0\n"
"ds_read_b64_tr_b16 v[140:141], v161 offset:3072\n"
"ds_read_b64_tr_b16 v[142:143], v161 offset:11264\n"
"v_mfma_f32_16x16x32_bf16 v[4:7], a[4:7], v[128:131], 0\n"
"s_add_u32 m0, s65, 49152\n"
"s_add_u32 s66, s69, 24576\n"
"buffer_load_dwordx4 v173, s[20:23], s66 offen sc1 lds\n"
"v_mfma_f32_16x16x32_bf16 v[8:11], a[8:11], v[128:131], 0\n"
"buffer_load_dwordx4 v174, s[20:23], s66 offen offset:1024 sc1 lds\n"
"v_mfma_f32_16x16x32_bf16 v[12:15], a[12:15], v[128:131], 0\n"
"v_lshrrev_b32 v239, 7, v158\n"
"v_bfe_u32 v236, v158, 8, 2\n"
"v_lshlrev_b32 v236, 1, v236\n"
"v_bfe_u32 v238, v158, 4, 3\n"
"v_xor_b32 v238, v236, v238\n"
"v_mfma_f32_16x16x32_bf16 v[16:19], a[0:3], v[132:135], 0\n"
"s_waitcnt vmcnt(7)\n"
"s_barrier\n"
"v_mfma_f32_16x16x32_bf16 v[20:23], a[4:7], v[132:135], 0\n"
"v_add_u32 v180, 16384, v178\n"
"ds_read_b128 a[16:19], v180\n"
"v_mfma_f32_16x16x32_bf16 v[24:27], a[8:11], v[132:135], 0\n"
"ds_read_b128 a[20:23], v180 offset:4096\n"
"ds_read_b128 a[24:27], v180 offset:8192\n"
"v_mfma_f32_16x16x32_bf16 v[28:31], a[12:15], v[132:135], 0\n"
"ds_read_b128 a[28:31], v180 offset:12288\n"
"s_waitcnt lgkmcnt(4)\n"
"v_mfma_f32_16x16x32_bf16 v[32:35], a[0:3], v[136:139], 0\n"
"ds_read_b64_tr_b16 v[144:145], v160 offset:18432\n"
"ds_read_b64_tr_b16 v[146:147], v160 offset:26624\n"
"v_mfma_f32_16x16x32_bf16 v[36:39], a[4:7], v[136:139], 0\n"
"ds_read_b64_tr_b16 v[128:129], v161 offset:18432\n"
"ds_read_b64_tr_b16 v[130:131], v161 offset:26624\n"
"v_mfma_f32_16x16x32_bf16 v[40:43], a[8:11], v[136:139], 0\n"
"s_add_u32 s67, s7, 24576\n"
"buffer_load_dwordx4 v175, s[20:23], s67 offen offset:2048 sc1 lds\n"
"v_mfma_f32_16x16x32_bf16 v[44:47], a[12:15], v[136:139], 0\n"
"buffer_load_dwordx4 v176, s[20:23], s67 offen offset:3072 sc1 lds\n"
"v_mfma_f32_16x16x32_bf16 v[48:51], a[0:3], v[140:143], 0\n"
"ds_read_b64_tr_b16 v[132:133], v160 offset:19456\n"
"ds_read_b64_tr_b16 v[134:135], v160 offset:27648\n"
"v_mfma_f32_16x16x32_bf16 v[52:55], a[4:7], v[140:143], 0\n"
"s_add_u32 m0, s65, 65536\n"
"s_add_u32 s66, s69, 32768\n"
"buffer_load_dwordx4 v173, s[20:23], s66 offen sc1 lds\n"
"v_mfma_f32_16x16x32_bf16 v[56:59], a[8:11], v[140:143], 0\n"
"buffer_load_dwordx4 v174, s[20:23], s66 offen offset:1024 sc1 lds\n"
"v_mfma_f32_16x16x32_bf16 v[60:63], a[12:15], v[140:143], 0\n"
"s_waitcnt lgkmcnt(2)\n"
"v_mfma_f32_16x16x32_bf16 v[0:3], a[16:19], v[144:147], v[0:3]\n"
"ds_read_b64_tr_b16 v[136:137], v161 offset:19456\n"
"ds_read_b64_tr_b16 v[138:139], v161 offset:27648\n"
"v_mfma_f32_16x16x32_bf16 v[4:7], a[20:23], v[144:147], v[4:7]\n"
"s_add_u32 s67, s7, 32768\n"
"buffer_load_dwordx4 v175, s[20:23], s67 offen offset:2048 sc1 lds\n"
"v_mfma_f32_16x16x32_bf16 v[8:11], a[24:27], v[144:147], v[8:11]\n"
"buffer_load_dwordx4 v176, s[20:23], s67 offen offset:3072 sc1 lds\n"
"v_mfma_f32_16x16x32_bf16 v[12:15], a[28:31], v[144:147], v[12:15]\n"
"s_lshl_b32 s16, s60, 3\n"
"v_add_u32 v237, s16, v239\n"
"s_movk_i32 s16, 9216\n"
"v_mul_lo_u32 v236, s16, v237\n"
"v_lshl_add_u32 v236, v238, 4, v236\n"
"v_mfma_f32_16x16x32_bf16 v[16:19], a[16:19], v[128:131], v[16:19]\n"
"s_waitcnt vmcnt(9)\n"
"s_barrier\n"
"v_mfma_f32_16x16x32_bf16 v[20:23], a[20:23], v[128:131], v[20:23]\n"
"v_add_u32 v180, 32768, v178\n"
"ds_read_b128 a[32:35], v180\n"
"v_mfma_f32_16x16x32_bf16 v[24:27], a[24:27], v[128:131], v[24:27]\n"
"ds_read_b128 a[36:39], v180 offset:4096\n"
"ds_read_b128 a[40:43], v180 offset:8192\n"
"v_mfma_f32_16x16x32_bf16 v[28:31], a[28:31], v[128:131], v[28:31]\n"
"ds_read_b128 a[44:47], v180 offset:12288\n"
"s_waitcnt lgkmcnt(4)\n"
"v_mfma_f32_16x16x32_bf16 v[32:35], a[16:19], v[132:135], v[32:35]\n"
"ds_read_b64_tr_b16 v[140:141], v160 offset:34816\n"
"ds_read_b64_tr_b16 v[142:143], v160 offset:43008\n"
"v_mfma_f32_16x16x32_bf16 v[36:39], a[20:23], v[132:135], v[36:39]\n"
"ds_read_b64_tr_b16 v[144:145], v161 offset:34816\n"
"ds_read_b64_tr_b16 v[146:147], v161 offset:43008\n"
"v_mfma_f32_16x16x32_bf16 v[40:43], a[24:27], v[132:135], v[40:43]\n"
"s_add_u32 m0, s65, 81920\n"
"s_add_u32 s66, s69, 40960\n"
"buffer_load_dwordx4 v173, s[20:23], s66 offen sc1 lds\n"
"v_mfma_f32_16x16x32_bf16 v[44:47], a[28:31], v[132:135], v[44:47]\n"
"buffer_load_dwordx4 v174, s[20:23], s66 offen offset:1024 sc1 lds\n"
"v_mfma_f32_16x16x32_bf16 v[48:51], a[16:19], v[136:139], v[48:51]\n"
"ds_read_b64_tr_b16 v[128:129], v160 offset:35840\n"
"ds_read_b64_tr_b16 v[130:131], v160 offset:44032\n"
"v_mfma_f32_16x16x32_bf16 v[52:55], a[20:23], v[136:139], v[52:55]\n"
"s_add_u32 s67, s7, 40960\n"
"buffer_load_dwordx4 v175, s[20:23], s67 offen offset:2048 sc1 lds\n"
"v_mfma_f32_16x16x32_bf16 v[56:59], a[24:27], v[136:139], v[56:59]\n"
"buffer_load_dwordx4 v176, s[20:23], s67 offen offset:3072 sc1 lds\n"
"v_mfma_f32_16x16x32_bf16 v[60:63], a[28:31], v[136:139], v[60:63]\n"
"s_waitcnt lgkmcnt(2)\n"
"v_mfma_f32_16x16x32_bf16 v[0:3], a[32:35], v[140:143], v[0:3]\n"
"ds_read_b64_tr_b16 v[132:133], v161 offset:35840\n"
"ds_read_b64_tr_b16 v[134:135], v161 offset:44032\n"
"v_mfma_f32_16x16x32_bf16 v[4:7], a[36:39], v[140:143], v[4:7]\n"
"s_add_u32 m0, s65, 98304\n"
"s_add_u32 s66, s69, 49152\n"
"buffer_load_dwordx4 v173, s[20:23], s66 offen sc1 lds\n"
"v_mfma_f32_16x16x32_bf16 v[8:11], a[40:43], v[140:143], v[8:11]\n"
"buffer_load_dwordx4 v174, s[20:23], s66 offen offset:1024 sc1 lds\n"
"v_mfma_f32_16x16x32_bf16 v[12:15], a[44:47], v[140:143], v[12:15]\n"
"v_add_u32 v169, 3072, v236\n"
"v_add_u32 v170, 2048, v236\n"
"v_add_u32 v171, 295936, v236\n"
"v_add_u32 v172, 294912, v236\n"
"v_mfma_f32_16x16x32_bf16 v[16:19], a[32:35], v[144:147], v[16:19]\n"
"s_waitcnt vmcnt(10)\n"
"s_barrier\n"
"v_mfma_f32_16x16x32_bf16 v[20:23], a[36:39], v[144:147], v[20:23]\n"
"v_add_u32 v180, 49152, v178\n"
"ds_read_b128 a[48:51], v180\n"
"v_mfma_f32_16x16x32_bf16 v[24:27], a[40:43], v[144:147], v[24:27]\n"
"ds_read_b128 a[52:55], v180 offset:4096\n"
"ds_read_b128 a[56:59], v180 offset:8192\n"
"v_mfma_f32_16x16x32_bf16 v[28:31], a[44:47], v[144:147], v[28:31]\n"
"ds_read_b128 a[60:63], v180 offset:12288\n"
"s_waitcnt lgkmcnt(4)\n"
"v_mfma_f32_16x16x32_bf16 v[32:35], a[32:35], v[128:131], v[32:35]\n"
"ds_read_b64_tr_b16 v[136:137], v160 offset:51200\n"
"ds_read_b64_tr_b16 v[138:139], v160 offset:59392\n"
"v_mfma_f32_16x16x32_bf16 v[36:39], a[36:39], v[128:131], v[36:39]\n"
"ds_read_b64_tr_b16 v[140:141], v161 offset:51200\n"
"ds_read_b64_tr_b16 v[142:143], v161 offset:59392\n"
"v_mfma_f32_16x16x32_bf16 v[40:43], a[40:43], v[128:131], v[40:43]\n"
"s_add_u32 s67, s7, 49152\n"
"buffer_load_dwordx4 v175, s[20:23], s67 offen offset:2048 sc1 lds\n"
"v_mfma_f32_16x16x32_bf16 v[44:47], a[44:47], v[128:131], v[44:47]\n"
"buffer_load_dwordx4 v176, s[20:23], s67 offen offset:3072 sc1 lds\n"
"v_mfma_f32_16x16x32_bf16 v[48:51], a[32:35], v[132:135], v[48:51]\n"
"ds_read_b64_tr_b16 v[144:145], v160 offset:52224\n"
"ds_read_b64_tr_b16 v[146:147], v160 offset:60416\n"
"v_mfma_f32_16x16x32_bf16 v[52:55], a[36:39], v[132:135], v[52:55]\n"
"s_add_u32 m0, s65, 114688\n"
"s_add_u32 s66, s69, 57344\n"
"buffer_load_dwordx4 v173, s[20:23], s66 offen sc1 lds\n"
"v_mfma_f32_16x16x32_bf16 v[56:59], a[40:43], v[132:135], v[56:59]\n"
"buffer_load_dwordx4 v174, s[20:23], s66 offen offset:1024 sc1 lds\n"
"v_mfma_f32_16x16x32_bf16 v[60:63], a[44:47], v[132:135], v[60:63]\n"
"s_waitcnt lgkmcnt(2)\n"
"v_mfma_f32_16x16x32_bf16 v[0:3], a[48:51], v[136:139], v[0:3]\n"
"ds_read_b64_tr_b16 v[128:129], v161 offset:52224\n"
"ds_read_b64_tr_b16 v[130:131], v161 offset:60416\n"
"v_mfma_f32_16x16x32_bf16 v[4:7], a[52:55], v[136:139], v[4:7]\n"
"s_add_u32 s67, s7, 57344\n"
"buffer_load_dwordx4 v175, s[20:23], s67 offen offset:2048 sc1 lds\n"
"v_mfma_f32_16x16x32_bf16 v[8:11], a[56:59], v[136:139], v[8:11]\n"
"buffer_load_dwordx4 v176, s[20:23], s67 offen offset:3072 sc1 lds\n"
"v_mfma_f32_16x16x32_bf16 v[12:15], a[60:63], v[136:139], v[12:15]\n"
"s_lshr_b32 s16, s63, 1\n"
"s_mul_i32 s16, s16, 576\n"
"s_and_b32 s17, s63, 1\n"
"s_mul_i32 s18, s17, 320\n"
"s_add_u32 s18, s18, s16\n"
"s_lshl_b32 s70, s18, 1\n"
"s_mul_i32 s72, s18, 1152\n"
"s_add_u32 s18, s16, 256\n"
"s_lshl_b32 s17, s17, 5\n"
"s_add_u32 s18, s18, s17\n"
"s_mul_i32 s73, s18, 1152\n"
"v_mfma_f32_16x16x32_bf16 v[16:19], a[48:51], v[140:143], v[16:19]\n"
"s_waitcnt vmcnt(12)\n"
"s_barrier\n"
"v_mfma_f32_16x16x32_bf16 v[20:23], a[52:55], v[140:143], v[20:23]\n"
"v_add_u32 v180, 65536, v178\n"
"ds_read_b128 a[64:67], v180\n"
"v_mfma_f32_16x16x32_bf16 v[24:27], a[56:59], v[140:143], v[24:27]\n"
"ds_read_b128 a[68:71], v180 offset:4096\n"
"ds_read_b128 a[72:75], v180 offset:8192\n"
"v_mfma_f32_16x16x32_bf16 v[28:31], a[60:63], v[140:143], v[28:31]\n"
"ds_read_b128 a[76:79], v180 offset:12288\n"
"s_waitcnt lgkmcnt(4)\n"
"v_mfma_f32_16x16x32_bf16 v[32:35], a[48:51], v[144:147], v[32:35]\n"
"ds_read_b64_tr_b16 v[132:133], v162 offset:2048\n"
"ds_read_b64_tr_b16 v[134:135], v162 offset:10240\n"
"v_mfma_f32_16x16x32_bf16 v[36:39], a[52:55], v[144:147], v[36:39]\n"
"ds_read_b64_tr_b16 v[136:137], v163 offset:2048\n"
"ds_read_b64_tr_b16 v[138:139], v163 offset:10240\n"
"v_mfma_f32_16x16x32_bf16 v[40:43], a[56:59], v[144:147], v[40:43]\n"
"s_add_u32 m0, s65, 131072\n"
"s_add_u32 s66, s69, 65536\n"
"buffer_load_dwordx4 v173, s[20:23], s66 offen sc1 lds\n"
"v_mfma_f32_16x16x32_bf16 v[44:47], a[60:63], v[144:147], v[44:47]\n"
"buffer_load_dwordx4 v174, s[20:23], s66 offen offset:1024 sc1 lds\n"
"v_mfma_f32_16x16x32_bf16 v[48:51], a[48:51], v[128:131], v[48:51]\n"
"ds_read_b64_tr_b16 v[140:141], v162 offset:3072\n"
"ds_read_b64_tr_b16 v[142:143], v162 offset:11264\n"
"v_mfma_f32_16x16x32_bf16 v[52:55], a[52:55], v[128:131], v[52:55]\n"
"s_add_u32 s67, s7, 65536\n"
"buffer_load_dwordx4 v175, s[20:23], s67 offen offset:2048 sc1 lds\n"
"v_mfma_f32_16x16x32_bf16 v[56:59], a[56:59], v[128:131], v[56:59]\n"
"buffer_load_dwordx4 v176, s[20:23], s67 offen offset:3072 sc1 lds\n"
"v_mfma_f32_16x16x32_bf16 v[60:63], a[60:63], v[128:131], v[60:63]\n"
"s_waitcnt lgkmcnt(2)\n"
"v_mfma_f32_16x16x32_bf16 v[0:3], a[64:67], v[132:135], v[0:3]\n"
"ds_read_b64_tr_b16 v[144:145], v163 offset:3072\n"
"ds_read_b64_tr_b16 v[146:147], v163 offset:11264\n"
"v_mfma_f32_16x16x32_bf16 v[4:7], a[68:71], v[132:135], v[4:7]\n"
"s_add_u32 m0, s65, 147456\n"
"s_add_u32 s66, s69, 73728\n"
"buffer_load_dwordx4 v173, s[20:23], s66 offen sc1 lds\n"
"v_mfma_f32_16x16x32_bf16 v[8:11], a[72:75], v[132:135], v[8:11]\n"
"buffer_load_dwordx4 v174, s[20:23], s66 offen offset:1024 sc1 lds\n"
"v_mfma_f32_16x16x32_bf16 v[12:15], a[76:79], v[132:135], v[12:15]\n"
"s_mul_i32 s9, s63, 73728\n"
"s_add_u32 s9, s9, 4715520\n"
"s_mul_i32 s71, s63, 45056\n"
"s_add_u32 s71, s71, 5895168\n"
"v_mfma_f32_16x16x32_bf16 v[16:19], a[64:67], v[136:139], v[16:19]\n"
"s_waitcnt vmcnt(14)\n"
"s_barrier\n"
"v_mfma_f32_16x16x32_bf16 v[20:23], a[68:71], v[136:139], v[20:23]\n"
"v_add_u32 v180, 81920, v178\n"
"ds_read_b128 a[80:83], v180\n"
"v_mfma_f32_16x16x32_bf16 v[24:27], a[72:75], v[136:139], v[24:27]\n"
"ds_read_b128 a[84:87], v180 offset:4096\n"
"ds_read_b128 a[88:91], v180 offset:8192\n"
"v_mfma_f32_16x16x32_bf16 v[28:31], a[76:79], v[136:139], v[28:31]\n"
"ds_read_b128 a[92:95], v180 offset:12288\n"
"s_waitcnt lgkmcnt(4)\n"
"v_mfma_f32_16x16x32_bf16 v[32:35], a[64:67], v[140:143], v[32:35]\n"
"ds_read_b64_tr_b16 v[128:129], v162 offset:18432\n"
"ds_read_b64_tr_b16 v[130:131], v162 offset:26624\n"
"v_mfma_f32_16x16x32_bf16 v[36:39], a[68:71], v[140:143], v[36:39]\n"
"ds_read_b64_tr_b16 v[132:133], v163 offset:18432\n"
"ds_read_b64_tr_b16 v[134:135], v163 offset:26624\n"
"v_mfma_f32_16x16x32_bf16 v[40:43], a[72:75], v[140:143], v[40:43]\n"
"s_add_u32 s67, s7, 73728\n"
"buffer_load_dwordx4 v175, s[20:23], s67 offen offset:2048 sc1 lds\n"
"v_mfma_f32_16x16x32_bf16 v[44:47], a[76:79], v[140:143], v[44:47]\n"
"buffer_load_dwordx4 v176, s[20:23], s67 offen offset:3072 sc1 lds\n"
"v_mfma_f32_16x16x32_bf16 v[48:51], a[64:67], v[144:147], v[48:51]\n"
"ds_read_b64_tr_b16 v[136:137], v162 offset:19456\n"
"ds_read_b64_tr_b16 v[138:139], v162 offset:27648\n"
"v_mfma_f32_16x16x32_bf16 v[52:55], a[68:71], v[144:147], v[52:55]\n"
"s_add_u32 m0, s65, 0\n"
"s_add_u32 s66, s69, 81920\n"
"buffer_load_dwordx4 v173, s[20:23], s66 offen sc1 lds\n"
"v_mfma_f32_16x16x32_bf16 v[56:59], a[72:75], v[144:147], v[56:59]\n"
"buffer_load_dwordx4 v174, s[20:23], s66 offen offset:1024 sc1 lds\n"
"v_mfma_f32_16x16x32_bf16 v[60:63], a[76:79], v[144:147], v[60:63]\n"
"s_waitcnt lgkmcnt(2)\n"
"v_mfma_f32_16x16x32_bf16 v[0:3], a[80:83], v[128:131], v[0:3]\n"
"ds_read_b64_tr_b16 v[140:141], v163 offset:19456\n"
"ds_read_b64_tr_b16 v[142:143], v163 offset:27648\n"
"v_mfma_f32_16x16x32_bf16 v[4:7], a[84:87], v[128:131], v[4:7]\n"
"s_add_u32 s67, s7, 81920\n"
"buffer_load_dwordx4 v175, s[20:23], s67 offen offset:2048 sc1 lds\n"
"v_mfma_f32_16x16x32_bf16 v[8:11], a[88:91], v[128:131], v[8:11]\n"
"buffer_load_dwordx4 v176, s[20:23], s67 offen offset:3072 sc1 lds\n"
"v_mfma_f32_16x16x32_bf16 v[12:15], a[92:95], v[128:131], v[12:15]\n"
"s_lshr_b32 s16, s63, 1\n"
"s_mul_i32 s16, s16, 576\n"
"s_and_b32 s17, s63, 1\n"
"s_xor_b32 s17, s17, 1\n"
"s_mul_i32 s18, s17, 320\n"
"s_add_u32 s18, s18, s16\n"
"s_mul_i32 s24, s18, 1152\n"
"s_add_u32 s18, s16, 256\n"
"s_lshl_b32 s17, s17, 5\n"
"s_add_u32 s18, s18, s17\n"
"s_mul_i32 s25, s18, 1152\n"
"v_mfma_f32_16x16x32_bf16 v[16:19], a[80:83], v[132:135], v[16:19]\n"
"s_waitcnt vmcnt(16)\n"
"s_barrier\n"
"v_mfma_f32_16x16x32_bf16 v[20:23], a[84:87], v[132:135], v[20:23]\n"
"v_add_u32 v180, 98304, v178\n"
"ds_read_b128 a[96:99], v180\n"
"v_mfma_f32_16x16x32_bf16 v[24:27], a[88:91], v[132:135], v[24:27]\n"
"ds_read_b128 a[100:103], v180 offset:4096\n"
"ds_read_b128 a[104:107], v180 offset:8192\n"
"v_mfma_f32_16x16x32_bf16 v[28:31], a[92:95], v[132:135], v[28:31]\n"
"ds_read_b128 a[108:111], v180 offset:12288\n"
"s_waitcnt lgkmcnt(4)\n"
"v_mfma_f32_16x16x32_bf16 v[32:35], a[80:83], v[136:139], v[32:35]\n"
"ds_read_b64_tr_b16 v[144:145], v162 offset:34816\n"
"ds_read_b64_tr_b16 v[146:147], v162 offset:43008\n"
"v_mfma_f32_16x16x32_bf16 v[36:39], a[84:87], v[136:139], v[36:39]\n"
"ds_read_b64_tr_b16 v[128:129], v163 offset:34816\n"
"ds_read_b64_tr_b16 v[130:131], v163 offset:43008\n"
"v_mfma_f32_16x16x32_bf16 v[40:43], a[88:91], v[136:139], v[40:43]\n"
"s_add_u32 m0, s65, 16384\n"
"s_add_u32 s66, s69, 90112\n"
"buffer_load_dwordx4 v173, s[20:23], s66 offen sc1 lds\n"
"v_mfma_f32_16x16x32_bf16 v[44:47], a[92:95], v[136:139], v[44:47]\n"
"buffer_load_dwordx4 v174, s[20:23], s66 offen offset:1024 sc1 lds\n"
"v_mfma_f32_16x16x32_bf16 v[48:51], a[80:83], v[140:143], v[48:51]\n"
"ds_read_b64_tr_b16 v[132:133], v162 offset:35840\n"
"ds_read_b64_tr_b16 v[134:135], v162 offset:44032\n"
"v_mfma_f32_16x16x32_bf16 v[52:55], a[84:87], v[140:143], v[52:55]\n"
"s_add_u32 s67, s7, 90112\n"
"buffer_load_dwordx4 v175, s[20:23], s67 offen offset:2048 sc1 lds\n"
"v_mfma_f32_16x16x32_bf16 v[56:59], a[88:91], v[140:143], v[56:59]\n"
"buffer_load_dwordx4 v176, s[20:23], s67 offen offset:3072 sc1 lds\n"
"v_mfma_f32_16x16x32_bf16 v[60:63], a[92:95], v[140:143], v[60:63]\n"
"s_waitcnt lgkmcnt(2)\n"
"v_mfma_f32_16x16x32_bf16 v[0:3], a[96:99], v[144:147], v[0:3]\n"
"ds_read_b64_tr_b16 v[136:137], v163 offset:35840\n"
"ds_read_b64_tr_b16 v[138:139], v163 offset:44032\n"
"v_mfma_f32_16x16x32_bf16 v[4:7], a[100:103], v[144:147], v[4:7]\n"
"s_add_u32 m0, s65, 32768\n"
"s_add_u32 s66, s69, 98304\n"
"buffer_load_dwordx4 v173, s[20:23], s66 offen sc1 lds\n"
"v_mfma_f32_16x16x32_bf16 v[8:11], a[104:107], v[144:147], v[8:11]\n"
"buffer_load_dwordx4 v174, s[20:23], s66 offen offset:1024 sc1 lds\n"
"v_mfma_f32_16x16x32_bf16 v[12:15], a[108:111], v[144:147], v[12:15]\n"
"s_mul_i32 s74, s63, 41472\n"
"s_xor_b32 s16, s63, 1\n"
"s_mul_i32 s26, s16, 41472\n"
"s_and_b32 s16, s63, 1\n"
"s_mul_i32 s8, s16, 640\n"
"v_mfma_f32_16x16x32_bf16 v[16:19], a[96:99], v[128:131], v[16:19]\n"
"s_waitcnt vmcnt(18)\n"
"s_barrier\n"
"v_mfma_f32_16x16x32_bf16 v[20:23], a[100:103], v[128:131], v[20:23]\n"
"v_add_u32 v180, 114688, v178\n"
"ds_read_b128 a[112:115], v180\n"
"v_mfma_f32_16x16x32_bf16 v[24:27], a[104:107], v[128:131], v[24:27]\n"
"ds_read_b128 a[116:119], v180 offset:4096\n"
"ds_read_b128 a[120:123], v180 offset:8192\n"
"v_mfma_f32_16x16x32_bf16 v[28:31], a[108:111], v[128:131], v[28:31]\n"
"ds_read_b128 a[124:127], v180 offset:12288\n"
"s_waitcnt lgkmcnt(4)\n"
"v_mfma_f32_16x16x32_bf16 v[32:35], a[96:99], v[132:135], v[32:35]\n"
"ds_read_b64_tr_b16 v[140:141], v162 offset:51200\n"
"ds_read_b64_tr_b16 v[142:143], v162 offset:59392\n"
"v_mfma_f32_16x16x32_bf16 v[36:39], a[100:103], v[132:135], v[36:39]\n"
"ds_read_b64_tr_b16 v[144:145], v163 offset:51200\n"
"ds_read_b64_tr_b16 v[146:147], v163 offset:59392\n"
"v_mfma_f32_16x16x32_bf16 v[40:43], a[104:107], v[132:135], v[40:43]\n"
"s_add_u32 s67, s7, 98304\n"
"buffer_load_dwordx4 v175, s[20:23], s67 offen offset:2048 sc1 lds\n"
"v_mfma_f32_16x16x32_bf16 v[44:47], a[108:111], v[132:135], v[44:47]\n"
"buffer_load_dwordx4 v176, s[20:23], s67 offen offset:3072 sc1 lds\n"
"v_mfma_f32_16x16x32_bf16 v[48:51], a[96:99], v[136:139], v[48:51]\n"
"ds_read_b64_tr_b16 v[128:129], v162 offset:52224\n"
"ds_read_b64_tr_b16 v[130:131], v162 offset:60416\n"
"v_mfma_f32_16x16x32_bf16 v[52:55], a[100:103], v[136:139], v[52:55]\n"
"s_add_u32 m0, s65, 49152\n"
"s_add_u32 s66, s69, 106496\n"
"buffer_load_dwordx4 v173, s[20:23], s66 offen sc1 lds\n"
"v_mfma_f32_16x16x32_bf16 v[56:59], a[104:107], v[136:139], v[56:59]\n"
"buffer_load_dwordx4 v174, s[20:23], s66 offen offset:1024 sc1 lds\n"
"v_mfma_f32_16x16x32_bf16 v[60:63], a[108:111], v[136:139], v[60:63]\n"
"s_waitcnt lgkmcnt(2)\n"
"v_mfma_f32_16x16x32_bf16 v[0:3], a[112:115], v[140:143], v[0:3]\n"
"ds_read_b64_tr_b16 v[132:133], v163 offset:52224\n"
"ds_read_b64_tr_b16 v[134:135], v163 offset:60416\n"
"v_mfma_f32_16x16x32_bf16 v[4:7], a[116:119], v[140:143], v[4:7]\n"
"s_add_u32 s67, s7, 106496\n"
"buffer_load_dwordx4 v175, s[20:23], s67 offen offset:2048 sc1 lds\n"
"v_mfma_f32_16x16x32_bf16 v[8:11], a[120:123], v[140:143], v[8:11]\n"
"buffer_load_dwordx4 v176, s[20:23], s67 offen offset:3072 sc1 lds\n"
"v_mfma_f32_16x16x32_bf16 v[12:15], a[124:127], v[140:143], v[12:15]\n"
"s_movk_i32 s16, 1152\n"
"v_mul_lo_u32 v191, s16, v237\n"
"v_lshl_add_u32 v191, v238, 4, v191\n"
"v_mfma_f32_16x16x32_bf16 v[16:19], a[112:115], v[144:147], v[16:19]\n"
"s_waitcnt vmcnt(20)\n"
"s_barrier\n"
"v_mfma_f32_16x16x32_bf16 v[20:23], a[116:119], v[144:147], v[20:23]\n"
"v_add_u32 v180, 131072, v178\n"
"ds_read_b128 a[128:131], v180\n"
"v_mfma_f32_16x16x32_bf16 v[24:27], a[120:123], v[144:147], v[24:27]\n"
"ds_read_b128 a[132:135], v180 offset:4096\n"
"ds_read_b128 a[136:139], v180 offset:8192\n"
"v_mfma_f32_16x16x32_bf16 v[28:31], a[124:127], v[144:147], v[28:31]\n"
"ds_read_b128 a[140:143], v180 offset:12288\n"
"s_waitcnt lgkmcnt(4)\n"
"v_mfma_f32_16x16x32_bf16 v[32:35], a[112:115], v[128:131], v[32:35]\n"
"ds_read_b64_tr_b16 v[136:137], v164 offset:2048\n"
"ds_read_b64_tr_b16 v[138:139], v164 offset:10240\n"
"v_mfma_f32_16x16x32_bf16 v[36:39], a[116:119], v[128:131], v[36:39]\n"
"ds_read_b64_tr_b16 v[140:141], v165 offset:2048\n"
"ds_read_b64_tr_b16 v[142:143], v165 offset:10240\n"
"v_mfma_f32_16x16x32_bf16 v[40:43], a[120:123], v[128:131], v[40:43]\n"
"s_add_u32 m0, s65, 65536\n"
"s_add_u32 s66, s69, 114688\n"
"buffer_load_dwordx4 v173, s[20:23], s66 offen sc1 lds\n"
"v_mfma_f32_16x16x32_bf16 v[44:47], a[124:127], v[128:131], v[44:47]\n"
"buffer_load_dwordx4 v174, s[20:23], s66 offen offset:1024 sc1 lds\n"
"v_mfma_f32_16x16x32_bf16 v[48:51], a[112:115], v[132:135], v[48:51]\n"
"ds_read_b64_tr_b16 v[144:145], v164 offset:3072\n"
"ds_read_b64_tr_b16 v[146:147], v164 offset:11264\n"
"v_mfma_f32_16x16x32_bf16 v[52:55], a[116:119], v[132:135], v[52:55]\n"
"s_add_u32 s67, s7, 114688\n"
"buffer_load_dwordx4 v175, s[20:23], s67 offen offset:2048 sc1 lds\n"
"v_mfma_f32_16x16x32_bf16 v[56:59], a[120:123], v[132:135], v[56:59]\n"
"buffer_load_dwordx4 v176, s[20:23], s67 offen offset:3072 sc1 lds\n"
"v_mfma_f32_16x16x32_bf16 v[60:63], a[124:127], v[132:135], v[60:63]\n"
"s_waitcnt lgkmcnt(2)\n"
"v_mfma_f32_16x16x32_bf16 v[0:3], a[128:131], v[136:139], v[0:3]\n"
"ds_read_b64_tr_b16 v[128:129], v165 offset:3072\n"
"ds_read_b64_tr_b16 v[130:131], v165 offset:11264\n"
"v_mfma_f32_16x16x32_bf16 v[4:7], a[132:135], v[136:139], v[4:7]\n"
"s_add_u32 m0, s65, 81920\n"
"s_add_u32 s66, s69, 122880\n"
"buffer_load_dwordx4 v173, s[20:23], s66 offen sc1 lds\n"
"v_mfma_f32_16x16x32_bf16 v[8:11], a[136:139], v[136:139], v[8:11]\n"
"s_lshl_b32 s16, s62, 13\n"
"s_lshl_b32 s17, s61, 12\n"
"s_add_u32 s16, s16, s17\n"
"v_add_u32 v177, s16, v179\n"
"v_mfma_f32_16x16x32_bf16 v[12:15], a[140:143], v[136:139], v[12:15]\n"
"s_mov_b32 s75, 0x409d30fc\n"
"s_mov_b32 s76, 0x40088e99\n"
"s_mov_b32 s77, 0x40450298\n"
"s_mov_b32 s78, 0x358637bd\n"
"s_mov_b32 s5, 0x3eeff558\n"
"v_mfma_f32_16x16x32_bf16 v[16:19], a[128:131], v[140:143], v[16:19]\n"
"s_waitcnt vmcnt(21)\n"
"s_barrier\n"
"v_mfma_f32_16x16x32_bf16 v[20:23], a[132:135], v[140:143], v[20:23]\n"
"v_add_u32 v180, 147456, v178\n"
"ds_read_b128 a[144:147], v180\n"
"v_mfma_f32_16x16x32_bf16 v[24:27], a[136:139], v[140:143], v[24:27]\n"
"ds_read_b128 a[148:151], v180 offset:4096\n"
"ds_read_b128 a[152:155], v180 offset:8192\n"
"v_mfma_f32_16x16x32_bf16 v[28:31], a[140:143], v[140:143], v[28:31]\n"
"ds_read_b128 a[156:159], v180 offset:12288\n"
"s_waitcnt lgkmcnt(4)\n"
"v_mfma_f32_16x16x32_bf16 v[32:35], a[128:131], v[144:147], v[32:35]\n"
"ds_read_b64_tr_b16 v[132:133], v164 offset:18432\n"
"ds_read_b64_tr_b16 v[134:135], v164 offset:26624\n"
"v_mfma_f32_16x16x32_bf16 v[36:39], a[132:135], v[144:147], v[36:39]\n"
"ds_read_b64_tr_b16 v[136:137], v165 offset:18432\n"
"ds_read_b64_tr_b16 v[138:139], v165 offset:26624\n"
"v_mfma_f32_16x16x32_bf16 v[40:43], a[136:139], v[144:147], v[40:43]\n"
"buffer_load_dwordx4 v174, s[20:23], s66 offen offset:1024 sc1 lds\n"
"v_mfma_f32_16x16x32_bf16 v[44:47], a[140:143], v[144:147], v[44:47]\n"
"s_cmp_eq_u32 s62, 0\n"
"s_cselect_b32 s80, 0xff00ff, -1\n"
"s_mov_b32 s81, s80\n"
"s_cselect_b32 s82, 0xf000f00, 0\n"
"s_mov_b32 s83, s82\n"
"v_mfma_f32_16x16x32_bf16 v[48:51], a[128:131], v[128:131], v[48:51]\n"
"ds_read_b64_tr_b16 v[140:141], v164 offset:19456\n"
"ds_read_b64_tr_b16 v[142:143], v164 offset:27648\n"
"v_mfma_f32_16x16x32_bf16 v[52:55], a[132:135], v[128:131], v[52:55]\n"
"s_add_u32 s67, s7, 122880\n"
"buffer_load_dwordx4 v175, s[20:23], s67 offen offset:2048 sc1 lds\n"
"v_mfma_f32_16x16x32_bf16 v[56:59], a[136:139], v[128:131], v[56:59]\n"
"s_cmp_eq_u32 s62, 0\n"
"s_mov_b32 s16, 0\n"
"s_cselect_b32 s12, s16, 0\n"
"v_mfma_f32_16x16x32_bf16 v[60:63], a[140:143], v[128:131], v[60:63]\n"
"s_waitcnt lgkmcnt(2)\n"
"v_mfma_f32_16x16x32_bf16 v[0:3], a[144:147], v[132:135], v[0:3]\n"
"ds_read_b64_tr_b16 v[144:145], v165 offset:19456\n"
"ds_read_b64_tr_b16 v[146:147], v165 offset:27648\n"
"v_mfma_f32_16x16x32_bf16 v[4:7], a[148:151], v[132:135], v[4:7]\n"
"buffer_load_dwordx4 v176, s[20:23], s67 offen offset:3072 sc1 lds\n"
"v_mfma_f32_16x16x32_bf16 v[8:11], a[152:155], v[132:135], v[8:11]\n"
"s_cmp_eq_u32 s62, 0\n"
"s_mov_b32 s16, 8\n"
"s_cselect_b32 s13, s16, 2048\n"
"v_mfma_f32_16x16x32_bf16 v[12:15], a[156:159], v[132:135], v[12:15]\n"
"s_cmp_eq_u32 s62, 0\n"
"s_mov_b32 s16, 1024\n"
"s_cselect_b32 s14, s16, 4096\n"
"v_mfma_f32_16x16x32_bf16 v[16:19], a[144:147], v[136:139], v[16:19]\n"
"s_waitcnt vmcnt(20)\n"
"s_barrier\n"
"v_mfma_f32_16x16x32_bf16 v[20:23], a[148:151], v[136:139], v[20:23]\n"
"v_add_u32 v180, 0, v178\n"
"ds_read_b128 a[160:163], v180\n"
"v_mfma_f32_16x16x32_bf16 v[24:27], a[152:155], v[136:139], v[24:27]\n"
"ds_read_b128 a[164:167], v180 offset:4096\n"
"ds_read_b128 a[168:171], v180 offset:8192\n"
"v_mfma_f32_16x16x32_bf16 v[28:31], a[156:159], v[136:139], v[28:31]\n"
"ds_read_b128 a[172:175], v180 offset:12288\n"
"s_waitcnt lgkmcnt(4)\n"
"v_mfma_f32_16x16x32_bf16 v[32:35], a[144:147], v[140:143], v[32:35]\n"
"ds_read_b64_tr_b16 v[128:129], v160 offset:2048\n"
"ds_read_b64_tr_b16 v[130:131], v160 offset:10240\n"
"v_mfma_f32_16x16x32_bf16 v[36:39], a[148:151], v[140:143], v[36:39]\n"
"ds_read_b64_tr_b16 v[132:133], v161 offset:2048\n"
"ds_read_b64_tr_b16 v[134:135], v161 offset:10240\n"
"v_mfma_f32_16x16x32_bf16 v[40:43], a[152:155], v[140:143], v[40:43]\n"
"s_add_u32 m0, s65, 98304\n"
"s_add_u32 s66, s69, 131072\n"
"buffer_load_dwordx4 v173, s[20:23], s66 offen sc1 lds\n"
"v_mfma_f32_16x16x32_bf16 v[44:47], a[156:159], v[140:143], v[44:47]\n"
"s_cmp_eq_u32 s62, 0\n"
"s_mov_b32 s16, 1032\n"
"s_cselect_b32 s15, s16, 6144\n"
"v_mfma_f32_16x16x32_bf16 v[48:51], a[144:147], v[144:147], v[48:51]\n"
"ds_read_b64_tr_b16 v[136:137], v160 offset:3072\n"
"ds_read_b64_tr_b16 v[138:139], v160 offset:11264\n"
"v_mfma_f32_16x16x32_bf16 v[52:55], a[148:151], v[144:147], v[52:55]\n"
"buffer_load_dwordx4 v174, s[20:23], s66 offen offset:1024 sc1 lds\n"
"v_mfma_f32_16x16x32_bf16 v[56:59], a[152:155], v[144:147], v[56:59]\n"
"s_mov_b32 s84, 65535\n"
"s_mov_b32 s85, 0\n"
"v_mfma_f32_16x16x32_bf16 v[60:63], a[156:159], v[144:147], v[60:63]\n"
"s_waitcnt lgkmcnt(2)\n"
"v_mfma_f32_16x16x32_bf16 v[0:3], a[160:163], v[128:131], v[0:3]\n"
"ds_read_b64_tr_b16 v[140:141], v161 offset:3072\n"
"ds_read_b64_tr_b16 v[142:143], v161 offset:11264\n"
"v_mfma_f32_16x16x32_bf16 v[4:7], a[164:167], v[128:131], v[4:7]\n"
"s_add_u32 s67, s7, 131072\n"
"buffer_load_dwordx4 v175, s[20:23], s67 offen offset:2048 sc1 lds\n"
"v_mfma_f32_16x16x32_bf16 v[8:11], a[168:171], v[128:131], v[8:11]\n"
"s_lshl_b32 s16, s63, 4\n"
"s_add_u32 s16, s16, s64\n"
"s_lshl_b32 s16, s16, 3\n"
"s_lshl_b32 s17, s60, 1\n"
"s_add_u32 s16, s16, s17\n"
"s_lshl_b32 s16, s16, 10\n"
"s_add_u32 s94, s16, 6815744\n"
"v_mfma_f32_16x16x32_bf16 v[12:15], a[172:175], v[128:131], v[12:15]\n"
"s_lshl_b32 s16, s64, 4\n"
"s_add_u32 s16, s16, s63\n"
"s_mul_i32 s17, s16, 90112\n"
"s_lshl_b32 s18, s61, 12\n"
"s_add_u32 s17, s17, s18\n"
"s_add_u32 s27, s17, 9437184\n"
"s_xor_b32 s16, s16, 1\n"
"s_mul_i32 s17, s16, 90112\n"
"s_add_u32 s6, s17, 9437184\n"
"v_mfma_f32_16x16x32_bf16 v[16:19], a[160:163], v[132:135], v[16:19]\n"
"s_waitcnt vmcnt(19)\n"
"s_barrier\n"
"v_mfma_f32_16x16x32_bf16 v[20:23], a[164:167], v[132:135], v[20:23]\n"
"v_add_u32 v180, 16384, v178\n"
"ds_read_b128 a[176:179], v180\n"
"v_mfma_f32_16x16x32_bf16 v[24:27], a[168:171], v[132:135], v[24:27]\n"
"ds_read_b128 a[180:183], v180 offset:4096\n"
"ds_read_b128 a[184:187], v180 offset:8192\n"
"v_mfma_f32_16x16x32_bf16 v[28:31], a[172:175], v[132:135], v[28:31]\n"
"ds_read_b128 a[188:191], v180 offset:12288\n"
"s_waitcnt lgkmcnt(4)\n"
"v_mfma_f32_16x16x32_bf16 v[32:35], a[160:163], v[136:139], v[32:35]\n"
"ds_read_b64_tr_b16 v[144:145], v160 offset:18432\n"
"ds_read_b64_tr_b16 v[146:147], v160 offset:26624\n"
"v_mfma_f32_16x16x32_bf16 v[36:39], a[164:167], v[136:139], v[36:39]\n"
"ds_read_b64_tr_b16 v[128:129], v161 offset:18432\n"
"ds_read_b64_tr_b16 v[130:131], v161 offset:26624\n"
"v_mfma_f32_16x16x32_bf16 v[40:43], a[168:171], v[136:139], v[40:43]\n"
"buffer_load_dwordx4 v176, s[20:23], s67 offen offset:3072 sc1 lds\n"
"v_mfma_f32_16x16x32_bf16 v[44:47], a[172:175], v[136:139], v[44:47]\n"
"s_and_b32 s16, s63, 1\n"
"s_lshl_b32 s17, s64, 1\n"
"s_add_u32 s16, s16, s17\n"
"s_lshl_b32 s16, s16, 3\n"
"s_lshr_b32 s17, s63, 1\n"
"s_add_u32 s16, s16, s17\n"
"s_mul_i32 s16, s16, 73728\n"
"s_add_u32 s95, s16, 32505856\n"
"v_mfma_f32_16x16x32_bf16 v[48:51], a[160:163], v[140:143], v[48:51]\n"
"ds_read_b64_tr_b16 v[132:133], v160 offset:19456\n"
"ds_read_b64_tr_b16 v[134:135], v160 offset:27648\n"
"v_mfma_f32_16x16x32_bf16 v[52:55], a[164:167], v[140:143], v[52:55]\n"
"s_add_u32 m0, s65, 131072\n"
"s_add_u32 s66, s69, 139264\n"
"buffer_load_dwordx4 v173, s[20:23], s66 offen sc1 lds\n"
"v_mfma_f32_16x16x32_bf16 v[56:59], a[168:171], v[140:143], v[56:59]\n"
"s_mov_b32 s96, 0\n"
"s_mov_b32 s99, 0\n"
"s_mov_b32 s97, 6144\n"
"s_mov_b32 s98, 3\n"
"s_mov_b32 s18, 0\n"
"s_mov_b32 s19, 0\n"
"s_mov_b32 s79, 0\n"
"v_mfma_f32_16x16x32_bf16 v[60:63], a[172:175], v[140:143], v[60:63]\n"
"s_waitcnt lgkmcnt(2)\n"
"v_mfma_f32_16x16x32_bf16 v[0:3], a[176:179], v[144:147], v[0:3]\n"
"ds_read_b64_tr_b16 v[136:137], v161 offset:19456\n"
"ds_read_b64_tr_b16 v[138:139], v161 offset:27648\n"
"v_mfma_f32_16x16x32_bf16 v[4:7], a[180:183], v[144:147], v[4:7]\n"
"buffer_load_dwordx4 v174, s[20:23], s66 offen offset:1024 sc1 lds\n"
"v_mfma_f32_16x16x32_bf16 v[8:11], a[184:187], v[144:147], v[8:11]\n"
"s_cmp_eq_u32 s64, 0\n"
"s_cselect_b32 s96, 0x0, s96\n"
"s_cselect_b32 s99, 0x0, s99\n"
"s_cselect_b32 s97, 0x0, s97\n"
"s_cselect_b32 s98, 0x4, s98\n"
"s_cselect_b32 s18, 0xffffffff, s18\n"
"s_cselect_b32 s19, 0x0, s19\n"
"s_cselect_b32 s79, 0x0, s79\n"
"v_mfma_f32_16x16x32_bf16 v[12:15], a[188:191], v[144:147], v[12:15]\n"
"s_cmp_eq_u32 s64, 1\n"
"s_cselect_b32 s96, 0x400, s96\n"
"s_cselect_b32 s99, 0x0, s99\n"
"s_cselect_b32 s97, 0x8, s97\n"
"s_cselect_b32 s98, 0x4, s98\n"
"s_cselect_b32 s18, 0xffffffff, s18\n"
"s_cselect_b32 s19, 0x0, s19\n"
"s_cselect_b32 s79, 0x1, s79\n"
"v_mfma_f32_16x16x32_bf16 v[16:19], a[176:179], v[128:131], v[16:19]\n"
"s_waitcnt vmcnt(18)\n"
"s_barrier\n"
"v_mfma_f32_16x16x32_bf16 v[20:23], a[180:183], v[128:131], v[20:23]\n"
"v_add_u32 v180, 32768, v178\n"
"ds_read_b128 a[192:195], v180\n"
"v_mfma_f32_16x16x32_bf16 v[24:27], a[184:187], v[128:131], v[24:27]\n"
"ds_read_b128 a[196:199], v180 offset:4096\n"
"ds_read_b128 a[200:203], v180 offset:8192\n"
"v_mfma_f32_16x16x32_bf16 v[28:31], a[188:191], v[128:131], v[28:31]\n"
"ds_read_b128 a[204:207], v180 offset:12288\n"
"s_waitcnt lgkmcnt(4)\n"
"v_mfma_f32_16x16x32_bf16 v[32:35], a[176:179], v[132:135], v[32:35]\n"
"ds_read_b64_tr_b16 v[140:141], v160 offset:34816\n"
"ds_read_b64_tr_b16 v[142:143], v160 offset:43008\n"
"v_mfma_f32_16x16x32_bf16 v[36:39], a[180:183], v[132:135], v[36:39]\n"
"ds_read_b64_tr_b16 v[144:145], v161 offset:34816\n"
"ds_read_b64_tr_b16 v[146:147], v161 offset:43008\n"
"v_mfma_f32_16x16x32_bf16 v[40:43], a[184:187], v[132:135], v[40:43]\n"
"s_add_u32 s67, s7, 139264\n"
"buffer_load_dwordx4 v175, s[20:23], s67 offen offset:2048 sc1 lds\n"
"v_mfma_f32_16x16x32_bf16 v[44:47], a[188:191], v[132:135], v[44:47]\n"
"s_cmp_eq_u32 s64, 2\n"
"s_cselect_b32 s96, 0x800, s96\n"
"s_cselect_b32 s99, 0x0, s99\n"
"s_cselect_b32 s97, 0x400, s97\n"
"s_cselect_b32 s98, 0x4, s98\n"
"s_cselect_b32 s18, 0x30003, s18\n"
"s_cselect_b32 s19, 0x0, s19\n"
"s_cselect_b32 s79, 0x0, s79\n"
"v_mfma_f32_16x16x32_bf16 v[48:51], a[176:179], v[136:139], v[48:51]\n"
"ds_read_b64_tr_b16 v[128:129], v160 offset:35840\n"
"ds_read_b64_tr_b16 v[130:131], v160 offset:44032\n"
"v_mfma_f32_16x16x32_bf16 v[52:55], a[180:183], v[136:139], v[52:55]\n"
"buffer_load_dwordx4 v176, s[20:23], s67 offen offset:3072 sc1 lds\n"
"v_mfma_f32_16x16x32_bf16 v[56:59], a[184:187], v[136:139], v[56:59]\n"
"s_cmp_eq_u32 s64, 3\n"
"s_cselect_b32 s96, 0xc00, s96\n"
"s_cselect_b32 s99, 0x0, s99\n"
"s_cselect_b32 s97, 0x408, s97\n"
"s_cselect_b32 s98, 0x4, s98\n"
"s_cselect_b32 s18, 0x30003, s18\n"
"s_cselect_b32 s19, 0xfffcfffc, s19\n"
"s_cselect_b32 s79, 0x1, s79\n"
"v_mfma_f32_16x16x32_bf16 v[60:63], a[188:191], v[136:139], v[60:63]\n"
"s_waitcnt lgkmcnt(2)\n"
"v_mfma_f32_16x16x32_bf16 v[0:3], a[192:195], v[140:143], v[0:3]\n"
"ds_read_b64_tr_b16 v[132:133], v161 offset:35840\n"
"ds_read_b64_tr_b16 v[134:135], v161 offset:44032\n"
"v_mfma_f32_16x16x32_bf16 v[4:7], a[196:199], v[140:143], v[4:7]\n"
"s_add_u32 m0, s65, 0\n"
"s_add_u32 s66, s70, 0\n"
"buffer_load_dwordx4 v169, s[28:31], s66 offen lds\n"
"v_mfma_f32_16x16x32_bf16 v[8:11], a[200:203], v[140:143], v[8:11]\n"
"s_cmp_eq_u32 s64, 4\n"
"s_cselect_b32 s96, 0x1800, s96\n"
"s_cselect_b32 s99, 0x0, s99\n"
"s_cselect_b32 s97, 0xc00, s97\n"
"s_cselect_b32 s98, 0x4, s98\n"
"s_cselect_b32 s18, 0xfffcfffc, s18\n"
"s_cselect_b32 s19, 0x0, s19\n"
"s_cselect_b32 s79, 0x2, s79\n"
"v_mfma_f32_16x16x32_bf16 v[12:15], a[204:207], v[140:143], v[12:15]\n"
"s_cmp_eq_u32 s64, 5\n"
"s_cselect_b32 s96, 0x1400, s96\n"
"s_cselect_b32 s99, 0x0, s99\n"
"s_cselect_b32 s97, 0x1000, s97\n"
"s_cselect_b32 s98, 0x3, s98\n"
"s_cselect_b32 s18, 0x0, s18\n"
"s_cselect_b32 s19, 0xf000f000, s19\n"
"s_cselect_b32 s79, 0x1, s79\n"
"v_mfma_f32_16x16x32_bf16 v[16:19], a[192:195], v[144:147], v[16:19]\n"
"s_waitcnt vmcnt(17)\n"
"s_barrier\n"
"v_mfma_f32_16x16x32_bf16 v[20:23], a[196:199], v[144:147], v[20:23]\n"
"v_add_u32 v180, 49152, v178\n"
"ds_read_b128 a[208:211], v180\n"
"v_mfma_f32_16x16x32_bf16 v[24:27], a[200:203], v[144:147], v[24:27]\n"
"ds_read_b128 a[212:215], v180 offset:4096\n"
"ds_read_b128 a[216:219], v180 offset:8192\n"
"v_mfma_f32_16x16x32_bf16 v[28:31], a[204:207], v[144:147], v[28:31]\n"
"ds_read_b128 a[220:223], v180 offset:12288\n"
"s_waitcnt lgkmcnt(4)\n"
"v_mfma_f32_16x16x32_bf16 v[32:35], a[192:195], v[128:131], v[32:35]\n"
"ds_read_b64_tr_b16 v[136:137], v160 offset:51200\n"
"ds_read_b64_tr_b16 v[138:139], v160 offset:59392\n"
"v_mfma_f32_16x16x32_bf16 v[36:39], a[196:199], v[128:131], v[36:39]\n"
"ds_read_b64_tr_b16 v[140:141], v161 offset:51200\n"
"ds_read_b64_tr_b16 v[142:143], v161 offset:59392\n"
"v_mfma_f32_16x16x32_bf16 v[40:43], a[200:203], v[128:131], v[40:43]\n"
"buffer_load_dwordx4 v170, s[36:39], s66 offen offset:1024 lds\n"
"v_mfma_f32_16x16x32_bf16 v[44:47], a[204:207], v[128:131], v[44:47]\n"
"s_cmp_eq_u32 s64, 6\n"
"s_cselect_b32 s96, 0x1c00, s96\n"
"s_cselect_b32 s99, 0x0, s99\n"
"s_cselect_b32 s97, 0xc08, s97\n"
"s_cselect_b32 s98, 0x4, s98\n"
"s_cselect_b32 s18, 0xf000f000, s18\n"
"s_cselect_b32 s19, 0x0, s19\n"
"s_cselect_b32 s79, 0x2, s79\n"
"v_mfma_f32_16x16x32_bf16 v[48:51], a[192:195], v[132:135], v[48:51]\n"
"ds_read_b64_tr_b16 v[144:145], v160 offset:52224\n"
"ds_read_b64_tr_b16 v[146:147], v160 offset:60416\n"
"v_mfma_f32_16x16x32_bf16 v[52:55], a[196:199], v[132:135], v[52:55]\n"
"buffer_load_dwordx4 v171, s[28:31], s66 offen offset:2048 lds\n"
"v_mfma_f32_16x16x32_bf16 v[56:59], a[200:203], v[132:135], v[56:59]\n"
"s_cmp_eq_u32 s64, 7\n"
"s_cselect_b32 s96, 0x1000, s96\n"
"s_cselect_b32 s99, 0x1, s99\n"
"s_cselect_b32 s97, 0x800, s97\n"
"s_cselect_b32 s98, 0x4, s98\n"
"s_cselect_b32 s18, 0x55555555, s18\n"
"s_cselect_b32 s19, 0x0, s19\n"
"s_cselect_b32 s79, 0x0, s79\n"
"v_mfma_f32_16x16x32_bf16 v[60:63], a[204:207], v[132:135], v[60:63]\n"
"s_waitcnt lgkmcnt(2)\n"
"v_mfma_f32_16x16x32_bf16 v[0:3], a[208:211], v[136:139], v[0:3]\n"
"ds_read_b64_tr_b16 v[128:129], v161 offset:52224\n"
"ds_read_b64_tr_b16 v[130:131], v161 offset:60416\n"
"v_mfma_f32_16x16x32_bf16 v[4:7], a[212:215], v[136:139], v[4:7]\n"
"buffer_load_dwordx4 v172, s[36:39], s66 offen offset:3072 lds\n"
"v_mfma_f32_16x16x32_bf16 v[8:11], a[216:219], v[136:139], v[8:11]\n"
"s_cmp_eq_u32 s64, 8\n"
"s_cselect_b32 s96, 0x1400, s96\n"
"s_cselect_b32 s99, 0x1, s99\n"
"s_cselect_b32 s97, 0x808, s97\n"
"s_cselect_b32 s98, 0x4, s98\n"
"s_cselect_b32 s18, 0x55555555, s18\n"
"s_cselect_b32 s19, 0xaaaaaaaa, s19\n"
"s_cselect_b32 s79, 0x1, s79\n"
"v_mfma_f32_16x16x32_bf16 v[12:15], a[220:223], v[136:139], v[12:15]\n"
"s_cmp_eq_u32 s64, 9\n"
"s_cselect_b32 s96, 0x1c00, s96\n"
"s_cselect_b32 s99, 0x1, s99\n"
"s_cselect_b32 s97, 0x1200, s97\n"
"s_cselect_b32 s98, 0x3, s98\n"
"s_cselect_b32 s18, 0xaaaaaaaa, s18\n"
"s_cselect_b32 s19, 0x0, s19\n"
"s_cselect_b32 s79, 0x2, s79\n"
"v_mfma_f32_16x16x32_bf16 v[16:19], a[208:211], v[140:143], v[16:19]\n"
"s_waitcnt vmcnt(16)\n"
"s_barrier\n"
"v_mfma_f32_16x16x32_bf16 v[20:23], a[212:215], v[140:143], v[20:23]\n"
"v_add_u32 v180, 65536, v178\n"
"ds_read_b128 a[224:227], v180\n"
"v_mfma_f32_16x16x32_bf16 v[24:27], a[216:219], v[140:143], v[24:27]\n"
"ds_read_b128 a[228:231], v180 offset:4096\n"
"ds_read_b128 a[232:235], v180 offset:8192\n"
"v_mfma_f32_16x16x32_bf16 v[28:31], a[220:223], v[140:143], v[28:31]\n"
"ds_read_b128 a[236:239], v180 offset:12288\n"
"s_waitcnt lgkmcnt(4)\n"
"v_mfma_f32_16x16x32_bf16 v[32:35], a[208:211], v[144:147], v[32:35]\n"
"ds_read_b64_tr_b16 v[132:133], v162 offset:2048\n"
"ds_read_b64_tr_b16 v[134:135], v162 offset:10240\n"
"v_mfma_f32_16x16x32_bf16 v[36:39], a[212:215], v[144:147], v[36:39]\n"
"ds_read_b64_tr_b16 v[136:137], v163 offset:2048\n"
"ds_read_b64_tr_b16 v[138:139], v163 offset:10240\n"
"v_mfma_f32_16x16x32_bf16 v[40:43], a[216:219], v[144:147], v[40:43]\n"
"s_add_u32 m0, s65, 16384\n"
"s_add_u32 s66, s70, 589824\n"
"buffer_load_dwordx4 v169, s[28:31], s66 offen lds\n"
"v_mfma_f32_16x16x32_bf16 v[44:47], a[220:223], v[144:147], v[44:47]\n"
"s_mov_b32 s10, 0\n"
"s_cmp_eq_u32 s79, 0\n"
"s_cselect_b32 s10, 1, s10\n"
"s_cmp_eq_u32 s60, 0\n"
"s_cselect_b32 s11, 1, 0\n"
"s_cmp_eq_u32 s79, 1\n"
"s_cselect_b32 s11, s11, 0\n"
"s_or_b32 s10, s10, s11\n"
"s_cmp_eq_u32 s60, 1\n"
"s_cselect_b32 s11, 1, 0\n"
"s_cmp_eq_u32 s79, 2\n"
"s_cselect_b32 s11, s11, 0\n"
"s_or_b32 s10, s10, s11\n"
"s_cmp_lg_u32 s10, 0\n"
"s_cselect_b32 s86, s18, s19\n"
"s_mov_b32 s87, s86\n"
"v_mfma_f32_16x16x32_bf16 v[48:51], a[208:211], v[128:131], v[48:51]\n"
"ds_read_b64_tr_b16 v[140:141], v162 offset:3072\n"
"ds_read_b64_tr_b16 v[142:143], v162 offset:11264\n"
"v_mfma_f32_16x16x32_bf16 v[52:55], a[212:215], v[128:131], v[52:55]\n"
"buffer_load_dwordx4 v170, s[36:39], s66 offen offset:1024 lds\n"
"v_mfma_f32_16x16x32_bf16 v[56:59], a[216:219], v[128:131], v[56:59]\n"
"s_mov_b32 s10, 0\n"
"s_cmp_eq_u32 s79, 0\n"
"s_cselect_b32 s10, 1, s10\n"
"s_cmp_eq_u32 s60, 0\n"
"s_cselect_b32 s11, 1, 0\n"
"s_cmp_eq_u32 s79, 1\n"
"s_cselect_b32 s11, s11, 0\n"
"s_or_b32 s10, s10, s11\n"
"s_cmp_eq_u32 s60, 1\n"
"s_cselect_b32 s11, 1, 0\n"
"s_cmp_eq_u32 s79, 2\n"
"s_cselect_b32 s11, s11, 0\n"
"s_or_b32 s10, s10, s11\n"
"s_cmp_lg_u32 s10, 0\n"
"s_cselect_b32 s88, s18, s19\n"
"s_mov_b32 s89, s88\n"
"v_mfma_f32_16x16x32_bf16 v[60:63], a[220:223], v[128:131], v[60:63]\n"
"s_waitcnt lgkmcnt(2)\n"
"v_mfma_f32_16x16x32_bf16 v[0:3], a[224:227], v[132:135], v[0:3]\n"
"ds_read_b64_tr_b16 v[144:145], v163 offset:3072\n"
"ds_read_b64_tr_b16 v[146:147], v163 offset:11264\n"
"v_mfma_f32_16x16x32_bf16 v[4:7], a[228:231], v[132:135], v[4:7]\n"
"buffer_load_dwordx4 v171, s[28:31], s66 offen offset:2048 lds\n"
"v_mfma_f32_16x16x32_bf16 v[8:11], a[232:235], v[132:135], v[8:11]\n"
"s_mov_b32 s10, 0\n"
"s_cmp_eq_u32 s79, 0\n"
"s_cselect_b32 s10, 1, s10\n"
"s_cmp_eq_u32 s60, 1\n"
"s_cselect_b32 s11, 1, 0\n"
"s_cmp_eq_u32 s79, 2\n"
"s_cselect_b32 s11, s11, 0\n"
"s_or_b32 s10, s10, s11\n"
"s_cmp_lg_u32 s10, 0\n"
"s_cselect_b32 s90, s18, s19\n"
"s_mov_b32 s91, s90\n"
"v_mfma_f32_16x16x32_bf16 v[12:15], a[236:239], v[132:135], v[12:15]\n"
"s_mov_b32 s10, 0\n"
"s_cmp_eq_u32 s79, 0\n"
"s_cselect_b32 s10, 1, s10\n"
"s_cmp_eq_u32 s60, 1\n"
"s_cselect_b32 s11, 1, 0\n"
"s_cmp_eq_u32 s79, 2\n"
"s_cselect_b32 s11, s11, 0\n"
"s_or_b32 s10, s10, s11\n"
"s_cmp_lg_u32 s10, 0\n"
"s_cselect_b32 s92, s18, s19\n"
"s_mov_b32 s93, s92\n"
"v_mfma_f32_16x16x32_bf16 v[16:19], a[224:227], v[136:139], v[16:19]\n"
"s_waitcnt vmcnt(15)\n"
"s_barrier\n"
"v_mfma_f32_16x16x32_bf16 v[20:23], a[228:231], v[136:139], v[20:23]\n"
"v_add_u32 v180, 81920, v178\n"
"ds_read_b128 a[240:243], v180\n"
"v_mfma_f32_16x16x32_bf16 v[24:27], a[232:235], v[136:139], v[24:27]\n"
"ds_read_b128 a[244:247], v180 offset:4096\n"
"ds_read_b128 a[248:251], v180 offset:8192\n"
"v_mfma_f32_16x16x32_bf16 v[28:31], a[236:239], v[136:139], v[28:31]\n"
"ds_read_b128 a[252:255], v180 offset:12288\n"
"s_waitcnt lgkmcnt(4)\n"
"v_mfma_f32_16x16x32_bf16 v[32:35], a[224:227], v[140:143], v[32:35]\n"
"ds_read_b64_tr_b16 v[128:129], v162 offset:18432\n"
"ds_read_b64_tr_b16 v[130:131], v162 offset:26624\n"
"v_mfma_f32_16x16x32_bf16 v[36:39], a[228:231], v[140:143], v[36:39]\n"
"ds_read_b64_tr_b16 v[132:133], v163 offset:18432\n"
"ds_read_b64_tr_b16 v[134:135], v163 offset:26624\n"
"v_mfma_f32_16x16x32_bf16 v[40:43], a[232:235], v[140:143], v[40:43]\n"
"buffer_load_dwordx4 v172, s[36:39], s66 offen offset:3072 lds\n"
"v_mfma_f32_16x16x32_bf16 v[44:47], a[236:239], v[140:143], v[44:47]\n"
"s_lshl_b32 s10, s63, 17\n"
"s_add_u32 s96, s96, s10\n"
"s_add_u32 s96, s96, 6815744\n"
"s_lshl_b32 s10, s63, 13\n"
"s_add_u32 s97, s97, s10\n"
"s_add_u32 s97, s97, 8912896\n"
"v_mfma_f32_16x16x32_bf16 v[48:51], a[224:227], v[144:147], v[48:51]\n"
"ds_read_b64_tr_b16 v[136:137], v162 offset:19456\n"
"ds_read_b64_tr_b16 v[138:139], v162 offset:27648\n"
"v_mfma_f32_16x16x32_bf16 v[52:55], a[228:231], v[144:147], v[52:55]\n"
"s_add_u32 m0, s65, 32768\n"
"s_add_u32 s66, s70, 1179648\n"
"buffer_load_dwordx4 v169, s[28:31], s66 offen lds\n"
"v_mfma_f32_16x16x32_bf16 v[56:59], a[232:235], v[144:147], v[56:59]\n"
"v_mfma_f32_16x16x32_bf16 v[60:63], a[236:239], v[144:147], v[60:63]\n"
"s_waitcnt lgkmcnt(2)\n"
"v_mfma_f32_16x16x32_bf16 v[0:3], a[240:243], v[128:131], v[0:3]\n"
"ds_read_b64_tr_b16 v[140:141], v163 offset:19456\n"
"ds_read_b64_tr_b16 v[142:143], v163 offset:27648\n"
"v_mfma_f32_16x16x32_bf16 v[4:7], a[244:247], v[128:131], v[4:7]\n"
"buffer_load_dwordx4 v170, s[36:39], s66 offen offset:1024 lds\n"
"v_mfma_f32_16x16x32_bf16 v[8:11], a[248:251], v[128:131], v[8:11]\n"
"v_mfma_f32_16x16x32_bf16 v[12:15], a[252:255], v[128:131], v[12:15]\n"
"v_mfma_f32_16x16x32_bf16 v[16:19], a[240:243], v[132:135], v[16:19]\n"
"s_waitcnt vmcnt(14)\n"
"s_barrier\n"
"v_mfma_f32_16x16x32_bf16 v[20:23], a[244:247], v[132:135], v[20:23]\n"
"v_add_u32 v180, 98304, v178\n"
"ds_read_b128 v[192:195], v180\n"
"v_mfma_f32_16x16x32_bf16 v[24:27], a[248:251], v[132:135], v[24:27]\n"
"ds_read_b128 v[196:199], v180 offset:4096\n"
"ds_read_b128 v[200:203], v180 offset:8192\n"
"v_mfma_f32_16x16x32_bf16 v[28:31], a[252:255], v[132:135], v[28:31]\n"
"ds_read_b128 v[204:207], v180 offset:12288\n"
"s_waitcnt lgkmcnt(4)\n"
"v_mfma_f32_16x16x32_bf16 v[32:35], a[240:243], v[136:139], v[32:35]\n"
"ds_read_b64_tr_b16 v[144:145], v162 offset:34816\n"
"ds_read_b64_tr_b16 v[146:147], v162 offset:43008\n"
"v_mfma_f32_16x16x32_bf16 v[36:39], a[244:247], v[136:139], v[36:39]\n"
"ds_read_b64_tr_b16 v[128:129], v163 offset:34816\n"
"ds_read_b64_tr_b16 v[130:131], v163 offset:43008\n"
"v_mfma_f32_16x16x32_bf16 v[40:43], a[248:251], v[136:139], v[40:43]\n"
"buffer_load_dwordx4 v171, s[28:31], s66 offen offset:2048 lds\n"
"v_mfma_f32_16x16x32_bf16 v[44:47], a[252:255], v[136:139], v[44:47]\n"
"v_mfma_f32_16x16x32_bf16 v[48:51], a[240:243], v[140:143], v[48:51]\n"
"ds_read_b64_tr_b16 v[132:133], v162 offset:35840\n"
"ds_read_b64_tr_b16 v[134:135], v162 offset:44032\n"
"v_mfma_f32_16x16x32_bf16 v[52:55], a[244:247], v[140:143], v[52:55]\n"
"buffer_load_dwordx4 v172, s[36:39], s66 offen offset:3072 lds\n"
"v_mfma_f32_16x16x32_bf16 v[56:59], a[248:251], v[140:143], v[56:59]\n"
"v_mfma_f32_16x16x32_bf16 v[60:63], a[252:255], v[140:143], v[60:63]\n"
"s_waitcnt lgkmcnt(2)\n"
"v_mfma_f32_16x16x32_bf16 v[0:3], v[192:195], v[144:147], v[0:3]\n"
"ds_read_b64_tr_b16 v[136:137], v163 offset:35840\n"
"ds_read_b64_tr_b16 v[138:139], v163 offset:44032\n"
"v_mfma_f32_16x16x32_bf16 v[4:7], v[196:199], v[144:147], v[4:7]\n"
"s_add_u32 m0, s65, 49152\n"
"s_add_u32 s66, s70, 1769472\n"
"buffer_load_dwordx4 v169, s[28:31], s66 offen lds\n"
"v_mfma_f32_16x16x32_bf16 v[8:11], v[200:203], v[144:147], v[8:11]\n"
"v_mfma_f32_16x16x32_bf16 v[12:15], v[204:207], v[144:147], v[12:15]\n"
"v_mfma_f32_16x16x32_bf16 v[16:19], v[192:195], v[128:131], v[16:19]\n"
"s_waitcnt vmcnt(13)\n"
"s_barrier\n"
"v_mfma_f32_16x16x32_bf16 v[20:23], v[196:199], v[128:131], v[20:23]\n"
"v_add_u32 v180, 131072, v178\n"
"ds_read_b128 v[208:211], v180\n"
"v_mfma_f32_16x16x32_bf16 v[24:27], v[200:203], v[128:131], v[24:27]\n"
"ds_read_b128 v[212:215], v180 offset:4096\n"
"ds_read_b128 v[216:219], v180 offset:8192\n"
"v_mfma_f32_16x16x32_bf16 v[28:31], v[204:207], v[128:131], v[28:31]\n"
"ds_read_b128 v[220:223], v180 offset:12288\n"
"s_waitcnt lgkmcnt(4)\n"
"v_mfma_f32_16x16x32_bf16 v[32:35], v[192:195], v[132:135], v[32:35]\n"
"ds_read_b64_tr_b16 v[140:141], v164 offset:2048\n"
"ds_read_b64_tr_b16 v[142:143], v164 offset:10240\n"
"v_mfma_f32_16x16x32_bf16 v[36:39], v[196:199], v[132:135], v[36:39]\n"
"ds_read_b64_tr_b16 v[144:145], v165 offset:2048\n"
"ds_read_b64_tr_b16 v[146:147], v165 offset:10240\n"
"v_mfma_f32_16x16x32_bf16 v[40:43], v[200:203], v[132:135], v[40:43]\n"
"buffer_load_dwordx4 v170, s[36:39], s66 offen offset:1024 lds\n"
"v_mfma_f32_16x16x32_bf16 v[44:47], v[204:207], v[132:135], v[44:47]\n"
"v_mfma_f32_16x16x32_bf16 v[48:51], v[192:195], v[136:139], v[48:51]\n"
"ds_read_b64_tr_b16 v[128:129], v164 offset:3072\n"
"ds_read_b64_tr_b16 v[130:131], v164 offset:11264\n"
"v_mfma_f32_16x16x32_bf16 v[52:55], v[196:199], v[136:139], v[52:55]\n"
"buffer_load_dwordx4 v171, s[28:31], s66 offen offset:2048 lds\n"
"v_mfma_f32_16x16x32_bf16 v[56:59], v[200:203], v[136:139], v[56:59]\n"
"v_mfma_f32_16x16x32_bf16 v[60:63], v[204:207], v[136:139], v[60:63]\n"
"s_waitcnt lgkmcnt(2)\n"
"v_mfma_f32_16x16x32_bf16 v[0:3], v[208:211], v[140:143], v[0:3]\n"
"ds_read_b64_tr_b16 v[132:133], v165 offset:3072\n"
"ds_read_b64_tr_b16 v[134:135], v165 offset:11264\n"
"v_mfma_f32_16x16x32_bf16 v[4:7], v[212:215], v[140:143], v[4:7]\n"
"buffer_load_dwordx4 v172, s[36:39], s66 offen offset:3072 lds\n"
"v_mfma_f32_16x16x32_bf16 v[8:11], v[216:219], v[140:143], v[8:11]\n"
"v_mfma_f32_16x16x32_bf16 v[12:15], v[220:223], v[140:143], v[12:15]\n"
"v_mfma_f32_16x16x32_bf16 v[16:19], v[208:211], v[144:147], v[16:19]\n"
"s_waitcnt vmcnt(12)\n"
"s_barrier\n"
"v_mfma_f32_16x16x32_bf16 v[20:23], v[212:215], v[144:147], v[20:23]\n"
"ds_read_b64_tr_b16 v[136:137], v160\n"
"ds_read_b64_tr_b16 v[138:139], v160 offset:8192\n"
"v_mfma_f32_16x16x32_bf16 v[24:27], v[216:219], v[144:147], v[24:27]\n"
"s_add_u32 m0, s65, 65536\n"
"s_add_u32 s66, s70, 2359296\n"
"buffer_load_dwordx4 v169, s[28:31], s66 offen lds\n"
"v_mfma_f32_16x16x32_bf16 v[28:31], v[220:223], v[144:147], v[28:31]\n"
"s_waitcnt lgkmcnt(2)\n"
"v_mfma_f32_16x16x32_bf16 v[32:35], v[208:211], v[128:131], v[32:35]\n"
"ds_read_b64_tr_b16 v[140:141], v161\n"
"ds_read_b64_tr_b16 v[142:143], v161 offset:8192\n"
"v_mfma_f32_16x16x32_bf16 v[36:39], v[212:215], v[128:131], v[36:39]\n"
"buffer_load_dwordx4 v170, s[36:39], s66 offen offset:1024 lds\n"
"v_mfma_f32_16x16x32_bf16 v[40:43], v[216:219], v[128:131], v[40:43]\n"
"v_mfma_f32_16x16x32_bf16 v[44:47], v[220:223], v[128:131], v[44:47]\n"
"v_mfma_f32_16x16x32_bf16 v[48:51], v[208:211], v[132:135], v[48:51]\n"
"ds_read_b64_tr_b16 v[144:145], v160 offset:1024\n"
"ds_read_b64_tr_b16 v[146:147], v160 offset:9216\n"
"v_mfma_f32_16x16x32_bf16 v[52:55], v[212:215], v[132:135], v[52:55]\n"
"buffer_load_dwordx4 v171, s[28:31], s66 offen offset:2048 lds\n"
"v_mfma_f32_16x16x32_bf16 v[56:59], v[216:219], v[132:135], v[56:59]\n"
"v_mfma_f32_16x16x32_bf16 v[60:63], v[220:223], v[132:135], v[60:63]\n"
"s_waitcnt lgkmcnt(2)\n"
"v_mfma_f32_16x16x32_bf16 v[64:67], v[136:139], a[0:3], 0\n"
"ds_read_b64_tr_b16 v[128:129], v161 offset:1024\n"
"ds_read_b64_tr_b16 v[130:131], v161 offset:9216\n"
"v_mfma_f32_16x16x32_bf16 v[68:71], v[136:139], a[4:7], 0\n"
"buffer_load_dwordx4 v172, s[36:39], s66 offen offset:3072 lds\n"
"v_mfma_f32_16x16x32_bf16 v[72:75], v[136:139], a[8:11], 0\n"
"v_mfma_f32_16x16x32_bf16 v[76:79], v[136:139], a[12:15], 0\n"
"v_mfma_f32_16x16x32_bf16 v[80:83], v[140:143], a[0:3], 0\n"
"ds_read_b64_tr_b16 v[132:133], v160 offset:2048\n"
"ds_read_b64_tr_b16 v[134:135], v160 offset:10240\n"
"v_mfma_f32_16x16x32_bf16 v[84:87], v[140:143], a[4:7], 0\n"
"s_lshl_b32 s16, s62, 12\n"
"s_lshl_b32 s17, s61, 11\n"
"s_add_u32 s16, s16, s17\n"
"s_add_u32 s16, s16, 114688\n"
"v_add_u32 v156, s16, v179\n"
"v_mfma_f32_16x16x32_bf16 v[88:91], v[140:143], a[8:11], 0\n"
"v_mul_f32 v148, s77, v0\n"
"v_mul_f32 v149, s77, v1\n"
"v_mfma_f32_16x16x32_bf16 v[92:95], v[140:143], a[12:15], 0\n"
"v_max_f32 v150, 0, v0\n"
"s_waitcnt lgkmcnt(2)\n"
"v_mfma_f32_16x16x32_bf16 v[96:99], v[144:147], a[0:3], 0\n"
"ds_read_b64_tr_b16 v[136:137], v161 offset:2048\n"
"ds_read_b64_tr_b16 v[138:139], v161 offset:10240\n"
"v_mfma_f32_16x16x32_bf16 v[100:103], v[144:147], a[4:7], 0\n"
"s_add_u32 m0, s65, 81920\n"
"s_add_u32 s66, s70, 2949120\n"
"buffer_load_dwordx4 v169, s[28:31], s66 offen lds\n"
"v_mfma_f32_16x16x32_bf16 v[104:107], v[144:147], a[8:11], 0\n"
"s_mul_i32 s18, s64, 36\n"
"s_mul_i32 s10, s60, 9\n"
"s_add_u32 s18, s18, s10\n"
"v_lshrrev_b32 v182, 4, v158\n"
"v_cmp_gt_u32 vcc, 9, v182\n"
"v_add_u32 v184, s18, v182\n"
"s_movk_i32 s18, 9216\n"
"v_mul_lo_u32 v184, s18, v184\n"
"v_mov_b32 v185, 2147483647\n"
"v_cndmask_b32 v184, v185, v184, vcc\n"
"s_add_u32 s18, s70, 3328\n"
"buffer_load_dword v186, v184, s[28:31], s18 offen\n"
"buffer_load_dword v186, v184, s[36:39], s18 offen\n"
"v_mfma_f32_16x16x32_bf16 v[108:111], v[144:147], a[12:15], 0\n"
"v_exp_f32_e64 v148, v148 clamp\n"
"v_mfma_f32_16x16x32_bf16 v[112:115], v[128:131], a[0:3], 0\n"
"ds_read_b64_tr_b16 v[140:141], v160 offset:3072\n"
"ds_read_b64_tr_b16 v[142:143], v160 offset:11264\n"
"v_mfma_f32_16x16x32_bf16 v[116:119], v[128:131], a[4:7], 0\n"
"v_exp_f32_e64 v149, v149 clamp\n"
"v_mfma_f32_16x16x32_bf16 v[120:123], v[128:131], a[8:11], 0\n"
"v_max_f32 v151, 0, v1\n"
"v_fma_f32 v148, v150, s76, v148\n"
"v_mfma_f32_16x16x32_bf16 v[124:127], v[128:131], a[12:15], 0\n"
"v_fma_f32 v149, v151, s76, v149\n"
"s_waitcnt lgkmcnt(2)\n"
"v_mfma_f32_16x16x32_bf16 v[64:67], v[132:135], a[16:19], v[64:67]\n"
"ds_read_b64_tr_b16 v[144:145], v161 offset:3072\n"
"ds_read_b64_tr_b16 v[146:147], v161 offset:11264\n"
"v_mfma_f32_16x16x32_bf16 v[68:71], v[132:135], a[20:23], v[68:71]\n"
"buffer_load_dwordx4 v170, s[36:39], s66 offen offset:1024 lds\n"
"v_add_f32 v0, s78, v148\n"
"v_mfma_f32_16x16x32_bf16 v[72:75], v[132:135], a[24:27], v[72:75]\n"
"v_add_f32 v1, s78, v149\n"
"v_cvt_pk_bf16_f32 v152, v0, v1\n"
"v_mfma_f32_16x16x32_bf16 v[76:79], v[132:135], a[28:31], v[76:79]\n"
"v_mul_f32 v148, s77, v2\n"
"v_mul_f32 v149, s77, v3\n"
"v_mfma_f32_16x16x32_bf16 v[80:83], v[136:139], a[16:19], v[80:83]\n"
"s_waitcnt vmcnt(16)\n"
"s_barrier\n"
"v_mfma_f32_16x16x32_bf16 v[84:87], v[136:139], a[20:23], v[84:87]\n"
"ds_read_b64_tr_b16 v[128:129], v160 offset:16384\n"
"ds_read_b64_tr_b16 v[130:131], v160 offset:24576\n"
"v_mfma_f32_16x16x32_bf16 v[88:91], v[136:139], a[24:27], v[88:91]\n"
"buffer_load_dwordx4 v171, s[28:31], s66 offen offset:2048 lds\n"
"v_max_f32 v150, 0, v2\n"
"v_mfma_f32_16x16x32_bf16 v[92:95], v[136:139], a[28:31], v[92:95]\n"
"v_max_f32 v151, 0, v3\n"
"s_waitcnt lgkmcnt(2)\n"
"v_mfma_f32_16x16x32_bf16 v[96:99], v[140:143], a[16:19], v[96:99]\n"
"ds_read_b64_tr_b16 v[132:133], v161 offset:16384\n"
"ds_read_b64_tr_b16 v[134:135], v161 offset:24576\n"
"v_mfma_f32_16x16x32_bf16 v[100:103], v[140:143], a[20:23], v[100:103]\n"
"buffer_load_dwordx4 v172, s[36:39], s66 offen offset:3072 lds\n"
"v_mfma_f32_16x16x32_bf16 v[104:107], v[140:143], a[24:27], v[104:107]\n"
"v_exp_f32_e64 v148, v148 clamp\n"
"v_mfma_f32_16x16x32_bf16 v[108:111], v[140:143], a[28:31], v[108:111]\n"
"v_exp_f32_e64 v149, v149 clamp\n"
"v_mfma_f32_16x16x32_bf16 v[112:115], v[144:147], a[16:19], v[112:115]\n"
"ds_read_b64_tr_b16 v[136:137], v160 offset:17408\n"
"ds_read_b64_tr_b16 v[138:139], v160 offset:25600\n"
"v_mfma_f32_16x16x32_bf16 v[116:119], v[144:147], a[20:23], v[116:119]\n"
"v_fma_f32 v148, v150, s76, v148\n"
"v_fma_f32 v149, v151, s76, v149\n"
"v_mfma_f32_16x16x32_bf16 v[120:123], v[144:147], a[24:27], v[120:123]\n"
"v_add_f32 v2, s78, v148\n"
"v_add_f32 v3, s78, v149\n"
"v_mfma_f32_16x16x32_bf16 v[124:127], v[144:147], a[28:31], v[124:127]\n"
"v_cvt_pk_bf16_f32 v153, v2, v3\n"
"s_waitcnt lgkmcnt(2)\n"
"v_mfma_f32_16x16x32_bf16 v[64:67], v[128:131], a[32:35], v[64:67]\n"
"ds_read_b64_tr_b16 v[140:141], v161 offset:17408\n"
"ds_read_b64_tr_b16 v[142:143], v161 offset:25600\n"
"v_mfma_f32_16x16x32_bf16 v[68:71], v[128:131], a[36:39], v[68:71]\n"
"s_add_u32 m0, s65, 131072\n"
"s_add_u32 s66, s70, 3538944\n"
"buffer_load_dwordx4 v169, s[28:31], s66 offen lds\n"
"v_mfma_f32_16x16x32_bf16 v[72:75], v[128:131], a[40:43], v[72:75]\n"
"v_mul_f32 v148, s77, v4\n"
"v_mul_f32 v149, s77, v5\n"
"v_mfma_f32_16x16x32_bf16 v[76:79], v[128:131], a[44:47], v[76:79]\n"
"v_exp_f32_e64 v148, v148 clamp\n"
"v_mfma_f32_16x16x32_bf16 v[80:83], v[132:135], a[32:35], v[80:83]\n"
"ds_read_b64_tr_b16 v[144:145], v160 offset:18432\n"
"ds_read_b64_tr_b16 v[146:147], v160 offset:26624\n"
"v_mfma_f32_16x16x32_bf16 v[84:87], v[132:135], a[36:39], v[84:87]\n"
"v_exp_f32_e64 v149, v149 clamp\n"
"v_mfma_f32_16x16x32_bf16 v[88:91], v[132:135], a[40:43], v[88:91]\n"
"v_max_f32 v150, 0, v4\n"
"v_max_f32 v151, 0, v5\n"
"v_mfma_f32_16x16x32_bf16 v[92:95], v[132:135], a[44:47], v[92:95]\n"
"v_fma_f32 v148, v150, s76, v148\n"
"s_waitcnt lgkmcnt(2)\n"
"v_mfma_f32_16x16x32_bf16 v[96:99], v[136:139], a[32:35], v[96:99]\n"
"ds_read_b64_tr_b16 v[128:129], v161 offset:18432\n"
"ds_read_b64_tr_b16 v[130:131], v161 offset:26624\n"
"v_mfma_f32_16x16x32_bf16 v[100:103], v[136:139], a[36:39], v[100:103]\n"
"buffer_load_dwordx4 v170, s[36:39], s66 offen offset:1024 lds\n"
"v_fma_f32 v149, v151, s76, v149\n"
"v_mfma_f32_16x16x32_bf16 v[104:107], v[136:139], a[40:43], v[104:107]\n"
"v_add_f32 v4, s78, v148\n"
"v_add_f32 v5, s78, v149\n"
"v_mfma_f32_16x16x32_bf16 v[108:111], v[136:139], a[44:47], v[108:111]\n"
"v_cvt_pk_bf16_f32 v154, v4, v5\n"
"v_mul_f32 v148, s77, v6\n"
"v_mfma_f32_16x16x32_bf16 v[112:115], v[140:143], a[32:35], v[112:115]\n"
"ds_read_b64_tr_b16 v[132:133], v160 offset:19456\n"
"ds_read_b64_tr_b16 v[134:135], v160 offset:27648\n"
"v_mfma_f32_16x16x32_bf16 v[116:119], v[140:143], a[36:39], v[116:119]\n"
"v_mul_f32 v149, s77, v7\n"
"v_max_f32 v150, 0, v6\n"
"v_mfma_f32_16x16x32_bf16 v[120:123], v[140:143], a[40:43], v[120:123]\n"
"v_exp_f32_e64 v148, v148 clamp\n"
"v_mfma_f32_16x16x32_bf16 v[124:127], v[140:143], a[44:47], v[124:127]\n"
"v_max_f32 v151, 0, v7\n"
"s_waitcnt lgkmcnt(2)\n"
"v_mfma_f32_16x16x32_bf16 v[64:67], v[144:147], a[48:51], v[64:67]\n"
"ds_read_b64_tr_b16 v[136:137], v161 offset:19456\n"
"ds_read_b64_tr_b16 v[138:139], v161 offset:27648\n"
"v_mfma_f32_16x16x32_bf16 v[68:71], v[144:147], a[52:55], v[68:71]\n"
"buffer_load_dwordx4 v171, s[28:31], s66 offen offset:2048 lds\n"
"v_fma_f32 v148, v150, s76, v148\n"
"v_mfma_f32_16x16x32_bf16 v[72:75], v[144:147], a[56:59], v[72:75]\n"
"v_exp_f32_e64 v149, v149 clamp\n"
"v_mfma_f32_16x16x32_bf16 v[76:79], v[144:147], a[60:63], v[76:79]\n"
"v_fma_f32 v149, v151, s76, v149\n"
"v_add_f32 v6, s78, v148\n"
"v_mfma_f32_16x16x32_bf16 v[80:83], v[128:131], a[48:51], v[80:83]\n"
"s_waitcnt vmcnt(17)\n"
"s_barrier\n"
"v_mfma_f32_16x16x32_bf16 v[84:87], v[128:131], a[52:55], v[84:87]\n"
"ds_read_b64_tr_b16 v[140:141], v160 offset:32768\n"
"ds_read_b64_tr_b16 v[142:143], v160 offset:40960\n"
"v_mfma_f32_16x16x32_bf16 v[88:91], v[128:131], a[56:59], v[88:91]\n"
"buffer_load_dwordx4 v172, s[36:39], s66 offen offset:3072 lds\n"
"v_add_f32 v7, s78, v149\n"
"v_mfma_f32_16x16x32_bf16 v[92:95], v[128:131], a[60:63], v[92:95]\n"
"v_cvt_pk_bf16_f32 v155, v6, v7\n"
"s_waitcnt lgkmcnt(2)\n"
"v_mfma_f32_16x16x32_bf16 v[96:99], v[132:135], a[48:51], v[96:99]\n"
"ds_read_b64_tr_b16 v[144:145], v161 offset:32768\n"
"ds_read_b64_tr_b16 v[146:147], v161 offset:40960\n"
"v_mfma_f32_16x16x32_bf16 v[100:103], v[132:135], a[52:55], v[100:103]\n"
"ds_write_b128 v156, v[152:155]\n"
"v_mul_f32 v148, s77, v8\n"
"v_mfma_f32_16x16x32_bf16 v[104:107], v[132:135], a[56:59], v[104:107]\n"
"v_mul_f32 v149, s77, v9\n"
"v_max_f32 v150, 0, v8\n"
"v_mfma_f32_16x16x32_bf16 v[108:111], v[132:135], a[60:63], v[108:111]\n"
"v_exp_f32_e64 v148, v148 clamp\n"
"v_mfma_f32_16x16x32_bf16 v[112:115], v[136:139], a[48:51], v[112:115]\n"
"ds_read_b64_tr_b16 v[128:129], v160 offset:33792\n"
"ds_read_b64_tr_b16 v[130:131], v160 offset:41984\n"
"v_mfma_f32_16x16x32_bf16 v[116:119], v[136:139], a[52:55], v[116:119]\n"
"s_add_u32 m0, s65, 0\n"
"s_add_u32 s66, s70, 4128768\n"
"buffer_load_dwordx4 v169, s[28:31], s66 offen lds\n"
"v_mfma_f32_16x16x32_bf16 v[120:123], v[136:139], a[56:59], v[120:123]\n"
"v_exp_f32_e64 v149, v149 clamp\n"
"v_mfma_f32_16x16x32_bf16 v[124:127], v[136:139], a[60:63], v[124:127]\n"
"v_max_f32 v151, 0, v9\n"
"s_waitcnt lgkmcnt(3)\n"
"v_mfma_f32_16x16x32_bf16 v[64:67], v[140:143], a[64:67], v[64:67]\n"
"ds_read_b64_tr_b16 v[132:133], v161 offset:33792\n"
"ds_read_b64_tr_b16 v[134:135], v161 offset:41984\n"
"v_mfma_f32_16x16x32_bf16 v[68:71], v[140:143], a[68:71], v[68:71]\n"
"v_fma_f32 v148, v150, s76, v148\n"
"v_fma_f32 v149, v151, s76, v149\n"
"v_mfma_f32_16x16x32_bf16 v[72:75], v[140:143], a[72:75], v[72:75]\n"
"v_add_f32 v8, s78, v148\n"
"v_add_f32 v9, s78, v149\n"
"v_mfma_f32_16x16x32_bf16 v[76:79], v[140:143], a[76:79], v[76:79]\n"
"v_cvt_pk_bf16_f32 v152, v8, v9\n"
"v_mul_f32 v148, s77, v10\n"
"v_mfma_f32_16x16x32_bf16 v[80:83], v[144:147], a[64:67], v[80:83]\n"
"ds_read_b64_tr_b16 v[136:137], v160 offset:34816\n"
"ds_read_b64_tr_b16 v[138:139], v160 offset:43008\n"
"v_mfma_f32_16x16x32_bf16 v[84:87], v[144:147], a[68:71], v[84:87]\n"
"buffer_load_dwordx4 v170, s[36:39], s66 offen offset:1024 lds\n"
"v_mul_f32 v149, s77, v11\n"
"v_mfma_f32_16x16x32_bf16 v[88:91], v[144:147], a[72:75], v[88:91]\n"
"v_exp_f32_e64 v148, v148 clamp\n"
"v_mfma_f32_16x16x32_bf16 v[92:95], v[144:147], a[76:79], v[92:95]\n"
"v_max_f32 v150, 0, v10\n"
"s_waitcnt lgkmcnt(2)\n"
"v_mfma_f32_16x16x32_bf16 v[96:99], v[128:131], a[64:67], v[96:99]\n"
"ds_read_b64_tr_b16 v[140:141], v161 offset:34816\n"
"ds_read_b64_tr_b16 v[142:143], v161 offset:43008\n"
"v_mfma_f32_16x16x32_bf16 v[100:103], v[128:131], a[68:71], v[100:103]\n"
"v_exp_f32_e64 v149, v149 clamp\n"
"v_mfma_f32_16x16x32_bf16 v[104:107], v[128:131], a[72:75], v[104:107]\n"
"v_max_f32 v151, 0, v11\n"
"v_fma_f32 v148, v150, s76, v148\n"
"v_mfma_f32_16x16x32_bf16 v[108:111], v[128:131], a[76:79], v[108:111]\n"
"v_fma_f32 v149, v151, s76, v149\n"
"v_add_f32 v10, s78, v148\n"
"v_mfma_f32_16x16x32_bf16 v[112:115], v[132:135], a[64:67], v[112:115]\n"
"ds_read_b64_tr_b16 v[144:145], v160 offset:35840\n"
"ds_read_b64_tr_b16 v[146:147], v160 offset:44032\n"
"v_mfma_f32_16x16x32_bf16 v[116:119], v[132:135], a[68:71], v[116:119]\n"
"buffer_load_dwordx4 v171, s[28:31], s66 offen offset:2048 lds\n"
"v_add_f32 v11, s78, v149\n"
"v_mfma_f32_16x16x32_bf16 v[120:123], v[132:135], a[72:75], v[120:123]\n"
"v_cvt_pk_bf16_f32 v153, v10, v11\n"
"v_mul_f32 v148, s77, v12\n"
"v_mfma_f32_16x16x32_bf16 v[124:127], v[132:135], a[76:79], v[124:127]\n"
"v_mul_f32 v149, s77, v13\n"
"s_waitcnt lgkmcnt(2)\n"
"v_mfma_f32_16x16x32_bf16 v[64:67], v[136:139], a[80:83], v[64:67]\n"
"ds_read_b64_tr_b16 v[128:129], v161 offset:35840\n"
"ds_read_b64_tr_b16 v[130:131], v161 offset:44032\n"
"v_mfma_f32_16x16x32_bf16 v[68:71], v[136:139], a[84:87], v[68:71]\n"
"v_exp_f32_e64 v148, v148 clamp\n"
"v_mfma_f32_16x16x32_bf16 v[72:75], v[136:139], a[88:91], v[72:75]\n"
"v_exp_f32_e64 v149, v149 clamp\n"
"v_mfma_f32_16x16x32_bf16 v[76:79], v[136:139], a[92:95], v[76:79]\n"
"v_max_f32 v150, 0, v12\n"
"v_max_f32 v151, 0, v13\n"
"v_mfma_f32_16x16x32_bf16 v[80:83], v[140:143], a[80:83], v[80:83]\n"
"s_waitcnt vmcnt(17)\n"
"s_barrier\n"
"v_mfma_f32_16x16x32_bf16 v[84:87], v[140:143], a[84:87], v[84:87]\n"
"ds_read_b64_tr_b16 v[132:133], v160 offset:49152\n"
"ds_read_b64_tr_b16 v[134:135], v160 offset:57344\n"
"v_mfma_f32_16x16x32_bf16 v[88:91], v[140:143], a[88:91], v[88:91]\n"
"buffer_load_dwordx4 v172, s[36:39], s66 offen offset:3072 lds\n"
"v_fma_f32 v148, v150, s76, v148\n"
"v_mfma_f32_16x16x32_bf16 v[92:95], v[140:143], a[92:95], v[92:95]\n"
"v_fma_f32 v149, v151, s76, v149\n"
"s_waitcnt lgkmcnt(2)\n"
"v_mfma_f32_16x16x32_bf16 v[96:99], v[144:147], a[80:83], v[96:99]\n"
"ds_read_b64_tr_b16 v[136:137], v161 offset:49152\n"
"ds_read_b64_tr_b16 v[138:139], v161 offset:57344\n"
"v_mfma_f32_16x16x32_bf16 v[100:103], v[144:147], a[84:87], v[100:103]\n"
"v_add_f32 v12, s78, v148\n"
"v_add_f32 v13, s78, v149\n"
"v_mfma_f32_16x16x32_bf16 v[104:107], v[144:147], a[88:91], v[104:107]\n"
"v_cvt_pk_bf16_f32 v154, v12, v13\n"
"v_mul_f32 v148, s77, v14\n"
"v_mfma_f32_16x16x32_bf16 v[108:111], v[144:147], a[92:95], v[108:111]\n"
"v_mul_f32 v149, s77, v15\n"
"v_max_f32 v150, 0, v14\n"
"v_mfma_f32_16x16x32_bf16 v[112:115], v[128:131], a[80:83], v[112:115]\n"
"ds_read_b64_tr_b16 v[140:141], v160 offset:50176\n"
"ds_read_b64_tr_b16 v[142:143], v160 offset:58368\n"
"v_mfma_f32_16x16x32_bf16 v[116:119], v[128:131], a[84:87], v[116:119]\n"
"s_add_u32 m0, s65, 16384\n"
"s_add_u32 s66, s70, 4718592\n"
"buffer_load_dwordx4 v169, s[28:31], s66 offen lds\n"
"v_mfma_f32_16x16x32_bf16 v[120:123], v[128:131], a[88:91], v[120:123]\n"
"v_exp_f32_e64 v148, v148 clamp\n"
"v_mfma_f32_16x16x32_bf16 v[124:127], v[128:131], a[92:95], v[124:127]\n"
"v_max_f32 v151, 0, v15\n"
"s_waitcnt lgkmcnt(2)\n"
"v_mfma_f32_16x16x32_bf16 v[64:67], v[132:135], a[96:99], v[64:67]\n"
"ds_read_b64_tr_b16 v[144:145], v161 offset:50176\n"
"ds_read_b64_tr_b16 v[146:147], v161 offset:58368\n"
"v_mfma_f32_16x16x32_bf16 v[68:71], v[132:135], a[100:103], v[68:71]\n"
"v_exp_f32_e64 v149, v149 clamp\n"
"v_mfma_f32_16x16x32_bf16 v[72:75], v[132:135], a[104:107], v[72:75]\n"
"v_fma_f32 v148, v150, s76, v148\n"
"v_fma_f32 v149, v151, s76, v149\n"
"v_mfma_f32_16x16x32_bf16 v[76:79], v[132:135], a[108:111], v[76:79]\n"
"v_add_f32 v14, s78, v148\n"
"v_add_f32 v15, s78, v149\n"
"v_mfma_f32_16x16x32_bf16 v[80:83], v[136:139], a[96:99], v[80:83]\n"
"ds_read_b64_tr_b16 v[128:129], v160 offset:51200\n"
"ds_read_b64_tr_b16 v[130:131], v160 offset:59392\n"
"v_mfma_f32_16x16x32_bf16 v[84:87], v[136:139], a[100:103], v[84:87]\n"
"buffer_load_dwordx4 v170, s[36:39], s66 offen offset:1024 lds\n"
"v_cvt_pk_bf16_f32 v155, v14, v15\n"
"v_mfma_f32_16x16x32_bf16 v[88:91], v[136:139], a[104:107], v[88:91]\n"
"ds_write_b128 v156, v[152:155] offset:1024\n"
"v_mfma_f32_16x16x32_bf16 v[92:95], v[136:139], a[108:111], v[92:95]\n"
"s_waitcnt lgkmcnt(3)\n"
"v_mfma_f32_16x16x32_bf16 v[96:99], v[140:143], a[96:99], v[96:99]\n"
"ds_read_b64_tr_b16 v[132:133], v161 offset:51200\n"
"ds_read_b64_tr_b16 v[134:135], v161 offset:59392\n"
"v_mfma_f32_16x16x32_bf16 v[100:103], v[140:143], a[100:103], v[100:103]\n"
"s_lshl_b32 s16, s62, 12\n"
"s_lshl_b32 s17, s61, 11\n"
"s_add_u32 s16, s16, s17\n"
"s_add_u32 s16, s16, 122880\n"
"v_add_u32 v156, s16, v179\n"
"v_mfma_f32_16x16x32_bf16 v[104:107], v[140:143], a[104:107], v[104:107]\n"
"v_cvt_pk_bf16_f32 v152, v16, v17\n"
"v_cvt_pk_bf16_f32 v153, v18, v19\n"
"v_mfma_f32_16x16x32_bf16 v[108:111], v[140:143], a[108:111], v[108:111]\n"
"v_cvt_pk_bf16_f32 v154, v20, v21\n"
"v_cvt_pk_bf16_f32 v155, v22, v23\n"
"v_mfma_f32_16x16x32_bf16 v[112:115], v[144:147], a[96:99], v[112:115]\n"
"ds_read_b64_tr_b16 v[136:137], v160 offset:52224\n"
"ds_read_b64_tr_b16 v[138:139], v160 offset:60416\n"
"v_mfma_f32_16x16x32_bf16 v[116:119], v[144:147], a[100:103], v[116:119]\n"
"buffer_load_dwordx4 v171, s[28:31], s66 offen offset:2048 lds\n"
"ds_write_b128 v156, v[152:155]\n"
"v_mfma_f32_16x16x32_bf16 v[120:123], v[144:147], a[104:107], v[120:123]\n"
"v_cvt_pk_bf16_f32 v152, v24, v25\n"
"v_cvt_pk_bf16_f32 v153, v26, v27\n"
"v_mfma_f32_16x16x32_bf16 v[124:127], v[144:147], a[108:111], v[124:127]\n"
"v_cvt_pk_bf16_f32 v154, v28, v29\n"
"s_waitcnt lgkmcnt(3)\n"
"v_mfma_f32_16x16x32_bf16 v[64:67], v[128:131], a[112:115], v[64:67]\n"
"ds_read_b64_tr_b16 v[140:141], v161 offset:52224\n"
"ds_read_b64_tr_b16 v[142:143], v161 offset:60416\n"
"v_mfma_f32_16x16x32_bf16 v[68:71], v[128:131], a[116:119], v[68:71]\n"
"v_cvt_pk_bf16_f32 v155, v30, v31\n"
"ds_write_b128 v156, v[152:155] offset:1024\n"
"v_mfma_f32_16x16x32_bf16 v[72:75], v[128:131], a[120:123], v[72:75]\n"
"v_lshrrev_b32 v148, 4, v158\n"
"v_and_b32 v149, 15, v148\n"
"v_lshrrev_b32 v148, 4, v148\n"
"v_lshlrev_b32 v148, 3, v148\n"
"v_lshl_add_u32 v157, v149, 5, v148\n"
"v_add_u32 v157, %[lds], v157\n"
"v_mfma_f32_16x16x32_bf16 v[76:79], v[128:131], a[124:127], v[76:79]\n"
"s_lshl_b32 s16, s61, 13\n"
"s_lshl_b32 s17, s62, 9\n"
"s_add_u32 s16, s16, s17\n"
"s_add_u32 s16, s16, 147456\n"
"v_add_u32 v156, s16, v157\n"
"v_mfma_f32_16x16x32_bf16 v[80:83], v[132:135], a[112:115], v[80:83]\n"
"s_waitcnt vmcnt(17)\n"
"s_barrier\n"
"v_mfma_f32_16x16x32_bf16 v[84:87], v[132:135], a[116:119], v[84:87]\n"
"ds_read_b64_tr_b16 v[144:145], v162\n"
"ds_read_b64_tr_b16 v[146:147], v162 offset:8192\n"
"v_mfma_f32_16x16x32_bf16 v[88:91], v[132:135], a[120:123], v[88:91]\n"
"buffer_load_dwordx4 v172, s[36:39], s66 offen offset:3072 lds\n"
"v_mul_f32 v148, s77, v32\n"
"v_mfma_f32_16x16x32_bf16 v[92:95], v[132:135], a[124:127], v[92:95]\n"
"v_mul_f32 v149, s77, v33\n"
"s_waitcnt lgkmcnt(3)\n"
"v_mfma_f32_16x16x32_bf16 v[96:99], v[136:139], a[112:115], v[96:99]\n"
"ds_read_b64_tr_b16 v[128:129], v163\n"
"ds_read_b64_tr_b16 v[130:131], v163 offset:8192\n"
"v_mfma_f32_16x16x32_bf16 v[100:103], v[136:139], a[116:119], v[100:103]\n"
"v_exp_f32_e64 v148, v148 clamp\n"
"v_mfma_f32_16x16x32_bf16 v[104:107], v[136:139], a[120:123], v[104:107]\n"
"v_exp_f32_e64 v149, v149 clamp\n"
"v_mfma_f32_16x16x32_bf16 v[108:111], v[136:139], a[124:127], v[108:111]\n"
"v_max_f32 v150, 0, v32\n"
"v_max_f32 v151, 0, v33\n"
"v_mfma_f32_16x16x32_bf16 v[112:115], v[140:143], a[112:115], v[112:115]\n"
"ds_read_b64_tr_b16 v[132:133], v162 offset:1024\n"
"ds_read_b64_tr_b16 v[134:135], v162 offset:9216\n"
"v_mfma_f32_16x16x32_bf16 v[116:119], v[140:143], a[116:119], v[116:119]\n"
"s_add_u32 m0, s65, 32768\n"
"s_add_u32 s66, s70, 128\n"
"buffer_load_dwordx4 v169, s[28:31], s66 offen lds\n"
"v_mfma_f32_16x16x32_bf16 v[120:123], v[140:143], a[120:123], v[120:123]\n"
"v_fma_f32 v148, v150, s76, v148\n"
"v_fma_f32 v149, v151, s76, v149\n"
"v_mfma_f32_16x16x32_bf16 v[124:127], v[140:143], a[124:127], v[124:127]\n"
"v_add_f32 v32, s78, v148\n"
"s_waitcnt lgkmcnt(2)\n"
"v_mfma_f32_16x16x32_bf16 v[64:67], v[144:147], a[128:131], v[64:67]\n"
"ds_read_b64_tr_b16 v[136:137], v163 offset:1024\n"
"ds_read_b64_tr_b16 v[138:139], v163 offset:9216\n"
"v_mfma_f32_16x16x32_bf16 v[68:71], v[144:147], a[132:135], v[68:71]\n"
"v_add_f32 v33, s78, v149\n"
"v_cvt_pk_bf16_f32 v152, v32, v33\n"
"v_mfma_f32_16x16x32_bf16 v[72:75], v[144:147], a[136:139], v[72:75]\n"
"v_mul_f32 v148, s77, v34\n"
"v_mul_f32 v149, s77, v35\n"
"v_mfma_f32_16x16x32_bf16 v[76:79], v[144:147], a[140:143], v[76:79]\n"
"v_exp_f32_e64 v148, v148 clamp\n"
"v_mfma_f32_16x16x32_bf16 v[80:83], v[128:131], a[128:131], v[80:83]\n"
"ds_read_b64_tr_b16 v[140:141], v162 offset:2048\n"
"ds_read_b64_tr_b16 v[142:143], v162 offset:10240\n"
"v_mfma_f32_16x16x32_bf16 v[84:87], v[128:131], a[132:135], v[84:87]\n"
"buffer_load_dwordx4 v170, s[36:39], s66 offen offset:1024 lds\n"
"v_max_f32 v150, 0, v34\n"
"v_mfma_f32_16x16x32_bf16 v[88:91], v[128:131], a[136:139], v[88:91]\n"
"v_exp_f32_e64 v149, v149 clamp\n"
"v_mfma_f32_16x16x32_bf16 v[92:95], v[128:131], a[140:143], v[92:95]\n"
"v_max_f32 v151, 0, v35\n"
"s_waitcnt lgkmcnt(2)\n"
"v_mfma_f32_16x16x32_bf16 v[96:99], v[132:135], a[128:131], v[96:99]\n"
"ds_read_b64_tr_b16 v[144:145], v163 offset:2048\n"
"ds_read_b64_tr_b16 v[146:147], v163 offset:10240\n"
"v_mfma_f32_16x16x32_bf16 v[100:103], v[132:135], a[132:135], v[100:103]\n"
"v_fma_f32 v148, v150, s76, v148\n"
"v_fma_f32 v149, v151, s76, v149\n"
"v_mfma_f32_16x16x32_bf16 v[104:107], v[132:135], a[136:139], v[104:107]\n"
"v_add_f32 v34, s78, v148\n"
"v_add_f32 v35, s78, v149\n"
"v_mfma_f32_16x16x32_bf16 v[108:111], v[132:135], a[140:143], v[108:111]\n"
"v_cvt_pk_bf16_f32 v153, v34, v35\n"
"ds_write_b64 v156, v[152:153]\n"
"v_mfma_f32_16x16x32_bf16 v[112:115], v[136:139], a[128:131], v[112:115]\n"
"ds_read_b64_tr_b16 v[128:129], v162 offset:3072\n"
"ds_read_b64_tr_b16 v[130:131], v162 offset:11264\n"
"v_mfma_f32_16x16x32_bf16 v[116:119], v[136:139], a[132:135], v[116:119]\n"
"buffer_load_dwordx4 v171, s[28:31], s66 offen offset:2048 lds\n"
"v_mul_f32 v148, s77, v36\n"
"v_mfma_f32_16x16x32_bf16 v[120:123], v[136:139], a[136:139], v[120:123]\n"
"v_mul_f32 v149, s77, v37\n"
"v_max_f32 v150, 0, v36\n"
"v_mfma_f32_16x16x32_bf16 v[124:127], v[136:139], a[140:143], v[124:127]\n"
"v_max_f32 v151, 0, v37\n"
"s_waitcnt lgkmcnt(3)\n"
"v_mfma_f32_16x16x32_bf16 v[64:67], v[140:143], a[144:147], v[64:67]\n"
"ds_read_b64_tr_b16 v[132:133], v163 offset:3072\n"
"ds_read_b64_tr_b16 v[134:135], v163 offset:11264\n"
"v_mfma_f32_16x16x32_bf16 v[68:71], v[140:143], a[148:151], v[68:71]\n"
"v_exp_f32_e64 v148, v148 clamp\n"
"v_mfma_f32_16x16x32_bf16 v[72:75], v[140:143], a[152:155], v[72:75]\n"
"v_exp_f32_e64 v149, v149 clamp\n"
"v_mfma_f32_16x16x32_bf16 v[76:79], v[140:143], a[156:159], v[76:79]\n"
"v_fma_f32 v148, v150, s76, v148\n"
"v_fma_f32 v149, v151, s76, v149\n"
"v_mfma_f32_16x16x32_bf16 v[80:83], v[144:147], a[144:147], v[80:83]\n"
"s_waitcnt vmcnt(15)\n"
"s_barrier\n"
"v_mfma_f32_16x16x32_bf16 v[84:87], v[144:147], a[148:151], v[84:87]\n"
"ds_read_b64_tr_b16 v[136:137], v162 offset:16384\n"
"ds_read_b64_tr_b16 v[138:139], v162 offset:24576\n"
"v_mfma_f32_16x16x32_bf16 v[88:91], v[144:147], a[152:155], v[88:91]\n"
"buffer_load_dwordx4 v172, s[36:39], s66 offen offset:3072 lds\n"
"v_add_f32 v36, s78, v148\n"
"v_mfma_f32_16x16x32_bf16 v[92:95], v[144:147], a[156:159], v[92:95]\n"
"v_add_f32 v37, s78, v149\n"
"s_waitcnt lgkmcnt(2)\n"
"v_mfma_f32_16x16x32_bf16 v[96:99], v[128:131], a[144:147], v[96:99]\n"
"ds_read_b64_tr_b16 v[140:141], v163 offset:16384\n"
"ds_read_b64_tr_b16 v[142:143], v163 offset:24576\n"
"v_mfma_f32_16x16x32_bf16 v[100:103], v[128:131], a[148:151], v[100:103]\n"
"v_cvt_pk_bf16_f32 v152, v36, v37\n"
"v_mul_f32 v148, s77, v38\n"
"v_mfma_f32_16x16x32_bf16 v[104:107], v[128:131], a[152:155], v[104:107]\n"
"v_mul_f32 v149, s77, v39\n"
"v_max_f32 v150, 0, v38\n"
"v_mfma_f32_16x16x32_bf16 v[108:111], v[128:131], a[156:159], v[108:111]\n"
"v_exp_f32_e64 v148, v148 clamp\n"
"v_mfma_f32_16x16x32_bf16 v[112:115], v[132:135], a[144:147], v[112:115]\n"
"ds_read_b64_tr_b16 v[144:145], v162 offset:17408\n"
"ds_read_b64_tr_b16 v[146:147], v162 offset:25600\n"
"v_mfma_f32_16x16x32_bf16 v[116:119], v[132:135], a[148:151], v[116:119]\n"
"s_add_u32 m0, s65, 49152\n"
"s_add_u32 s66, s70, 589952\n"
"buffer_load_dwordx4 v169, s[28:31], s66 offen lds\n"
"v_mfma_f32_16x16x32_bf16 v[120:123], v[132:135], a[152:155], v[120:123]\n"
"v_exp_f32_e64 v149, v149 clamp\n"
"v_mfma_f32_16x16x32_bf16 v[124:127], v[132:135], a[156:159], v[124:127]\n"
"v_max_f32 v151, 0, v39\n"
"s_waitcnt lgkmcnt(2)\n"
"v_mfma_f32_16x16x32_bf16 v[64:67], v[136:139], a[160:163], v[64:67]\n"
"ds_read_b64_tr_b16 v[128:129], v163 offset:17408\n"
"ds_read_b64_tr_b16 v[130:131], v163 offset:25600\n"
"v_mfma_f32_16x16x32_bf16 v[68:71], v[136:139], a[164:167], v[68:71]\n"
"s_lshr_b32 s18, %[bid], 3\n"
"s_lshl_b32 s18, s18, 2\n"
"s_add_u32 s18, s18, s60\n"
"s_mul_i32 s18, s18, 41\n"
"s_add_u32 s18, s18, 0\n"
"v_lshrrev_b32 v182, 4, v158\n"
"v_cmp_gt_u32 vcc, 11, v182\n"
"v_add_u32 v184, s18, v182\n"
"v_lshlrev_b32 v184, 7, v184\n"
"v_mov_b32 v185, 2147483647\n"
"v_cndmask_b32 v184, v185, v184, vcc\n"
"s_lshr_b32 s18, s63, 1\n"
"s_mul_i32 s18, s18, 663552\n"
"s_add_u32 s18, s18, 3072\n"
"buffer_load_dword v186, v184, s[40:43], s18 offen\n"
"v_mfma_f32_16x16x32_bf16 v[72:75], v[136:139], a[168:171], v[72:75]\n"
"v_fma_f32 v148, v150, s76, v148\n"
"v_fma_f32 v149, v151, s76, v149\n"
"v_mfma_f32_16x16x32_bf16 v[76:79], v[136:139], a[172:175], v[76:79]\n"
"v_add_f32 v38, s78, v148\n"
"v_add_f32 v39, s78, v149\n"
"v_mfma_f32_16x16x32_bf16 v[80:83], v[140:143], a[160:163], v[80:83]\n"
"ds_read_b64_tr_b16 v[132:133], v162 offset:18432\n"
"ds_read_b64_tr_b16 v[134:135], v162 offset:26624\n"
"v_mfma_f32_16x16x32_bf16 v[84:87], v[140:143], a[164:167], v[84:87]\n"
"buffer_load_dwordx4 v170, s[36:39], s66 offen offset:1024 lds\n"
"v_cvt_pk_bf16_f32 v153, v38, v39\n"
"v_mfma_f32_16x16x32_bf16 v[88:91], v[140:143], a[168:171], v[88:91]\n"
"ds_write_b64 v156, v[152:153] offset:2048\n"
"v_mul_f32 v148, s77, v40\n"
"v_mfma_f32_16x16x32_bf16 v[92:95], v[140:143], a[172:175], v[92:95]\n"
"v_mul_f32 v149, s77, v41\n"
"s_waitcnt lgkmcnt(3)\n"
"v_mfma_f32_16x16x32_bf16 v[96:99], v[144:147], a[160:163], v[96:99]\n"
"ds_read_b64_tr_b16 v[136:137], v163 offset:18432\n"
"ds_read_b64_tr_b16 v[138:139], v163 offset:26624\n"
"v_mfma_f32_16x16x32_bf16 v[100:103], v[144:147], a[164:167], v[100:103]\n"
"v_exp_f32_e64 v148, v148 clamp\n"
"v_mfma_f32_16x16x32_bf16 v[104:107], v[144:147], a[168:171], v[104:107]\n"
"v_exp_f32_e64 v149, v149 clamp\n"
"v_mfma_f32_16x16x32_bf16 v[108:111], v[144:147], a[172:175], v[108:111]\n"
"v_max_f32 v150, 0, v40\n"
"v_max_f32 v151, 0, v41\n"
"v_mfma_f32_16x16x32_bf16 v[112:115], v[128:131], a[160:163], v[112:115]\n"
"ds_read_b64_tr_b16 v[140:141], v162 offset:19456\n"
"ds_read_b64_tr_b16 v[142:143], v162 offset:27648\n"
"v_mfma_f32_16x16x32_bf16 v[116:119], v[128:131], a[164:167], v[116:119]\n"
"buffer_load_dwordx4 v171, s[28:31], s66 offen offset:2048 lds\n"
"v_fma_f32 v148, v150, s76, v148\n"
"v_mfma_f32_16x16x32_bf16 v[120:123], v[128:131], a[168:171], v[120:123]\n"
"v_fma_f32 v149, v151, s76, v149\n"
"v_add_f32 v40, s78, v148\n"
"v_mfma_f32_16x16x32_bf16 v[124:127], v[128:131], a[172:175], v[124:127]\n"
"v_add_f32 v41, s78, v149\n"
"s_waitcnt lgkmcnt(2)\n"
"v_mfma_f32_16x16x32_bf16 v[64:67], v[132:135], a[176:179], v[64:67]\n"
"ds_read_b64_tr_b16 v[144:145], v163 offset:19456\n"
"ds_read_b64_tr_b16 v[146:147], v163 offset:27648\n"
"v_mfma_f32_16x16x32_bf16 v[68:71], v[132:135], a[180:183], v[68:71]\n"
"v_cvt_pk_bf16_f32 v152, v40, v41\n"
"v_mul_f32 v148, s77, v42\n"
"v_mfma_f32_16x16x32_bf16 v[72:75], v[132:135], a[184:187], v[72:75]\n"
"v_mul_f32 v149, s77, v43\n"
"v_max_f32 v150, 0, v42\n"
"v_mfma_f32_16x16x32_bf16 v[76:79], v[132:135], a[188:191], v[76:79]\n"
"v_exp_f32_e64 v148, v148 clamp\n"
"v_mfma_f32_16x16x32_bf16 v[80:83], v[136:139], a[176:179], v[80:83]\n"
"s_waitcnt vmcnt(16)\n"
"s_barrier\n"
"v_mfma_f32_16x16x32_bf16 v[84:87], v[136:139], a[180:183], v[84:87]\n"
"ds_read_b64_tr_b16 v[128:129], v164\n"
"ds_read_b64_tr_b16 v[130:131], v164 offset:8192\n"
"v_mfma_f32_16x16x32_bf16 v[88:91], v[136:139], a[184:187], v[88:91]\n"
"buffer_load_dwordx4 v172, s[36:39], s66 offen offset:3072 lds\n"
"v_max_f32 v151, 0, v43\n"
"v_mfma_f32_16x16x32_bf16 v[92:95], v[136:139], a[188:191], v[92:95]\n"
"v_fma_f32 v148, v150, s76, v148\n"
"s_waitcnt lgkmcnt(2)\n"
"v_mfma_f32_16x16x32_bf16 v[96:99], v[140:143], a[176:179], v[96:99]\n"
"ds_read_b64_tr_b16 v[132:133], v165\n"
"ds_read_b64_tr_b16 v[134:135], v165 offset:8192\n"
"v_mfma_f32_16x16x32_bf16 v[100:103], v[140:143], a[180:183], v[100:103]\n"
"v_exp_f32_e64 v149, v149 clamp\n"
"v_mfma_f32_16x16x32_bf16 v[104:107], v[140:143], a[184:187], v[104:107]\n"
"v_fma_f32 v149, v151, s76, v149\n"
"v_add_f32 v42, s78, v148\n"
"v_mfma_f32_16x16x32_bf16 v[108:111], v[140:143], a[188:191], v[108:111]\n"
"v_add_f32 v43, s78, v149\n"
"v_cvt_pk_bf16_f32 v153, v42, v43\n"
"v_mfma_f32_16x16x32_bf16 v[112:115], v[144:147], a[176:179], v[112:115]\n"
"ds_read_b64_tr_b16 v[136:137], v164 offset:1024\n"
"ds_read_b64_tr_b16 v[138:139], v164 offset:9216\n"
"v_mfma_f32_16x16x32_bf16 v[116:119], v[144:147], a[180:183], v[116:119]\n"
"s_add_u32 m0, s65, 65536\n"
"s_add_u32 s66, s70, 1179776\n"
"buffer_load_dwordx4 v169, s[28:31], s66 offen lds\n"
"v_mfma_f32_16x16x32_bf16 v[120:123], v[144:147], a[184:187], v[120:123]\n"
"ds_write_b64 v156, v[152:153] offset:4096\n"
"v_mul_f32 v148, s77, v44\n"
"v_mfma_f32_16x16x32_bf16 v[124:127], v[144:147], a[188:191], v[124:127]\n"
"v_mul_f32 v149, s77, v45\n"
"s_waitcnt lgkmcnt(3)\n"
"v_mfma_f32_16x16x32_bf16 v[64:67], v[128:131], a[192:195], v[64:67]\n"
"ds_read_b64_tr_b16 v[140:141], v165 offset:1024\n"
"ds_read_b64_tr_b16 v[142:143], v165 offset:9216\n"
"v_mfma_f32_16x16x32_bf16 v[68:71], v[128:131], a[196:199], v[68:71]\n"
"v_exp_f32_e64 v148, v148 clamp\n"
"v_mfma_f32_16x16x32_bf16 v[72:75], v[128:131], a[200:203], v[72:75]\n"
"v_exp_f32_e64 v149, v149 clamp\n"
"v_mfma_f32_16x16x32_bf16 v[76:79], v[128:131], a[204:207], v[76:79]\n"
"v_max_f32 v150, 0, v44\n"
"v_max_f32 v151, 0, v45\n"
"v_mfma_f32_16x16x32_bf16 v[80:83], v[132:135], a[192:195], v[80:83]\n"
"ds_read_b64_tr_b16 v[144:145], v164 offset:2048\n"
"ds_read_b64_tr_b16 v[146:147], v164 offset:10240\n"
"v_mfma_f32_16x16x32_bf16 v[84:87], v[132:135], a[196:199], v[84:87]\n"
"buffer_load_dwordx4 v170, s[36:39], s66 offen offset:1024 lds\n"
"v_fma_f32 v148, v150, s76, v148\n"
"v_mfma_f32_16x16x32_bf16 v[88:91], v[132:135], a[200:203], v[88:91]\n"
"v_fma_f32 v149, v151, s76, v149\n"
"v_add_f32 v44, s78, v148\n"
"v_mfma_f32_16x16x32_bf16 v[92:95], v[132:135], a[204:207], v[92:95]\n"
"v_add_f32 v45, s78, v149\n"
"s_waitcnt lgkmcnt(2)\n"
"v_mfma_f32_16x16x32_bf16 v[96:99], v[136:139], a[192:195], v[96:99]\n"
"ds_read_b64_tr_b16 v[128:129], v165 offset:2048\n"
"ds_read_b64_tr_b16 v[130:131], v165 offset:10240\n"
"v_mfma_f32_16x16x32_bf16 v[100:103], v[136:139], a[196:199], v[100:103]\n"
"v_cvt_pk_bf16_f32 v152, v44, v45\n"
"v_mul_f32 v148, s77, v46\n"
"v_mfma_f32_16x16x32_bf16 v[104:107], v[136:139], a[200:203], v[104:107]\n"
"v_mul_f32 v149, s77, v47\n"
"v_max_f32 v150, 0, v46\n"
"v_mfma_f32_16x16x32_bf16 v[108:111], v[136:139], a[204:207], v[108:111]\n"
"v_exp_f32_e64 v148, v148 clamp\n"
"v_mfma_f32_16x16x32_bf16 v[112:115], v[140:143], a[192:195], v[112:115]\n"
"ds_read_b64_tr_b16 v[132:133], v164 offset:3072\n"
"ds_read_b64_tr_b16 v[134:135], v164 offset:11264\n"
"v_mfma_f32_16x16x32_bf16 v[116:119], v[140:143], a[196:199], v[116:119]\n"
"buffer_load_dwordx4 v171, s[28:31], s66 offen offset:2048 lds\n"
"v_max_f32 v151, 0, v47\n"
"v_mfma_f32_16x16x32_bf16 v[120:123], v[140:143], a[200:203], v[120:123]\n"
"v_exp_f32_e64 v149, v149 clamp\n"
"v_mfma_f32_16x16x32_bf16 v[124:127], v[140:143], a[204:207], v[124:127]\n"
"v_fma_f32 v148, v150, s76, v148\n"
"s_waitcnt lgkmcnt(2)\n"
"v_mfma_f32_16x16x32_bf16 v[64:67], v[144:147], a[208:211], v[64:67]\n"
"ds_read_b64_tr_b16 v[136:137], v165 offset:3072\n"
"ds_read_b64_tr_b16 v[138:139], v165 offset:11264\n"
"v_mfma_f32_16x16x32_bf16 v[68:71], v[144:147], a[212:215], v[68:71]\n"
"v_fma_f32 v149, v151, s76, v149\n"
"v_add_f32 v46, s78, v148\n"
"v_mfma_f32_16x16x32_bf16 v[72:75], v[144:147], a[216:219], v[72:75]\n"
"v_add_f32 v47, s78, v149\n"
"v_cvt_pk_bf16_f32 v153, v46, v47\n"
"v_mfma_f32_16x16x32_bf16 v[76:79], v[144:147], a[220:223], v[76:79]\n"
"ds_write_b64 v156, v[152:153] offset:6144\n"
"v_mfma_f32_16x16x32_bf16 v[80:83], v[128:131], a[208:211], v[80:83]\n"
"s_waitcnt vmcnt(16)\n"
"s_barrier\n"
"v_mfma_f32_16x16x32_bf16 v[84:87], v[128:131], a[212:215], v[84:87]\n"
"ds_read_b64_tr_b16 v[140:141], v160\n"
"ds_read_b64_tr_b16 v[142:143], v160 offset:8192\n"
"v_mfma_f32_16x16x32_bf16 v[88:91], v[128:131], a[216:219], v[88:91]\n"
"buffer_load_dwordx4 v172, s[36:39], s66 offen offset:3072 lds\n"
"v_mfma_f32_16x16x32_bf16 v[92:95], v[128:131], a[220:223], v[92:95]\n"
"s_waitcnt lgkmcnt(3)\n"
"v_mfma_f32_16x16x32_bf16 v[96:99], v[132:135], a[208:211], v[96:99]\n"
"ds_read_b64_tr_b16 v[144:145], v161\n"
"ds_read_b64_tr_b16 v[146:147], v161 offset:8192\n"
"v_mfma_f32_16x16x32_bf16 v[100:103], v[132:135], a[212:215], v[100:103]\n"
"s_lshl_b32 s16, s61, 11\n"
"s_add_u32 s16, s16, 98304\n"
"v_add_u32 v148, s16, v179\n"
"s_lshl_b32 s16, s61, 13\n"
"s_add_u32 s16, s16, 148480\n"
"v_add_u32 v149, s16, v157\n"
"s_cmp_eq_u32 s62, 0\n"
"s_cselect_b64 s[10:11], -1, 0\n"
"v_cndmask_b32_e64 v157, v149, v148, s[10:11]\n"
"v_mfma_f32_16x16x32_bf16 v[104:107], v[132:135], a[216:219], v[104:107]\n"
"v_mul_f32 v148, s77, v48\n"
"v_mul_f32 v149, s77, v49\n"
"v_mfma_f32_16x16x32_bf16 v[108:111], v[132:135], a[220:223], v[108:111]\n"
"v_exp_f32_e64 v148, v148 clamp\n"
"v_mfma_f32_16x16x32_bf16 v[112:115], v[136:139], a[208:211], v[112:115]\n"
"ds_read_b64_tr_b16 v[128:129], v160 offset:1024\n"
"ds_read_b64_tr_b16 v[130:131], v160 offset:9216\n"
"v_mfma_f32_16x16x32_bf16 v[116:119], v[136:139], a[212:215], v[116:119]\n"
"s_add_u32 m0, s65, 81920\n"
"s_add_u32 s66, s70, 1769600\n"
"buffer_load_dwordx4 v169, s[28:31], s66 offen lds\n"
"v_mfma_f32_16x16x32_bf16 v[120:123], v[136:139], a[216:219], v[120:123]\n"
"v_exp_f32_e64 v149, v149 clamp\n"
"v_mfma_f32_16x16x32_bf16 v[124:127], v[136:139], a[220:223], v[124:127]\n"
"v_max_f32 v150, 0, v48\n"
"s_waitcnt lgkmcnt(2)\n"
"v_mfma_f32_16x16x32_bf16 v[64:67], v[140:143], a[224:227], v[64:67]\n"
"ds_read_b64_tr_b16 v[132:133], v161 offset:1024\n"
"ds_read_b64_tr_b16 v[134:135], v161 offset:9216\n"
"v_mfma_f32_16x16x32_bf16 v[68:71], v[140:143], a[228:231], v[68:71]\n"
"v_max_f32 v151, 0, v49\n"
"v_fma_f32 v148, v150, s76, v148\n"
"v_mfma_f32_16x16x32_bf16 v[72:75], v[140:143], a[232:235], v[72:75]\n"
"v_fma_f32 v149, v151, s76, v149\n"
"v_add_f32 v148, s78, v148\n"
"v_mfma_f32_16x16x32_bf16 v[76:79], v[140:143], a[236:239], v[76:79]\n"
"v_add_f32 v149, s78, v149\n"
"v_cndmask_b32_e64 v48, v48, v148, s[80:81]\n"
"v_mfma_f32_16x16x32_bf16 v[80:83], v[144:147], a[224:227], v[80:83]\n"
"ds_read_b64_tr_b16 v[136:137], v160 offset:2048\n"
"ds_read_b64_tr_b16 v[138:139], v160 offset:10240\n"
"v_mfma_f32_16x16x32_bf16 v[84:87], v[144:147], a[228:231], v[84:87]\n"
"buffer_load_dwordx4 v170, s[36:39], s66 offen offset:1024 lds\n"
"v_cndmask_b32_e64 v49, v49, v149, s[80:81]\n"
"v_mfma_f32_16x16x32_bf16 v[88:91], v[144:147], a[232:235], v[88:91]\n"
"v_cndmask_b32_e64 v48, v48, 1.0, s[82:83]\n"
"v_cndmask_b32_e64 v49, v49, 1.0, s[82:83]\n"
"v_mfma_f32_16x16x32_bf16 v[92:95], v[144:147], a[236:239], v[92:95]\n"
"v_cvt_pk_bf16_f32 v152, v48, v49\n"
"s_waitcnt lgkmcnt(2)\n"
"v_mfma_f32_16x16x32_bf16 v[96:99], v[128:131], a[224:227], v[96:99]\n"
"ds_read_b64_tr_b16 v[140:141], v161 offset:2048\n"
"ds_read_b64_tr_b16 v[142:143], v161 offset:10240\n"
"v_mfma_f32_16x16x32_bf16 v[100:103], v[128:131], a[228:231], v[100:103]\n"
"v_mul_f32 v148, s77, v50\n"
"v_mul_f32 v149, s77, v51\n"
"v_mfma_f32_16x16x32_bf16 v[104:107], v[128:131], a[232:235], v[104:107]\n"
"v_exp_f32_e64 v148, v148 clamp\n"
"v_mfma_f32_16x16x32_bf16 v[108:111], v[128:131], a[236:239], v[108:111]\n"
"v_exp_f32_e64 v149, v149 clamp\n"
"v_mfma_f32_16x16x32_bf16 v[112:115], v[132:135], a[224:227], v[112:115]\n"
"ds_read_b64_tr_b16 v[144:145], v160 offset:3072\n"
"ds_read_b64_tr_b16 v[146:147], v160 offset:11264\n"
"v_mfma_f32_16x16x32_bf16 v[116:119], v[132:135], a[228:231], v[116:119]\n"
"buffer_load_dwordx4 v171, s[28:31], s66 offen offset:2048 lds\n"
"v_max_f32 v150, 0, v50\n"
"v_mfma_f32_16x16x32_bf16 v[120:123], v[132:135], a[232:235], v[120:123]\n"
"v_max_f32 v151, 0, v51\n"
"v_fma_f32 v148, v150, s76, v148\n"
"v_mfma_f32_16x16x32_bf16 v[124:127], v[132:135], a[236:239], v[124:127]\n"
"v_fma_f32 v149, v151, s76, v149\n"
"s_waitcnt lgkmcnt(2)\n"
"v_mfma_f32_16x16x32_bf16 v[64:67], v[136:139], a[240:243], v[64:67]\n"
"ds_read_b64_tr_b16 v[128:129], v161 offset:3072\n"
"ds_read_b64_tr_b16 v[130:131], v161 offset:11264\n"
"v_mfma_f32_16x16x32_bf16 v[68:71], v[136:139], a[244:247], v[68:71]\n"
"v_add_f32 v148, s78, v148\n"
"v_add_f32 v149, s78, v149\n"
"v_mfma_f32_16x16x32_bf16 v[72:75], v[136:139], a[248:251], v[72:75]\n"
"v_cndmask_b32_e64 v50, v50, v148, s[80:81]\n"
"v_cndmask_b32_e64 v51, v51, v149, s[80:81]\n"
"v_mfma_f32_16x16x32_bf16 v[76:79], v[136:139], a[252:255], v[76:79]\n"
"v_cndmask_b32_e64 v50, v50, 1.0, s[82:83]\n"
"v_cndmask_b32_e64 v51, v51, 1.0, s[82:83]\n"
"v_mfma_f32_16x16x32_bf16 v[80:83], v[140:143], a[240:243], v[80:83]\n"
"s_waitcnt vmcnt(16)\n"
"s_barrier\n"
"v_mfma_f32_16x16x32_bf16 v[84:87], v[140:143], a[244:247], v[84:87]\n"
"ds_read_b64_tr_b16 v[132:133], v160 offset:16384\n"
"ds_read_b64_tr_b16 v[134:135], v160 offset:24576\n"
"v_mfma_f32_16x16x32_bf16 v[88:91], v[140:143], a[248:251], v[88:91]\n"
"buffer_load_dwordx4 v172, s[36:39], s66 offen offset:3072 lds\n"
"v_cvt_pk_bf16_f32 v153, v50, v51\n"
"v_mfma_f32_16x16x32_bf16 v[92:95], v[140:143], a[252:255], v[92:95]\n"
"s_waitcnt lgkmcnt(2)\n"
"v_mfma_f32_16x16x32_bf16 v[96:99], v[144:147], a[240:243], v[96:99]\n"
"ds_read_b64_tr_b16 v[136:137], v161 offset:16384\n"
"ds_read_b64_tr_b16 v[138:139], v161 offset:24576\n"
"v_mfma_f32_16x16x32_bf16 v[100:103], v[144:147], a[244:247], v[100:103]\n"
"v_add_u32 v156, s12, v157\n"
"ds_write_b64 v156, v[152:153]\n"
"v_mfma_f32_16x16x32_bf16 v[104:107], v[144:147], a[248:251], v[104:107]\n"
"v_mul_f32 v148, s77, v52\n"
"v_mul_f32 v149, s77, v53\n"
"v_mfma_f32_16x16x32_bf16 v[108:111], v[144:147], a[252:255], v[108:111]\n"
"v_exp_f32_e64 v148, v148 clamp\n"
"v_mfma_f32_16x16x32_bf16 v[112:115], v[128:131], a[240:243], v[112:115]\n"
"ds_read_b64_tr_b16 v[140:141], v160 offset:17408\n"
"ds_read_b64_tr_b16 v[142:143], v160 offset:25600\n"
"v_mfma_f32_16x16x32_bf16 v[116:119], v[128:131], a[244:247], v[116:119]\n"
"v_exp_f32_e64 v149, v149 clamp\n"
"v_mfma_f32_16x16x32_bf16 v[120:123], v[128:131], a[248:251], v[120:123]\n"
"v_max_f32 v150, 0, v52\n"
"v_max_f32 v151, 0, v53\n"
"v_mfma_f32_16x16x32_bf16 v[124:127], v[128:131], a[252:255], v[124:127]\n"
"v_fma_f32 v148, v150, s76, v148\n"
"s_waitcnt lgkmcnt(3)\n"
"v_mfma_f32_16x16x32_bf16 v[64:67], v[132:135], v[192:195], v[64:67]\n"
"ds_read_b64_tr_b16 v[144:145], v161 offset:17408\n"
"ds_read_b64_tr_b16 v[146:147], v161 offset:25600\n"
"v_mfma_f32_16x16x32_bf16 v[68:71], v[132:135], v[196:199], v[68:71]\n"
"v_fma_f32 v149, v151, s76, v149\n"
"v_add_f32 v148, s78, v148\n"
"v_mfma_f32_16x16x32_bf16 v[72:75], v[132:135], v[200:203], v[72:75]\n"
"v_add_f32 v149, s78, v149\n"
"v_cndmask_b32_e64 v52, v52, v148, s[80:81]\n"
"v_mfma_f32_16x16x32_bf16 v[76:79], v[132:135], v[204:207], v[76:79]\n"
"v_cndmask_b32_e64 v53, v53, v149, s[80:81]\n"
"v_cndmask_b32_e64 v52, v52, 1.0, s[82:83]\n"
"v_mfma_f32_16x16x32_bf16 v[80:83], v[136:139], v[192:195], v[80:83]\n"
"ds_read_b64_tr_b16 v[128:129], v160 offset:18432\n"
"ds_read_b64_tr_b16 v[130:131], v160 offset:26624\n"
"v_mfma_f32_16x16x32_bf16 v[84:87], v[136:139], v[196:199], v[84:87]\n"
"v_cndmask_b32_e64 v53, v53, 1.0, s[82:83]\n"
"v_cvt_pk_bf16_f32 v152, v52, v53\n"
"v_mfma_f32_16x16x32_bf16 v[88:91], v[136:139], v[200:203], v[88:91]\n"
"v_mul_f32 v148, s77, v54\n"
"v_mul_f32 v149, s77, v55\n"
"v_mfma_f32_16x16x32_bf16 v[92:95], v[136:139], v[204:207], v[92:95]\n"
"v_max_f32 v150, 0, v54\n"
"s_waitcnt lgkmcnt(2)\n"
"v_mfma_f32_16x16x32_bf16 v[96:99], v[140:143], v[192:195], v[96:99]\n"
"ds_read_b64_tr_b16 v[132:133], v161 offset:18432\n"
"ds_read_b64_tr_b16 v[134:135], v161 offset:26624\n"
"v_mfma_f32_16x16x32_bf16 v[100:103], v[140:143], v[196:199], v[100:103]\n"
"v_exp_f32_e64 v148, v148 clamp\n"
"v_mfma_f32_16x16x32_bf16 v[104:107], v[140:143], v[200:203], v[104:107]\n"
"v_exp_f32_e64 v149, v149 clamp\n"
"v_mfma_f32_16x16x32_bf16 v[108:111], v[140:143], v[204:207], v[108:111]\n"
"v_max_f32 v151, 0, v55\n"
"v_fma_f32 v148, v150, s76, v148\n"
"v_mfma_f32_16x16x32_bf16 v[112:115], v[144:147], v[192:195], v[112:115]\n"
"ds_read_b64_tr_b16 v[136:137], v160 offset:19456\n"
"ds_read_b64_tr_b16 v[138:139], v160 offset:27648\n"
"v_mfma_f32_16x16x32_bf16 v[116:119], v[144:147], v[196:199], v[116:119]\n"
"v_fma_f32 v149, v151, s76, v149\n"
"v_add_f32 v148, s78, v148\n"
"v_mfma_f32_16x16x32_bf16 v[120:123], v[144:147], v[200:203], v[120:123]\n"
"v_add_f32 v149, s78, v149\n"
"v_cndmask_b32_e64 v54, v54, v148, s[80:81]\n"
"v_mfma_f32_16x16x32_bf16 v[124:127], v[144:147], v[204:207], v[124:127]\n"
"v_cndmask_b32_e64 v55, v55, v149, s[80:81]\n"
"s_waitcnt lgkmcnt(2)\n"
"v_mfma_f32_16x16x32_bf16 v[64:67], v[128:131], v[208:211], v[64:67]\n"
"ds_read_b64_tr_b16 v[140:141], v161 offset:19456\n"
"ds_read_b64_tr_b16 v[142:143], v161 offset:27648\n"
"v_mfma_f32_16x16x32_bf16 v[68:71], v[128:131], v[212:215], v[68:71]\n"
"v_cndmask_b32_e64 v54, v54, 1.0, s[82:83]\n"
"v_cndmask_b32_e64 v55, v55, 1.0, s[82:83]\n"
"v_mfma_f32_16x16x32_bf16 v[72:75], v[128:131], v[216:219], v[72:75]\n"
"v_cvt_pk_bf16_f32 v153, v54, v55\n"
"v_mfma_f32_16x16x32_bf16 v[76:79], v[128:131], v[220:223], v[76:79]\n"
"v_add_u32 v156, s13, v157\n"
"ds_write_b64 v156, v[152:153]\n"
"v_mfma_f32_16x16x32_bf16 v[80:83], v[132:135], v[208:211], v[80:83]\n"
"v_mul_f32 v148, s77, v56\n"
"v_mul_f32 v149, s77, v57\n"
"v_exp_f32_e64 v148, v148 clamp\n"
"v_exp_f32_e64 v149, v149 clamp\n"
"v_max_f32 v150, 0, v56\n"
"v_max_f32 v151, 0, v57\n"
"v_fma_f32 v148, v150, s76, v148\n"
"v_fma_f32 v149, v151, s76, v149\n"
"v_add_f32 v148, s78, v148\n"
"v_add_f32 v149, s78, v149\n"
"v_cndmask_b32_e64 v56, v56, v148, s[80:81]\n"
"v_cndmask_b32_e64 v57, v57, v149, s[80:81]\n"
"v_cndmask_b32_e64 v56, v56, 1.0, s[82:83]\n"
"v_cndmask_b32_e64 v57, v57, 1.0, s[82:83]\n"
"v_cvt_pk_bf16_f32 v152, v56, v57\n"
"v_mul_f32 v148, s77, v58\n"
"v_mul_f32 v149, s77, v59\n"
"v_exp_f32_e64 v148, v148 clamp\n"
"v_exp_f32_e64 v149, v149 clamp\n"
"v_max_f32 v150, 0, v58\n"
"v_max_f32 v151, 0, v59\n"
"v_fma_f32 v148, v150, s76, v148\n"
"v_fma_f32 v149, v151, s76, v149\n"
"v_add_f32 v148, s78, v148\n"
"v_add_f32 v149, s78, v149\n"
"v_cndmask_b32_e64 v58, v58, v148, s[80:81]\n"
"v_cndmask_b32_e64 v59, v59, v149, s[80:81]\n"
"v_cndmask_b32_e64 v58, v58, 1.0, s[82:83]\n"
"v_cndmask_b32_e64 v59, v59, 1.0, s[82:83]\n"
"v_cvt_pk_bf16_f32 v153, v58, v59\n"
"v_add_u32 v156, s14, v157\n"
"ds_write_b64 v156, v[152:153]\n"
"v_mul_f32 v148, s77, v60\n"
"v_mul_f32 v149, s77, v61\n"
"v_exp_f32_e64 v148, v148 clamp\n"
"v_exp_f32_e64 v149, v149 clamp\n"
"v_max_f32 v150, 0, v60\n"
"v_max_f32 v151, 0, v61\n"
"v_fma_f32 v148, v150, s76, v148\n"
"v_fma_f32 v149, v151, s76, v149\n"
"v_add_f32 v148, s78, v148\n"
"v_add_f32 v149, s78, v149\n"
"v_cndmask_b32_e64 v60, v60, v148, s[80:81]\n"
"v_cndmask_b32_e64 v61, v61, v149, s[80:81]\n"
"v_cndmask_b32_e64 v60, v60, 1.0, s[82:83]\n"
"v_cndmask_b32_e64 v61, v61, 1.0, s[82:83]\n"
"v_cvt_pk_bf16_f32 v152, v60, v61\n"
"v_mul_f32 v148, s77, v62\n"
"v_mul_f32 v149, s77, v63\n"
"v_exp_f32_e64 v148, v148 clamp\n"
"v_exp_f32_e64 v149, v149 clamp\n"
"v_max_f32 v150, 0, v62\n"
"v_max_f32 v151, 0, v63\n"
"v_fma_f32 v148, v150, s76, v148\n"
"v_fma_f32 v149, v151, s76, v149\n"
"v_add_f32 v148, s78, v148\n"
"v_add_f32 v149, s78, v149\n"
"v_cndmask_b32_e64 v62, v62, v148, s[80:81]\n"
"v_cndmask_b32_e64 v63, v63, v149, s[80:81]\n"
"v_cndmask_b32_e64 v62, v62, 1.0, s[82:83]\n"
"v_cndmask_b32_e64 v63, v63, 1.0, s[82:83]\n"
"v_cvt_pk_bf16_f32 v153, v62, v63\n"
"v_add_u32 v156, s15, v157\n"
"ds_write_b64 v156, v[152:153]\n"
"s_waitcnt lgkmcnt(0)\n"
"s_waitcnt vmcnt(13)\n"
"s_barrier\n"
"v_mfma_f32_16x16x32_bf16 v[84:87], v[132:135], v[212:215], v[84:87]\n"
"ds_read_b64_tr_b16 v[144:145], v160 offset:32768\n"
"ds_read_b64_tr_b16 v[146:147], v160 offset:40960\n"
"v_mfma_f32_16x16x32_bf16 v[88:91], v[132:135], v[216:219], v[88:91]\n"
"s_add_u32 m0, s65, 0\n"
"s_add_u32 s66, s70, 2359424\n"
"buffer_load_dwordx4 v169, s[28:31], s66 offen lds\n"
"v_mfma_f32_16x16x32_bf16 v[92:95], v[132:135], v[220:223], v[92:95]\n"
"v_mfma_f32_16x16x32_bf16 v[96:99], v[136:139], v[208:211], v[96:99]\n"
"ds_read_b64_tr_b16 v[128:129], v161 offset:32768\n"
"ds_read_b64_tr_b16 v[130:131], v161 offset:40960\n"
"v_mfma_f32_16x16x32_bf16 v[100:103], v[136:139], v[212:215], v[100:103]\n"
"v_mfma_f32_16x16x32_bf16 v[104:107], v[136:139], v[216:219], v[104:107]\n"
"v_mfma_f32_16x16x32_bf16 v[108:111], v[136:139], v[220:223], v[108:111]\n"
"v_mfma_f32_16x16x32_bf16 v[112:115], v[140:143], v[208:211], v[112:115]\n"
"ds_read_b64_tr_b16 v[132:133], v160 offset:33792\n"
"ds_read_b64_tr_b16 v[134:135], v160 offset:41984\n"
"v_mfma_f32_16x16x32_bf16 v[116:119], v[140:143], v[212:215], v[116:119]\n"
"buffer_load_dwordx4 v170, s[36:39], s66 offen offset:1024 lds\n"
"v_mfma_f32_16x16x32_bf16 v[120:123], v[140:143], v[216:219], v[120:123]\n"
"v_mfma_f32_16x16x32_bf16 v[124:127], v[140:143], v[220:223], v[124:127]\n"
"s_waitcnt lgkmcnt(2)\n"
"v_mfma_f32_16x16x32_bf16 v[0:3], v[144:147], a[0:3], 0\n"
"ds_read_b64_tr_b16 v[136:137], v161 offset:33792\n"
"ds_read_b64_tr_b16 v[138:139], v161 offset:41984\n"
"v_mfma_f32_16x16x32_bf16 v[4:7], v[144:147], a[4:7], 0\n"
"s_mov_b32 s18, 114688\n"
"s_cmp_eq_u32 s60, 1\n"
"s_cselect_b32 s18, 114688, s18\n"
"s_cmp_eq_u32 s60, 2\n"
"s_cselect_b32 s18, 114688, s18\n"
"s_cmp_eq_u32 s60, 3\n"
"s_cselect_b32 s18, 98304, s18\n"
"v_add_u32 v182, s18, v179\n"
"v_mfma_f32_16x16x32_bf16 v[8:11], v[144:147], a[8:11], 0\n"
"s_mov_b32 s18, 122880\n"
"s_cmp_eq_u32 s60, 1\n"
"s_cselect_b32 s18, 126976, s18\n"
"s_cmp_eq_u32 s60, 2\n"
"s_cselect_b32 s18, 98304, s18\n"
"s_cmp_eq_u32 s60, 3\n"
"s_cselect_b32 s18, 126976, s18\n"
"v_add_u32 v183, s18, v179\n"
"v_mfma_f32_16x16x32_bf16 v[12:15], v[144:147], a[12:15], 0\n"
"s_mov_b32 s18, 118784\n"
"s_cmp_eq_u32 s60, 1\n"
"s_cselect_b32 s18, 118784, s18\n"
"s_cmp_eq_u32 s60, 2\n"
"s_cselect_b32 s18, 118784, s18\n"
"s_cmp_eq_u32 s60, 3\n"
"s_cselect_b32 s18, 98304, s18\n"
"v_add_u32 v184, s18, v179\n"
"v_mfma_f32_16x16x32_bf16 v[16:19], v[128:131], a[0:3], 0\n"
"ds_read_b64_tr_b16 v[140:141], v160 offset:34816\n"
"ds_read_b64_tr_b16 v[142:143], v160 offset:43008\n"
"v_mfma_f32_16x16x32_bf16 v[20:23], v[128:131], a[4:7], 0\n"
"buffer_load_dwordx4 v171, s[28:31], s66 offen offset:2048 lds\n"
"v_add_u32 v156, 131072, v177\n"
"v_mfma_f32_16x16x32_bf16 v[24:27], v[128:131], a[8:11], 0\n"
"s_mov_b32 s18, 122880\n"
"s_cmp_eq_u32 s60, 1\n"
"s_cselect_b32 s18, 126976, s18\n"
"s_cmp_eq_u32 s60, 2\n"
"s_cselect_b32 s18, 98304, s18\n"
"s_cmp_eq_u32 s60, 3\n"
"s_cselect_b32 s18, 98304, s18\n"
"v_add_u32 v185, s18, v179\n"
"v_mfma_f32_16x16x32_bf16 v[28:31], v[128:131], a[12:15], 0\n"
"ds_read_b128 v[224:227], v182\n"
"s_waitcnt lgkmcnt(3)\n"
"v_mfma_f32_16x16x32_bf16 v[32:35], v[132:135], a[0:3], 0\n"
"ds_read_b64_tr_b16 v[144:145], v161 offset:34816\n"
"ds_read_b64_tr_b16 v[146:147], v161 offset:43008\n"
"v_mfma_f32_16x16x32_bf16 v[36:39], v[132:135], a[4:7], 0\n"
"ds_read_b128 v[228:231], v183\n"
"ds_read_b128 v[232:235], v184\n"
"v_mfma_f32_16x16x32_bf16 v[40:43], v[132:135], a[8:11], 0\n"
"ds_read_b128 v[236:239], v185\n"
"s_waitcnt lgkmcnt(0)\n"
"v_mfma_f32_16x16x32_bf16 v[44:47], v[132:135], a[12:15], 0\n"
"v_mfma_f32_16x16x32_bf16 v[240:243], v[224:227], v[228:231], 0\n"
"v_mfma_f32_16x16x32_bf16 v[48:51], v[136:139], a[0:3], 0\n"
"ds_read_b64_tr_b16 v[128:129], v160 offset:35840\n"
"ds_read_b64_tr_b16 v[130:131], v160 offset:44032\n"
"v_mfma_f32_16x16x32_bf16 v[52:55], v[136:139], a[4:7], 0\n"
"buffer_load_dwordx4 v172, s[36:39], s66 offen offset:3072 lds\n"
"v_mfma_f32_16x16x32_bf16 v[56:59], v[136:139], a[8:11], 0\n"
"v_mfma_f32_16x16x32_bf16 v[244:247], v[232:235], v[236:239], 0\n"
"v_mfma_f32_16x16x32_bf16 v[60:63], v[136:139], a[12:15], 0\n"
"ds_read_b128 v[224:227], v182 offset:1024\n"
"v_mfma_f32_16x16x32_bf16 v[0:3], v[140:143], a[16:19], v[0:3]\n"
"ds_read_b64_tr_b16 v[132:133], v161 offset:35840\n"
"ds_read_b64_tr_b16 v[134:135], v161 offset:44032\n"
"v_mfma_f32_16x16x32_bf16 v[4:7], v[140:143], a[20:23], v[4:7]\n"
"ds_read_b128 v[228:231], v183 offset:1024\n"
"ds_read_b128 v[232:235], v184 offset:1024\n"
"v_mfma_f32_16x16x32_bf16 v[8:11], v[140:143], a[24:27], v[8:11]\n"
"ds_read_b128 v[236:239], v185 offset:1024\n"
"s_waitcnt lgkmcnt(0)\n"
"v_mfma_f32_16x16x32_bf16 v[12:15], v[140:143], a[28:31], v[12:15]\n"
"v_mfma_f32_16x16x32_bf16 v[240:243], v[224:227], v[228:231], v[240:243]\n"
"v_mfma_f32_16x16x32_bf16 v[16:19], v[144:147], a[16:19], v[16:19]\n"
"s_waitcnt vmcnt(12)\n"
"s_barrier\n"
"v_mfma_f32_16x16x32_bf16 v[20:23], v[144:147], a[20:23], v[20:23]\n"
"ds_read_b64_tr_b16 v[136:137], v160 offset:49152\n"
"ds_read_b64_tr_b16 v[138:139], v160 offset:57344\n"
"v_mfma_f32_16x16x32_bf16 v[24:27], v[144:147], a[24:27], v[24:27]\n"
"s_add_u32 m0, s65, 16384\n"
"s_add_u32 s66, s70, 2949248\n"
"buffer_load_dwordx4 v169, s[28:31], s66 offen lds\n"
"v_mfma_f32_16x16x32_bf16 v[28:31], v[144:147], a[28:31], v[28:31]\n"
"v_mfma_f32_16x16x32_bf16 v[32:35], v[128:131], a[16:19], v[32:35]\n"
"ds_read_b64_tr_b16 v[140:141], v161 offset:49152\n"
"ds_read_b64_tr_b16 v[142:143], v161 offset:57344\n"
"v_mfma_f32_16x16x32_bf16 v[36:39], v[128:131], a[20:23], v[36:39]\n"
"v_mfma_f32_16x16x32_bf16 v[244:247], v[232:235], v[236:239], v[244:247]\n"
"v_mfma_f32_16x16x32_bf16 v[40:43], v[128:131], a[24:27], v[40:43]\n"
"ds_read_b128 v[224:227], v182 offset:2048\n"
"ds_read_b128 v[228:231], v183 offset:2048\n"
"v_mfma_f32_16x16x32_bf16 v[44:47], v[128:131], a[28:31], v[44:47]\n"
"ds_read_b128 v[232:235], v184 offset:2048\n"
"ds_read_b128 v[236:239], v185 offset:2048\n"
"v_mfma_f32_16x16x32_bf16 v[48:51], v[132:135], a[16:19], v[48:51]\n"
"ds_read_b64_tr_b16 v[144:145], v160 offset:50176\n"
"ds_read_b64_tr_b16 v[146:147], v160 offset:58368\n"
"v_mfma_f32_16x16x32_bf16 v[52:55], v[132:135], a[20:23], v[52:55]\n"
"buffer_load_dwordx4 v170, s[36:39], s66 offen offset:1024 lds\n"
"s_waitcnt lgkmcnt(2)\n"
"v_mfma_f32_16x16x32_bf16 v[56:59], v[132:135], a[24:27], v[56:59]\n"
"v_mfma_f32_16x16x32_bf16 v[240:243], v[224:227], v[228:231], v[240:243]\n"
"v_mfma_f32_16x16x32_bf16 v[60:63], v[132:135], a[28:31], v[60:63]\n"
"v_mfma_f32_16x16x32_bf16 v[0:3], v[136:139], a[32:35], v[0:3]\n"
"ds_read_b64_tr_b16 v[128:129], v161 offset:50176\n"
"ds_read_b64_tr_b16 v[130:131], v161 offset:58368\n"
"v_mfma_f32_16x16x32_bf16 v[4:7], v[136:139], a[36:39], v[4:7]\n"
"v_mfma_f32_16x16x32_bf16 v[244:247], v[232:235], v[236:239], v[244:247]\n"
"v_mfma_f32_16x16x32_bf16 v[8:11], v[136:139], a[40:43], v[8:11]\n"
"ds_read_b128 v[224:227], v182 offset:3072\n"
"ds_read_b128 v[228:231], v183 offset:3072\n"
"v_mfma_f32_16x16x32_bf16 v[12:15], v[136:139], a[44:47], v[12:15]\n"
"ds_read_b128 v[232:235], v184 offset:3072\n"
"ds_read_b128 v[236:239], v185 offset:3072\n"
"v_mfma_f32_16x16x32_bf16 v[16:19], v[140:143], a[32:35], v[16:19]\n"
"ds_read_b64_tr_b16 v[132:133], v160 offset:51200\n"
"ds_read_b64_tr_b16 v[134:135], v160 offset:59392\n"
"v_mfma_f32_16x16x32_bf16 v[20:23], v[140:143], a[36:39], v[20:23]\n"
"buffer_load_dwordx4 v171, s[28:31], s66 offen offset:2048 lds\n"
"s_waitcnt lgkmcnt(2)\n"
"v_mfma_f32_16x16x32_bf16 v[24:27], v[140:143], a[40:43], v[24:27]\n"
"v_mfma_f32_16x16x32_bf16 v[240:243], v[224:227], v[228:231], v[240:243]\n"
"v_mfma_f32_16x16x32_bf16 v[28:31], v[140:143], a[44:47], v[28:31]\n"
"v_mfma_f32_16x16x32_bf16 v[32:35], v[144:147], a[32:35], v[32:35]\n"
"ds_read_b64_tr_b16 v[136:137], v161 offset:51200\n"
"ds_read_b64_tr_b16 v[138:139], v161 offset:59392\n"
"v_mfma_f32_16x16x32_bf16 v[36:39], v[144:147], a[36:39], v[36:39]\n"
"v_mfma_f32_16x16x32_bf16 v[244:247], v[232:235], v[236:239], v[244:247]\n"
"v_mfma_f32_16x16x32_bf16 v[40:43], v[144:147], a[40:43], v[40:43]\n"
"s_nop 0\n"
"buffer_store_dwordx4 v[240:243], v158, s[48:51], s94 offen sc1\n"
"s_nop 4\n"
"buffer_store_dwordx4 v[244:247], v158, s[48:51], s94 offen offset:1024 sc1\n"
"v_mfma_f32_16x16x32_bf16 v[44:47], v[144:147], a[44:47], v[44:47]\n"
"s_mul_i32 s18, s64, 36\n"
"s_mul_i32 s10, s60, 9\n"
"s_add_u32 s18, s18, s10\n"
"v_lshrrev_b32 v182, 4, v158\n"
"v_cmp_gt_u32 vcc, 9, v182\n"
"v_add_u32 v184, s18, v182\n"
"s_movk_i32 s18, 9216\n"
"v_mul_lo_u32 v184, s18, v184\n"
"v_mov_b32 v185, 2147483647\n"
"v_cndmask_b32 v184, v185, v184, vcc\n"
"s_add_u32 s18, s70, 3456\n"
"buffer_load_dword v186, v184, s[28:31], s18 offen\n"
"buffer_load_dword v186, v184, s[36:39], s18 offen\n"
"v_mfma_f32_16x16x32_bf16 v[48:51], v[128:131], a[32:35], v[48:51]\n"
"ds_read_b64_tr_b16 v[140:141], v160 offset:52224\n"
"ds_read_b64_tr_b16 v[142:143], v160 offset:60416\n"
"v_mfma_f32_16x16x32_bf16 v[52:55], v[128:131], a[36:39], v[52:55]\n"
"buffer_load_dwordx4 v172, s[36:39], s66 offen offset:3072 lds\n"
"v_mfma_f32_16x16x32_bf16 v[56:59], v[128:131], a[40:43], v[56:59]\n"
"s_lshl_b32 s17, s62, 13\n"
"s_add_u32 s17, s17, s27\n"
"s_add_u32 s17, s17, 0\n"
"v_mfma_f32_16x16x32_bf16 v[60:63], v[128:131], a[44:47], v[60:63]\n"
"v_fma_f32 v148, v64, v64, s75\n"
"s_waitcnt lgkmcnt(2)\n"
"v_mfma_f32_16x16x32_bf16 v[0:3], v[132:135], a[48:51], v[0:3]\n"
"ds_read_b64_tr_b16 v[144:145], v161 offset:52224\n"
"ds_read_b64_tr_b16 v[146:147], v161 offset:60416\n"
"v_mfma_f32_16x16x32_bf16 v[4:7], v[132:135], a[52:55], v[4:7]\n"
"v_fma_f32 v149, v65, v65, s75\n"
"v_mul_f32 v148, v148, v64\n"
"v_mfma_f32_16x16x32_bf16 v[8:11], v[132:135], a[56:59], v[8:11]\n"
"v_mul_f32 v149, v149, v65\n"
"v_mul_f32 v96, v64, v96\n"
"v_mfma_f32_16x16x32_bf16 v[12:15], v[132:135], a[60:63], v[12:15]\n"
"v_exp_f32_e64 v148, -v148\n"
"v_mfma_f32_16x16x32_bf16 v[16:19], v[136:139], a[48:51], v[16:19]\n"
"s_waitcnt vmcnt(16)\n"
"s_barrier\n"
"v_mfma_f32_16x16x32_bf16 v[20:23], v[136:139], a[52:55], v[20:23]\n"
"ds_read_b64_tr_b16 v[128:129], v162\n"
"ds_read_b64_tr_b16 v[130:131], v162 offset:8192\n"
"v_mfma_f32_16x16x32_bf16 v[24:27], v[136:139], a[56:59], v[24:27]\n"
"s_add_u32 m0, s65, 32768\n"
"s_add_u32 s66, s70, 3539072\n"
"buffer_load_dwordx4 v169, s[28:31], s66 offen lds\n"
"v_mfma_f32_16x16x32_bf16 v[28:31], v[136:139], a[60:63], v[28:31]\n"
"v_mul_f32 v97, v65, v97\n"
"s_waitcnt lgkmcnt(2)\n"
"v_mfma_f32_16x16x32_bf16 v[32:35], v[140:143], a[48:51], v[32:35]\n"
"ds_read_b64_tr_b16 v[132:133], v163\n"
"ds_read_b64_tr_b16 v[134:135], v163 offset:8192\n"
"v_mfma_f32_16x16x32_bf16 v[36:39], v[140:143], a[52:55], v[36:39]\n"
"v_exp_f32_e64 v149, -v149\n"
"v_mfma_f32_16x16x32_bf16 v[40:43], v[140:143], a[56:59], v[40:43]\n"
"v_add_f32 v148, 1.0, v148\n"
"v_add_f32 v149, 1.0, v149\n"
"v_mfma_f32_16x16x32_bf16 v[44:47], v[140:143], a[60:63], v[44:47]\n"
"v_rcp_f32 v148, v148\n"
"v_mfma_f32_16x16x32_bf16 v[48:51], v[144:147], a[48:51], v[48:51]\n"
"ds_read_b64_tr_b16 v[136:137], v162 offset:1024\n"
"ds_read_b64_tr_b16 v[138:139], v162 offset:9216\n"
"v_mfma_f32_16x16x32_bf16 v[52:55], v[144:147], a[52:55], v[52:55]\n"
"buffer_load_dwordx4 v170, s[36:39], s66 offen offset:1024 lds\n"
"v_mul_f32 v96, v96, v148\n"
"v_mfma_f32_16x16x32_bf16 v[56:59], v[144:147], a[56:59], v[56:59]\n"
"v_rcp_f32 v149, v149\n"
"v_mfma_f32_16x16x32_bf16 v[60:63], v[144:147], a[60:63], v[60:63]\n"
"v_mul_f32 v97, v97, v149\n"
"s_waitcnt lgkmcnt(2)\n"
"v_mfma_f32_16x16x32_bf16 v[0:3], v[128:131], a[64:67], v[0:3]\n"
"ds_read_b64_tr_b16 v[140:141], v163 offset:1024\n"
"ds_read_b64_tr_b16 v[142:143], v163 offset:9216\n"
"v_mfma_f32_16x16x32_bf16 v[4:7], v[128:131], a[68:71], v[4:7]\n"
"v_cvt_pk_bf16_f32 v152, v96, v97\n"
"v_fma_f32 v150, v66, v66, s75\n"
"v_mfma_f32_16x16x32_bf16 v[8:11], v[128:131], a[72:75], v[8:11]\n"
"v_fma_f32 v151, v67, v67, s75\n"
"v_mul_f32 v150, v150, v66\n"
"v_mfma_f32_16x16x32_bf16 v[12:15], v[128:131], a[76:79], v[12:15]\n"
"v_mul_f32 v151, v151, v67\n"
"v_mul_f32 v98, v66, v98\n"
"v_mfma_f32_16x16x32_bf16 v[16:19], v[132:135], a[64:67], v[16:19]\n"
"ds_read_b64_tr_b16 v[144:145], v162 offset:2048\n"
"ds_read_b64_tr_b16 v[146:147], v162 offset:10240\n"
"v_mfma_f32_16x16x32_bf16 v[20:23], v[132:135], a[68:71], v[20:23]\n"
"buffer_load_dwordx4 v171, s[28:31], s66 offen offset:2048 lds\n"
"v_mul_f32 v99, v67, v99\n"
"v_mfma_f32_16x16x32_bf16 v[24:27], v[132:135], a[72:75], v[24:27]\n"
"v_exp_f32_e64 v150, -v150\n"
"v_mfma_f32_16x16x32_bf16 v[28:31], v[132:135], a[76:79], v[28:31]\n"
"v_add_f32 v150, 1.0, v150\n"
"s_waitcnt lgkmcnt(2)\n"
"v_mfma_f32_16x16x32_bf16 v[32:35], v[136:139], a[64:67], v[32:35]\n"
"ds_read_b64_tr_b16 v[128:129], v163 offset:2048\n"
"ds_read_b64_tr_b16 v[130:131], v163 offset:10240\n"
"v_mfma_f32_16x16x32_bf16 v[36:39], v[136:139], a[68:71], v[36:39]\n"
"v_exp_f32_e64 v151, -v151\n"
"v_mfma_f32_16x16x32_bf16 v[40:43], v[136:139], a[72:75], v[40:43]\n"
"v_add_f32 v151, 1.0, v151\n"
"v_fma_f32 v148, v80, v80, s75\n"
"v_mfma_f32_16x16x32_bf16 v[44:47], v[136:139], a[76:79], v[44:47]\n"
"v_rcp_f32 v150, v150\n"
"v_mfma_f32_16x16x32_bf16 v[48:51], v[140:143], a[64:67], v[48:51]\n"
"ds_read_b64_tr_b16 v[132:133], v162 offset:3072\n"
"ds_read_b64_tr_b16 v[134:135], v162 offset:11264\n"
"v_mfma_f32_16x16x32_bf16 v[52:55], v[140:143], a[68:71], v[52:55]\n"
"buffer_load_dwordx4 v172, s[36:39], s66 offen offset:3072 lds\n"
"v_mul_f32 v98, v98, v150\n"
"v_mfma_f32_16x16x32_bf16 v[56:59], v[140:143], a[72:75], v[56:59]\n"
"v_rcp_f32 v151, v151\n"
"v_mfma_f32_16x16x32_bf16 v[60:63], v[140:143], a[76:79], v[60:63]\n"
"v_mul_f32 v99, v99, v151\n"
"s_waitcnt lgkmcnt(2)\n"
"v_mfma_f32_16x16x32_bf16 v[0:3], v[144:147], a[80:83], v[0:3]\n"
"ds_read_b64_tr_b16 v[136:137], v163 offset:3072\n"
"ds_read_b64_tr_b16 v[138:139], v163 offset:11264\n"
"v_mfma_f32_16x16x32_bf16 v[4:7], v[144:147], a[84:87], v[4:7]\n"
"v_cvt_pk_bf16_f32 v153, v98, v99\n"
"v_fma_f32 v149, v81, v81, s75\n"
"v_mfma_f32_16x16x32_bf16 v[8:11], v[144:147], a[88:91], v[8:11]\n"
"v_mul_f32 v148, v148, v80\n"
"v_mul_f32 v149, v149, v81\n"
"v_mfma_f32_16x16x32_bf16 v[12:15], v[144:147], a[92:95], v[12:15]\n"
"v_exp_f32_e64 v148, -v148\n"
"v_mfma_f32_16x16x32_bf16 v[16:19], v[128:131], a[80:83], v[16:19]\n"
"s_waitcnt vmcnt(16)\n"
"s_barrier\n"
"v_mfma_f32_16x16x32_bf16 v[20:23], v[128:131], a[84:87], v[20:23]\n"
"ds_read_b64_tr_b16 v[140:141], v162 offset:16384\n"
"ds_read_b64_tr_b16 v[142:143], v162 offset:24576\n"
"v_mfma_f32_16x16x32_bf16 v[24:27], v[128:131], a[88:91], v[24:27]\n"
"s_add_u32 m0, s65, 49152\n"
"s_add_u32 s66, s70, 4128896\n"
"buffer_load_dwordx4 v169, s[28:31], s66 offen lds\n"
"v_mfma_f32_16x16x32_bf16 v[28:31], v[128:131], a[92:95], v[28:31]\n"
"v_mul_f32 v112, v80, v112\n"
"s_waitcnt lgkmcnt(2)\n"
"v_mfma_f32_16x16x32_bf16 v[32:35], v[132:135], a[80:83], v[32:35]\n"
"ds_read_b64_tr_b16 v[144:145], v163 offset:16384\n"
"ds_read_b64_tr_b16 v[146:147], v163 offset:24576\n"
"v_mfma_f32_16x16x32_bf16 v[36:39], v[132:135], a[84:87], v[36:39]\n"
"v_exp_f32_e64 v149, -v149\n"
"v_mfma_f32_16x16x32_bf16 v[40:43], v[132:135], a[88:91], v[40:43]\n"
"v_mul_f32 v113, v81, v113\n"
"v_add_f32 v148, 1.0, v148\n"
"v_mfma_f32_16x16x32_bf16 v[44:47], v[132:135], a[92:95], v[44:47]\n"
"v_add_f32 v149, 1.0, v149\n"
"v_fma_f32 v150, v82, v82, s75\n"
"v_mfma_f32_16x16x32_bf16 v[48:51], v[136:139], a[80:83], v[48:51]\n"
"ds_read_b64_tr_b16 v[128:129], v162 offset:17408\n"
"ds_read_b64_tr_b16 v[130:131], v162 offset:25600\n"
"v_mfma_f32_16x16x32_bf16 v[52:55], v[136:139], a[84:87], v[52:55]\n"
"buffer_load_dwordx4 v170, s[36:39], s66 offen offset:1024 lds\n"
"v_fma_f32 v151, v83, v83, s75\n"
"v_mfma_f32_16x16x32_bf16 v[56:59], v[136:139], a[88:91], v[56:59]\n"
"v_rcp_f32 v148, v148\n"
"v_mfma_f32_16x16x32_bf16 v[60:63], v[136:139], a[92:95], v[60:63]\n"
"v_mul_f32 v112, v112, v148\n"
"s_waitcnt lgkmcnt(2)\n"
"v_mfma_f32_16x16x32_bf16 v[0:3], v[140:143], a[96:99], v[0:3]\n"
"ds_read_b64_tr_b16 v[132:133], v163 offset:17408\n"
"ds_read_b64_tr_b16 v[134:135], v163 offset:25600\n"
"v_mfma_f32_16x16x32_bf16 v[4:7], v[140:143], a[100:103], v[4:7]\n"
"v_rcp_f32 v149, v149\n"
"v_mfma_f32_16x16x32_bf16 v[8:11], v[140:143], a[104:107], v[8:11]\n"
"v_mul_f32 v113, v113, v149\n"
"v_cvt_pk_bf16_f32 v154, v112, v113\n"
"v_mfma_f32_16x16x32_bf16 v[12:15], v[140:143], a[108:111], v[12:15]\n"
"v_mul_f32 v150, v150, v82\n"
"v_mul_f32 v151, v151, v83\n"
"v_mfma_f32_16x16x32_bf16 v[16:19], v[144:147], a[96:99], v[16:19]\n"
"ds_read_b64_tr_b16 v[136:137], v162 offset:18432\n"
"ds_read_b64_tr_b16 v[138:139], v162 offset:26624\n"
"v_mfma_f32_16x16x32_bf16 v[20:23], v[144:147], a[100:103], v[20:23]\n"
"buffer_load_dwordx4 v171, s[28:31], s66 offen offset:2048 lds\n"
"v_mul_f32 v114, v82, v114\n"
"v_mfma_f32_16x16x32_bf16 v[24:27], v[144:147], a[104:107], v[24:27]\n"
"v_exp_f32_e64 v150, -v150\n"
"v_mfma_f32_16x16x32_bf16 v[28:31], v[144:147], a[108:111], v[28:31]\n"
"v_mul_f32 v115, v83, v115\n"
"s_waitcnt lgkmcnt(2)\n"
"v_mfma_f32_16x16x32_bf16 v[32:35], v[128:131], a[96:99], v[32:35]\n"
"ds_read_b64_tr_b16 v[140:141], v163 offset:18432\n"
"ds_read_b64_tr_b16 v[142:143], v163 offset:26624\n"
"v_mfma_f32_16x16x32_bf16 v[36:39], v[128:131], a[100:103], v[36:39]\n"
"v_exp_f32_e64 v151, -v151\n"
"v_mfma_f32_16x16x32_bf16 v[40:43], v[128:131], a[104:107], v[40:43]\n"
"v_add_f32 v150, 1.0, v150\n"
"v_add_f32 v151, 1.0, v151\n"
"v_mfma_f32_16x16x32_bf16 v[44:47], v[128:131], a[108:111], v[44:47]\n"
"v_rcp_f32 v150, v150\n"
"v_mfma_f32_16x16x32_bf16 v[48:51], v[132:135], a[96:99], v[48:51]\n"
"ds_read_b64_tr_b16 v[144:145], v162 offset:19456\n"
"ds_read_b64_tr_b16 v[146:147], v162 offset:27648\n"
"v_mfma_f32_16x16x32_bf16 v[52:55], v[132:135], a[100:103], v[52:55]\n"
"buffer_load_dwordx4 v172, s[36:39], s66 offen offset:3072 lds\n"
"v_mul_f32 v114, v114, v150\n"
"v_mfma_f32_16x16x32_bf16 v[56:59], v[132:135], a[104:107], v[56:59]\n"
"v_rcp_f32 v151, v151\n"
"v_mfma_f32_16x16x32_bf16 v[60:63], v[132:135], a[108:111], v[60:63]\n"
"v_mul_f32 v115, v115, v151\n"
"s_waitcnt lgkmcnt(2)\n"
"v_mfma_f32_16x16x32_bf16 v[0:3], v[136:139], a[112:115], v[0:3]\n"
"ds_read_b64_tr_b16 v[128:129], v163 offset:19456\n"
"ds_read_b64_tr_b16 v[130:131], v163 offset:27648\n"
"v_mfma_f32_16x16x32_bf16 v[4:7], v[136:139], a[116:119], v[4:7]\n"
"v_cvt_pk_bf16_f32 v155, v114, v115\n"
"ds_write_b128 v156, v[152:155]\n"
"v_mfma_f32_16x16x32_bf16 v[8:11], v[136:139], a[120:123], v[8:11]\n"
"buffer_store_dwordx4 v[152:155], v158, s[48:51], s17 offen sc1\n"
"v_fma_f32 v148, v68, v68, s75\n"
"v_mfma_f32_16x16x32_bf16 v[12:15], v[136:139], a[124:127], v[12:15]\n"
"v_fma_f32 v149, v69, v69, s75\n"
"v_mul_f32 v148, v148, v68\n"
"v_mfma_f32_16x16x32_bf16 v[16:19], v[140:143], a[112:115], v[16:19]\n"
"s_waitcnt vmcnt(17)\n"
"s_barrier\n"
"v_mfma_f32_16x16x32_bf16 v[20:23], v[140:143], a[116:119], v[20:23]\n"
"ds_read_b64_tr_b16 v[132:133], v160\n"
"ds_read_b64_tr_b16 v[134:135], v160 offset:8192\n"
"v_mfma_f32_16x16x32_bf16 v[24:27], v[140:143], a[120:123], v[24:27]\n"
"s_add_u32 m0, s65, 65536\n"
"s_add_u32 s66, s70, 4718720\n"
"buffer_load_dwordx4 v169, s[28:31], s66 offen lds\n"
"v_mfma_f32_16x16x32_bf16 v[28:31], v[140:143], a[124:127], v[28:31]\n"
"v_mul_f32 v149, v149, v69\n"
"s_waitcnt lgkmcnt(3)\n"
"v_mfma_f32_16x16x32_bf16 v[32:35], v[144:147], a[112:115], v[32:35]\n"
"ds_read_b64_tr_b16 v[136:137], v161\n"
"ds_read_b64_tr_b16 v[138:139], v161 offset:8192\n"
"v_mfma_f32_16x16x32_bf16 v[36:39], v[144:147], a[116:119], v[36:39]\n"
"v_exp_f32_e64 v148, -v148\n"
"v_mfma_f32_16x16x32_bf16 v[40:43], v[144:147], a[120:123], v[40:43]\n"
"v_exp_f32_e64 v149, -v149\n"
"v_mfma_f32_16x16x32_bf16 v[44:47], v[144:147], a[124:127], v[44:47]\n"
"v_mul_f32 v100, v68, v100\n"
"v_mul_f32 v101, v69, v101\n"
"v_mfma_f32_16x16x32_bf16 v[48:51], v[128:131], a[112:115], v[48:51]\n"
"ds_read_b64_tr_b16 v[140:141], v160 offset:1024\n"
"ds_read_b64_tr_b16 v[142:143], v160 offset:9216\n"
"v_mfma_f32_16x16x32_bf16 v[52:55], v[128:131], a[116:119], v[52:55]\n"
"buffer_load_dwordx4 v170, s[36:39], s66 offen offset:1024 lds\n"
"v_add_f32 v148, 1.0, v148\n"
"v_mfma_f32_16x16x32_bf16 v[56:59], v[128:131], a[120:123], v[56:59]\n"
"v_add_f32 v149, 1.0, v149\n"
"v_fma_f32 v150, v70, v70, s75\n"
"v_mfma_f32_16x16x32_bf16 v[60:63], v[128:131], a[124:127], v[60:63]\n"
"v_fma_f32 v151, v71, v71, s75\n"
"s_waitcnt lgkmcnt(2)\n"
"v_mfma_f32_16x16x32_bf16 v[0:3], v[132:135], a[128:131], v[0:3]\n"
"ds_read_b64_tr_b16 v[144:145], v161 offset:1024\n"
"ds_read_b64_tr_b16 v[146:147], v161 offset:9216\n"
"v_mfma_f32_16x16x32_bf16 v[4:7], v[132:135], a[132:135], v[4:7]\n"
"v_rcp_f32 v148, v148\n"
"v_mfma_f32_16x16x32_bf16 v[8:11], v[132:135], a[136:139], v[8:11]\n"
"v_rcp_f32 v149, v149\n"
"v_mfma_f32_16x16x32_bf16 v[12:15], v[132:135], a[140:143], v[12:15]\n"
"v_mul_f32 v100, v100, v148\n"
"v_mul_f32 v101, v101, v149\n"
"v_mfma_f32_16x16x32_bf16 v[16:19], v[136:139], a[128:131], v[16:19]\n"
"ds_read_b64_tr_b16 v[128:129], v160 offset:2048\n"
"ds_read_b64_tr_b16 v[130:131], v160 offset:10240\n"
"v_mfma_f32_16x16x32_bf16 v[20:23], v[136:139], a[132:135], v[20:23]\n"
"buffer_load_dwordx4 v171, s[28:31], s66 offen offset:2048 lds\n"
"v_cvt_pk_bf16_f32 v152, v100, v101\n"
"v_mfma_f32_16x16x32_bf16 v[24:27], v[136:139], a[136:139], v[24:27]\n"
"v_mul_f32 v150, v150, v70\n"
"v_mul_f32 v151, v151, v71\n"
"v_mfma_f32_16x16x32_bf16 v[28:31], v[136:139], a[140:143], v[28:31]\n"
"v_mul_f32 v102, v70, v102\n"
"s_waitcnt lgkmcnt(2)\n"
"v_mfma_f32_16x16x32_bf16 v[32:35], v[140:143], a[128:131], v[32:35]\n"
"ds_read_b64_tr_b16 v[132:133], v161 offset:2048\n"
"ds_read_b64_tr_b16 v[134:135], v161 offset:10240\n"
"v_mfma_f32_16x16x32_bf16 v[36:39], v[140:143], a[132:135], v[36:39]\n"
"v_exp_f32_e64 v150, -v150\n"
"v_mfma_f32_16x16x32_bf16 v[40:43], v[140:143], a[136:139], v[40:43]\n"
"v_exp_f32_e64 v151, -v151\n"
"v_mfma_f32_16x16x32_bf16 v[44:47], v[140:143], a[140:143], v[44:47]\n"
"v_mul_f32 v103, v71, v103\n"
"v_add_f32 v150, 1.0, v150\n"
"v_mfma_f32_16x16x32_bf16 v[48:51], v[144:147], a[128:131], v[48:51]\n"
"ds_read_b64_tr_b16 v[136:137], v160 offset:3072\n"
"ds_read_b64_tr_b16 v[138:139], v160 offset:11264\n"
"v_mfma_f32_16x16x32_bf16 v[52:55], v[144:147], a[132:135], v[52:55]\n"
"buffer_load_dwordx4 v172, s[36:39], s66 offen offset:3072 lds\n"
"v_add_f32 v151, 1.0, v151\n"
"v_mfma_f32_16x16x32_bf16 v[56:59], v[144:147], a[136:139], v[56:59]\n"
"v_rcp_f32 v150, v150\n"
"v_mfma_f32_16x16x32_bf16 v[60:63], v[144:147], a[140:143], v[60:63]\n"
"v_mul_f32 v102, v102, v150\n"
"s_waitcnt lgkmcnt(2)\n"
"v_mfma_f32_16x16x32_bf16 v[0:3], v[128:131], a[144:147], v[0:3]\n"
"ds_read_b64_tr_b16 v[140:141], v161 offset:3072\n"
"ds_read_b64_tr_b16 v[142:143], v161 offset:11264\n"
"v_mfma_f32_16x16x32_bf16 v[4:7], v[128:131], a[148:151], v[4:7]\n"
"v_rcp_f32 v151, v151\n"
"v_mfma_f32_16x16x32_bf16 v[8:11], v[128:131], a[152:155], v[8:11]\n"
"v_mul_f32 v103, v103, v151\n"
"v_cvt_pk_bf16_f32 v153, v102, v103\n"
"v_mfma_f32_16x16x32_bf16 v[12:15], v[128:131], a[156:159], v[12:15]\n"
"v_fma_f32 v148, v84, v84, s75\n"
"v_fma_f32 v149, v85, v85, s75\n"
"v_mfma_f32_16x16x32_bf16 v[16:19], v[132:135], a[144:147], v[16:19]\n"
"s_waitcnt vmcnt(13)\n"
"s_barrier\n"
"s_lshl_b32 s68, s63, 8\n"
"s_add_u32 s68, s68, 0\n"
"s_cmp_eq_u32 s60, 0\n"
"s_cbranch_scc0 Lsig0_%=\n"
"v_mov_b32 v180, 1\n"
"s_mov_b64 exec, 1\n"
"buffer_atomic_add v180, off, s[48:51], s68\n"
"s_mov_b64 exec, -1\n"
"Lsig0_%=:\n"
"v_mfma_f32_16x16x32_bf16 v[20:23], v[132:135], a[148:151], v[20:23]\n"
"ds_read_b64_tr_b16 v[144:145], v160 offset:16384\n"
"ds_read_b64_tr_b16 v[146:147], v160 offset:24576\n"
"v_mfma_f32_16x16x32_bf16 v[24:27], v[132:135], a[152:155], v[24:27]\n"
"s_add_u32 m0, s65, 81920\n"
"s_add_u32 s66, s70, 256\n"
"buffer_load_dwordx4 v169, s[28:31], s66 offen lds\n"
"v_mfma_f32_16x16x32_bf16 v[28:31], v[132:135], a[156:159], v[28:31]\n"
"v_mul_f32 v148, v148, v84\n"
"s_waitcnt lgkmcnt(2)\n"
"v_mfma_f32_16x16x32_bf16 v[32:35], v[136:139], a[144:147], v[32:35]\n"
"ds_read_b64_tr_b16 v[128:129], v161 offset:16384\n"
"ds_read_b64_tr_b16 v[130:131], v161 offset:24576\n"
"v_mfma_f32_16x16x32_bf16 v[36:39], v[136:139], a[148:151], v[36:39]\n"
"v_mul_f32 v149, v149, v85\n"
"v_mul_f32 v116, v84, v116\n"
"v_mfma_f32_16x16x32_bf16 v[40:43], v[136:139], a[152:155], v[40:43]\n"
"v_exp_f32_e64 v148, -v148\n"
"v_mfma_f32_16x16x32_bf16 v[44:47], v[136:139], a[156:159], v[44:47]\n"
"v_exp_f32_e64 v149, -v149\n"
"v_mfma_f32_16x16x32_bf16 v[48:51], v[140:143], a[144:147], v[48:51]\n"
"ds_read_b64_tr_b16 v[132:133], v160 offset:17408\n"
"ds_read_b64_tr_b16 v[134:135], v160 offset:25600\n"
"v_mfma_f32_16x16x32_bf16 v[52:55], v[140:143], a[148:151], v[52:55]\n"
"buffer_load_dwordx4 v170, s[36:39], s66 offen offset:1024 lds\n"
"v_mul_f32 v117, v85, v117\n"
"v_mfma_f32_16x16x32_bf16 v[56:59], v[140:143], a[152:155], v[56:59]\n"
"v_add_f32 v148, 1.0, v148\n"
"v_add_f32 v149, 1.0, v149\n"
"v_mfma_f32_16x16x32_bf16 v[60:63], v[140:143], a[156:159], v[60:63]\n"
"v_fma_f32 v150, v86, v86, s75\n"
"s_waitcnt lgkmcnt(2)\n"
"v_mfma_f32_16x16x32_bf16 v[0:3], v[144:147], a[160:163], v[0:3]\n"
"ds_read_b64_tr_b16 v[136:137], v161 offset:17408\n"
"ds_read_b64_tr_b16 v[138:139], v161 offset:25600\n"
"v_mfma_f32_16x16x32_bf16 v[4:7], v[144:147], a[164:167], v[4:7]\n"
"s_lshr_b32 s18, %[bid], 3\n"
"s_lshl_b32 s18, s18, 2\n"
"s_add_u32 s18, s18, s60\n"
"s_mul_i32 s18, s18, 41\n"
"s_add_u32 s18, s18, 11\n"
"v_lshrrev_b32 v182, 4, v158\n"
"v_cmp_gt_u32 vcc, 11, v182\n"
"v_add_u32 v184, s18, v182\n"
"v_lshlrev_b32 v184, 7, v184\n"
"v_mov_b32 v185, 2147483647\n"
"v_cndmask_b32 v184, v185, v184, vcc\n"
"s_lshr_b32 s18, s63, 1\n"
"s_mul_i32 s18, s18, 663552\n"
"s_add_u32 s18, s18, 3072\n"
"buffer_load_dword v186, v184, s[40:43], s18 offen\n"
"v_mfma_f32_16x16x32_bf16 v[8:11], v[144:147], a[168:171], v[8:11]\n"
"v_rcp_f32 v148, v148\n"
"v_mfma_f32_16x16x32_bf16 v[12:15], v[144:147], a[172:175], v[12:15]\n"
"v_rcp_f32 v149, v149\n"
"v_mfma_f32_16x16x32_bf16 v[16:19], v[128:131], a[160:163], v[16:19]\n"
"ds_read_b64_tr_b16 v[140:141], v160 offset:18432\n"
"ds_read_b64_tr_b16 v[142:143], v160 offset:26624\n"
"v_mfma_f32_16x16x32_bf16 v[20:23], v[128:131], a[164:167], v[20:23]\n"
"buffer_load_dwordx4 v171, s[28:31], s66 offen offset:2048 lds\n"
"v_mul_f32 v116, v116, v148\n"
"v_mfma_f32_16x16x32_bf16 v[24:27], v[128:131], a[168:171], v[24:27]\n"
"v_mul_f32 v117, v117, v149\n"
"v_cvt_pk_bf16_f32 v154, v116, v117\n"
"v_mfma_f32_16x16x32_bf16 v[28:31], v[128:131], a[172:175], v[28:31]\n"
"v_fma_f32 v151, v87, v87, s75\n"
"s_waitcnt lgkmcnt(2)\n"
"v_mfma_f32_16x16x32_bf16 v[32:35], v[132:135], a[160:163], v[32:35]\n"
"ds_read_b64_tr_b16 v[144:145], v161 offset:18432\n"
"ds_read_b64_tr_b16 v[146:147], v161 offset:26624\n"
"v_mfma_f32_16x16x32_bf16 v[36:39], v[132:135], a[164:167], v[36:39]\n"
"v_mul_f32 v150, v150, v86\n"
"v_mul_f32 v151, v151, v87\n"
"v_mfma_f32_16x16x32_bf16 v[40:43], v[132:135], a[168:171], v[40:43]\n"
"v_exp_f32_e64 v150, -v150\n"
"v_mfma_f32_16x16x32_bf16 v[44:47], v[132:135], a[172:175], v[44:47]\n"
"v_exp_f32_e64 v151, -v151\n"
"v_mfma_f32_16x16x32_bf16 v[48:51], v[136:139], a[160:163], v[48:51]\n"
"ds_read_b64_tr_b16 v[128:129], v160 offset:19456\n"
"ds_read_b64_tr_b16 v[130:131], v160 offset:27648\n"
"v_mfma_f32_16x16x32_bf16 v[52:55], v[136:139], a[164:167], v[52:55]\n"
"buffer_load_dwordx4 v172, s[36:39], s66 offen offset:3072 lds\n"
"v_mul_f32 v118, v86, v118\n"
"v_mfma_f32_16x16x32_bf16 v[56:59], v[136:139], a[168:171], v[56:59]\n"
"v_mul_f32 v119, v87, v119\n"
"v_add_f32 v150, 1.0, v150\n"
"v_mfma_f32_16x16x32_bf16 v[60:63], v[136:139], a[172:175], v[60:63]\n"
"v_add_f32 v151, 1.0, v151\n"
"s_waitcnt lgkmcnt(2)\n"
"v_mfma_f32_16x16x32_bf16 v[0:3], v[140:143], a[176:179], v[0:3]\n"
"ds_read_b64_tr_b16 v[132:133], v161 offset:19456\n"
"ds_read_b64_tr_b16 v[134:135], v161 offset:27648\n"
"v_mfma_f32_16x16x32_bf16 v[4:7], v[140:143], a[180:183], v[4:7]\n"
"v_rcp_f32 v150, v150\n"
"v_mfma_f32_16x16x32_bf16 v[8:11], v[140:143], a[184:187], v[8:11]\n"
"v_rcp_f32 v151, v151\n"
"v_mfma_f32_16x16x32_bf16 v[12:15], v[140:143], a[188:191], v[12:15]\n"
"v_mul_f32 v118, v118, v150\n"
"v_mul_f32 v119, v119, v151\n"
"v_mfma_f32_16x16x32_bf16 v[16:19], v[144:147], a[176:179], v[16:19]\n"
"s_waitcnt vmcnt(14)\n"
"s_barrier\n"
"v_mfma_f32_16x16x32_bf16 v[20:23], v[144:147], a[180:183], v[20:23]\n"
"ds_read_b64_tr_b16 v[136:137], v160 offset:32768\n"
"ds_read_b64_tr_b16 v[138:139], v160 offset:40960\n"
"v_mfma_f32_16x16x32_bf16 v[24:27], v[144:147], a[184:187], v[24:27]\n"
"s_add_u32 m0, s65, 0\n"
"s_add_u32 s66, s70, 590080\n"
"buffer_load_dwordx4 v169, s[28:31], s66 offen lds\n"
"v_mfma_f32_16x16x32_bf16 v[28:31], v[144:147], a[188:191], v[28:31]\n"
"v_cvt_pk_bf16_f32 v155, v118, v119\n"
"s_waitcnt lgkmcnt(2)\n"
"v_mfma_f32_16x16x32_bf16 v[32:35], v[128:131], a[176:179], v[32:35]\n"
"ds_read_b64_tr_b16 v[140:141], v161 offset:32768\n"
"ds_read_b64_tr_b16 v[142:143], v161 offset:40960\n"
"v_mfma_f32_16x16x32_bf16 v[36:39], v[128:131], a[180:183], v[36:39]\n"
"ds_write_b128 v156, v[152:155] offset:1024\n"
"buffer_store_dwordx4 v[152:155], v158, s[48:51], s17 offen offset:1024 sc1\n"
"v_mfma_f32_16x16x32_bf16 v[40:43], v[128:131], a[184:187], v[40:43]\n"
"v_fma_f32 v148, v72, v72, s75\n"
"v_fma_f32 v149, v73, v73, s75\n"
"v_mfma_f32_16x16x32_bf16 v[44:47], v[128:131], a[188:191], v[44:47]\n"
"v_mul_f32 v148, v148, v72\n"
"v_mul_f32 v149, v149, v73\n"
"v_mfma_f32_16x16x32_bf16 v[48:51], v[132:135], a[176:179], v[48:51]\n"
"ds_read_b64_tr_b16 v[144:145], v160 offset:33792\n"
"ds_read_b64_tr_b16 v[146:147], v160 offset:41984\n"
"v_mfma_f32_16x16x32_bf16 v[52:55], v[132:135], a[180:183], v[52:55]\n"
"buffer_load_dwordx4 v170, s[36:39], s66 offen offset:1024 lds\n"
"v_mul_f32 v104, v72, v104\n"
"v_mfma_f32_16x16x32_bf16 v[56:59], v[132:135], a[184:187], v[56:59]\n"
"v_exp_f32_e64 v148, -v148\n"
"v_mfma_f32_16x16x32_bf16 v[60:63], v[132:135], a[188:191], v[60:63]\n"
"v_mul_f32 v105, v73, v105\n"
"s_waitcnt lgkmcnt(3)\n"
"v_mfma_f32_16x16x32_bf16 v[0:3], v[136:139], a[192:195], v[0:3]\n"
"ds_read_b64_tr_b16 v[128:129], v161 offset:33792\n"
"ds_read_b64_tr_b16 v[130:131], v161 offset:41984\n"
"v_mfma_f32_16x16x32_bf16 v[4:7], v[136:139], a[196:199], v[4:7]\n"
"v_exp_f32_e64 v149, -v149\n"
"v_mfma_f32_16x16x32_bf16 v[8:11], v[136:139], a[200:203], v[8:11]\n"
"v_add_f32 v148, 1.0, v148\n"
"v_add_f32 v149, 1.0, v149\n"
"v_mfma_f32_16x16x32_bf16 v[12:15], v[136:139], a[204:207], v[12:15]\n"
"v_rcp_f32 v148, v148\n"
"v_mfma_f32_16x16x32_bf16 v[16:19], v[140:143], a[192:195], v[16:19]\n"
"ds_read_b64_tr_b16 v[132:133], v160 offset:34816\n"
"ds_read_b64_tr_b16 v[134:135], v160 offset:43008\n"
"v_mfma_f32_16x16x32_bf16 v[20:23], v[140:143], a[196:199], v[20:23]\n"
"buffer_load_dwordx4 v171, s[28:31], s66 offen offset:2048 lds\n"
"v_mul_f32 v104, v104, v148\n"
"v_mfma_f32_16x16x32_bf16 v[24:27], v[140:143], a[200:203], v[24:27]\n"
"v_rcp_f32 v149, v149\n"
"v_mfma_f32_16x16x32_bf16 v[28:31], v[140:143], a[204:207], v[28:31]\n"
"v_mul_f32 v105, v105, v149\n"
"s_waitcnt lgkmcnt(2)\n"
"v_mfma_f32_16x16x32_bf16 v[32:35], v[144:147], a[192:195], v[32:35]\n"
"ds_read_b64_tr_b16 v[136:137], v161 offset:34816\n"
"ds_read_b64_tr_b16 v[138:139], v161 offset:43008\n"
"v_mfma_f32_16x16x32_bf16 v[36:39], v[144:147], a[196:199], v[36:39]\n"
"v_cvt_pk_bf16_f32 v152, v104, v105\n"
"v_fma_f32 v150, v74, v74, s75\n"
"v_mfma_f32_16x16x32_bf16 v[40:43], v[144:147], a[200:203], v[40:43]\n"
"v_fma_f32 v151, v75, v75, s75\n"
"v_mul_f32 v150, v150, v74\n"
"v_mfma_f32_16x16x32_bf16 v[44:47], v[144:147], a[204:207], v[44:47]\n"
"v_mul_f32 v151, v151, v75\n"
"v_mul_f32 v106, v74, v106\n"
"v_mfma_f32_16x16x32_bf16 v[48:51], v[128:131], a[192:195], v[48:51]\n"
"ds_read_b64_tr_b16 v[140:141], v160 offset:35840\n"
"ds_read_b64_tr_b16 v[142:143], v160 offset:44032\n"
"v_mfma_f32_16x16x32_bf16 v[52:55], v[128:131], a[196:199], v[52:55]\n"
"buffer_load_dwordx4 v172, s[36:39], s66 offen offset:3072 lds\n"
"v_mul_f32 v107, v75, v107\n"
"v_mfma_f32_16x16x32_bf16 v[56:59], v[128:131], a[200:203], v[56:59]\n"
"v_exp_f32_e64 v150, -v150\n"
"v_mfma_f32_16x16x32_bf16 v[60:63], v[128:131], a[204:207], v[60:63]\n"
"v_add_f32 v150, 1.0, v150\n"
"s_waitcnt lgkmcnt(2)\n"
"v_mfma_f32_16x16x32_bf16 v[0:3], v[132:135], a[208:211], v[0:3]\n"
"ds_read_b64_tr_b16 v[144:145], v161 offset:35840\n"
"ds_read_b64_tr_b16 v[146:147], v161 offset:44032\n"
"v_mfma_f32_16x16x32_bf16 v[4:7], v[132:135], a[212:215], v[4:7]\n"
"v_exp_f32_e64 v151, -v151\n"
"v_mfma_f32_16x16x32_bf16 v[8:11], v[132:135], a[216:219], v[8:11]\n"
"v_add_f32 v151, 1.0, v151\n"
"v_fma_f32 v148, v88, v88, s75\n"
"v_mfma_f32_16x16x32_bf16 v[12:15], v[132:135], a[220:223], v[12:15]\n"
"v_rcp_f32 v150, v150\n"
"v_mfma_f32_16x16x32_bf16 v[16:19], v[136:139], a[208:211], v[16:19]\n"
"s_waitcnt vmcnt(15)\n"
"s_barrier\n"
"v_mfma_f32_16x16x32_bf16 v[20:23], v[136:139], a[212:215], v[20:23]\n"
"ds_read_b64_tr_b16 v[128:129], v160 offset:49152\n"
"ds_read_b64_tr_b16 v[130:131], v160 offset:57344\n"
"v_mfma_f32_16x16x32_bf16 v[24:27], v[136:139], a[216:219], v[24:27]\n"
"s_add_u32 m0, s65, 16384\n"
"s_add_u32 s66, s70, 1179904\n"
"buffer_load_dwordx4 v169, s[28:31], s66 offen lds\n"
"v_mfma_f32_16x16x32_bf16 v[28:31], v[136:139], a[220:223], v[28:31]\n"
"v_mul_f32 v106, v106, v150\n"
"s_waitcnt lgkmcnt(2)\n"
"v_mfma_f32_16x16x32_bf16 v[32:35], v[140:143], a[208:211], v[32:35]\n"
"ds_read_b64_tr_b16 v[132:133], v161 offset:49152\n"
"ds_read_b64_tr_b16 v[134:135], v161 offset:57344\n"
"v_mfma_f32_16x16x32_bf16 v[36:39], v[140:143], a[212:215], v[36:39]\n"
"v_rcp_f32 v151, v151\n"
"v_mfma_f32_16x16x32_bf16 v[40:43], v[140:143], a[216:219], v[40:43]\n"
"v_mul_f32 v107, v107, v151\n"
"v_cvt_pk_bf16_f32 v153, v106, v107\n"
"v_mfma_f32_16x16x32_bf16 v[44:47], v[140:143], a[220:223], v[44:47]\n"
"v_fma_f32 v149, v89, v89, s75\n"
"v_mul_f32 v148, v148, v88\n"
"v_mfma_f32_16x16x32_bf16 v[48:51], v[144:147], a[208:211], v[48:51]\n"
"ds_read_b64_tr_b16 v[136:137], v160 offset:50176\n"
"ds_read_b64_tr_b16 v[138:139], v160 offset:58368\n"
"v_mfma_f32_16x16x32_bf16 v[52:55], v[144:147], a[212:215], v[52:55]\n"
"buffer_load_dwordx4 v170, s[36:39], s66 offen offset:1024 lds\n"
"v_mul_f32 v149, v149, v89\n"
"v_mfma_f32_16x16x32_bf16 v[56:59], v[144:147], a[216:219], v[56:59]\n"
"v_exp_f32_e64 v148, -v148\n"
"v_mfma_f32_16x16x32_bf16 v[60:63], v[144:147], a[220:223], v[60:63]\n"
"v_mul_f32 v120, v88, v120\n"
"s_waitcnt lgkmcnt(2)\n"
"v_mfma_f32_16x16x32_bf16 v[0:3], v[128:131], a[224:227], v[0:3]\n"
"ds_read_b64_tr_b16 v[140:141], v161 offset:50176\n"
"ds_read_b64_tr_b16 v[142:143], v161 offset:58368\n"
"v_mfma_f32_16x16x32_bf16 v[4:7], v[128:131], a[228:231], v[4:7]\n"
"v_exp_f32_e64 v149, -v149\n"
"v_mfma_f32_16x16x32_bf16 v[8:11], v[128:131], a[232:235], v[8:11]\n"
"v_mul_f32 v121, v89, v121\n"
"v_add_f32 v148, 1.0, v148\n"
"v_mfma_f32_16x16x32_bf16 v[12:15], v[128:131], a[236:239], v[12:15]\n"
"v_add_f32 v149, 1.0, v149\n"
"v_fma_f32 v150, v90, v90, s75\n"
"v_mfma_f32_16x16x32_bf16 v[16:19], v[132:135], a[224:227], v[16:19]\n"
"ds_read_b64_tr_b16 v[144:145], v160 offset:51200\n"
"ds_read_b64_tr_b16 v[146:147], v160 offset:59392\n"
"v_mfma_f32_16x16x32_bf16 v[20:23], v[132:135], a[228:231], v[20:23]\n"
"buffer_load_dwordx4 v171, s[28:31], s66 offen offset:2048 lds\n"
"v_fma_f32 v151, v91, v91, s75\n"
"v_mfma_f32_16x16x32_bf16 v[24:27], v[132:135], a[232:235], v[24:27]\n"
"v_rcp_f32 v148, v148\n"
"v_mfma_f32_16x16x32_bf16 v[28:31], v[132:135], a[236:239], v[28:31]\n"
"v_mul_f32 v120, v120, v148\n"
"s_waitcnt lgkmcnt(2)\n"
"v_mfma_f32_16x16x32_bf16 v[32:35], v[136:139], a[224:227], v[32:35]\n"
"ds_read_b64_tr_b16 v[128:129], v161 offset:51200\n"
"ds_read_b64_tr_b16 v[130:131], v161 offset:59392\n"
"v_mfma_f32_16x16x32_bf16 v[36:39], v[136:139], a[228:231], v[36:39]\n"
"v_rcp_f32 v149, v149\n"
"v_mfma_f32_16x16x32_bf16 v[40:43], v[136:139], a[232:235], v[40:43]\n"
"v_mul_f32 v121, v121, v149\n"
"v_cvt_pk_bf16_f32 v154, v120, v121\n"
"v_mfma_f32_16x16x32_bf16 v[44:47], v[136:139], a[236:239], v[44:47]\n"
"v_mul_f32 v150, v150, v90\n"
"v_mul_f32 v151, v151, v91\n"
"v_mfma_f32_16x16x32_bf16 v[48:51], v[140:143], a[224:227], v[48:51]\n"
"ds_read_b64_tr_b16 v[132:133], v160 offset:52224\n"
"ds_read_b64_tr_b16 v[134:135], v160 offset:60416\n"
"v_mfma_f32_16x16x32_bf16 v[52:55], v[140:143], a[228:231], v[52:55]\n"
"buffer_load_dwordx4 v172, s[36:39], s66 offen offset:3072 lds\n"
"v_mul_f32 v122, v90, v122\n"
"v_mfma_f32_16x16x32_bf16 v[56:59], v[140:143], a[232:235], v[56:59]\n"
"v_exp_f32_e64 v150, -v150\n"
"v_mfma_f32_16x16x32_bf16 v[60:63], v[140:143], a[236:239], v[60:63]\n"
"v_mul_f32 v123, v91, v123\n"
"s_waitcnt lgkmcnt(2)\n"
"v_mfma_f32_16x16x32_bf16 v[0:3], v[144:147], a[240:243], v[0:3]\n"
"ds_read_b64_tr_b16 v[136:137], v161 offset:52224\n"
"ds_read_b64_tr_b16 v[138:139], v161 offset:60416\n"
"v_mfma_f32_16x16x32_bf16 v[4:7], v[144:147], a[244:247], v[4:7]\n"
"v_exp_f32_e64 v151, -v151\n"
"v_mfma_f32_16x16x32_bf16 v[8:11], v[144:147], a[248:251], v[8:11]\n"
"v_add_f32 v150, 1.0, v150\n"
"v_add_f32 v151, 1.0, v151\n"
"v_mfma_f32_16x16x32_bf16 v[12:15], v[144:147], a[252:255], v[12:15]\n"
"v_rcp_f32 v150, v150\n"
"v_mfma_f32_16x16x32_bf16 v[16:19], v[128:131], a[240:243], v[16:19]\n"
"s_waitcnt vmcnt(14)\n"
"s_barrier\n"
"v_mfma_f32_16x16x32_bf16 v[20:23], v[128:131], a[244:247], v[20:23]\n"
"ds_read_b64_tr_b16 v[140:141], v162\n"
"ds_read_b64_tr_b16 v[142:143], v162 offset:8192\n"
"v_mfma_f32_16x16x32_bf16 v[24:27], v[128:131], a[248:251], v[24:27]\n"
"s_add_u32 m0, s65, 32768\n"
"s_add_u32 s66, s70, 1769728\n"
"buffer_load_dwordx4 v169, s[28:31], s66 offen lds\n"
"v_mfma_f32_16x16x32_bf16 v[28:31], v[128:131], a[252:255], v[28:31]\n"
"v_mul_f32 v122, v122, v150\n"
"s_waitcnt lgkmcnt(2)\n"
"v_mfma_f32_16x16x32_bf16 v[32:35], v[132:135], a[240:243], v[32:35]\n"
"ds_read_b64_tr_b16 v[144:145], v163\n"
"ds_read_b64_tr_b16 v[146:147], v163 offset:8192\n"
"v_mfma_f32_16x16x32_bf16 v[36:39], v[132:135], a[244:247], v[36:39]\n"
"v_rcp_f32 v151, v151\n"
"v_mfma_f32_16x16x32_bf16 v[40:43], v[132:135], a[248:251], v[40:43]\n"
"v_mul_f32 v123, v123, v151\n"
"v_cvt_pk_bf16_f32 v155, v122, v123\n"
"v_mfma_f32_16x16x32_bf16 v[44:47], v[132:135], a[252:255], v[44:47]\n"
"ds_write_b128 v156, v[152:155] offset:2048\n"
"buffer_store_dwordx4 v[152:155], v158, s[48:51], s17 offen offset:2048 sc1\n"
"v_mfma_f32_16x16x32_bf16 v[48:51], v[136:139], a[240:243], v[48:51]\n"
"ds_read_b64_tr_b16 v[128:129], v162 offset:1024\n"
"ds_read_b64_tr_b16 v[130:131], v162 offset:9216\n"
"v_mfma_f32_16x16x32_bf16 v[52:55], v[136:139], a[244:247], v[52:55]\n"
"buffer_load_dwordx4 v170, s[36:39], s66 offen offset:1024 lds\n"
"v_fma_f32 v148, v76, v76, s75\n"
"v_mfma_f32_16x16x32_bf16 v[56:59], v[136:139], a[248:251], v[56:59]\n"
"v_fma_f32 v149, v77, v77, s75\n"
"v_mul_f32 v148, v148, v76\n"
"v_mfma_f32_16x16x32_bf16 v[60:63], v[136:139], a[252:255], v[60:63]\n"
"v_mul_f32 v149, v149, v77\n"
"s_waitcnt lgkmcnt(3)\n"
"v_mfma_f32_16x16x32_bf16 v[0:3], v[140:143], v[192:195], v[0:3]\n"
"ds_read_b64_tr_b16 v[132:133], v163 offset:1024\n"
"ds_read_b64_tr_b16 v[134:135], v163 offset:9216\n"
"v_mfma_f32_16x16x32_bf16 v[4:7], v[140:143], v[196:199], v[4:7]\n"
"v_exp_f32_e64 v148, -v148\n"
"v_mfma_f32_16x16x32_bf16 v[8:11], v[140:143], v[200:203], v[8:11]\n"
"v_exp_f32_e64 v149, -v149\n"
"v_mfma_f32_16x16x32_bf16 v[12:15], v[140:143], v[204:207], v[12:15]\n"
"v_mul_f32 v108, v76, v108\n"
"v_mul_f32 v109, v77, v109\n"
"v_mfma_f32_16x16x32_bf16 v[16:19], v[144:147], v[192:195], v[16:19]\n"
"ds_read_b64_tr_b16 v[136:137], v162 offset:2048\n"
"ds_read_b64_tr_b16 v[138:139], v162 offset:10240\n"
"v_mfma_f32_16x16x32_bf16 v[20:23], v[144:147], v[196:199], v[20:23]\n"
"buffer_load_dwordx4 v171, s[28:31], s66 offen offset:2048 lds\n"
"v_add_f32 v148, 1.0, v148\n"
"v_mfma_f32_16x16x32_bf16 v[24:27], v[144:147], v[200:203], v[24:27]\n"
"v_add_f32 v149, 1.0, v149\n"
"v_fma_f32 v150, v78, v78, s75\n"
"v_mfma_f32_16x16x32_bf16 v[28:31], v[144:147], v[204:207], v[28:31]\n"
"v_fma_f32 v151, v79, v79, s75\n"
"s_waitcnt lgkmcnt(2)\n"
"v_mfma_f32_16x16x32_bf16 v[32:35], v[128:131], v[192:195], v[32:35]\n"
"ds_read_b64_tr_b16 v[140:141], v163 offset:2048\n"
"ds_read_b64_tr_b16 v[142:143], v163 offset:10240\n"
"v_mfma_f32_16x16x32_bf16 v[36:39], v[128:131], v[196:199], v[36:39]\n"
"v_rcp_f32 v148, v148\n"
"v_mfma_f32_16x16x32_bf16 v[40:43], v[128:131], v[200:203], v[40:43]\n"
"v_rcp_f32 v149, v149\n"
"v_mfma_f32_16x16x32_bf16 v[44:47], v[128:131], v[204:207], v[44:47]\n"
"v_mul_f32 v108, v108, v148\n"
"v_mul_f32 v109, v109, v149\n"
"v_mfma_f32_16x16x32_bf16 v[48:51], v[132:135], v[192:195], v[48:51]\n"
"ds_read_b64_tr_b16 v[144:145], v162 offset:3072\n"
"ds_read_b64_tr_b16 v[146:147], v162 offset:11264\n"
"v_mfma_f32_16x16x32_bf16 v[52:55], v[132:135], v[196:199], v[52:55]\n"
"buffer_load_dwordx4 v172, s[36:39], s66 offen offset:3072 lds\n"
"v_cvt_pk_bf16_f32 v152, v108, v109\n"
"v_mfma_f32_16x16x32_bf16 v[56:59], v[132:135], v[200:203], v[56:59]\n"
"v_mul_f32 v150, v150, v78\n"
"v_mul_f32 v151, v151, v79\n"
"v_mfma_f32_16x16x32_bf16 v[60:63], v[132:135], v[204:207], v[60:63]\n"
"v_mul_f32 v110, v78, v110\n"
"s_waitcnt lgkmcnt(2)\n"
"v_mfma_f32_16x16x32_bf16 v[0:3], v[136:139], v[208:211], v[0:3]\n"
"ds_read_b64_tr_b16 v[128:129], v163 offset:3072\n"
"ds_read_b64_tr_b16 v[130:131], v163 offset:11264\n"
"v_mfma_f32_16x16x32_bf16 v[4:7], v[136:139], v[212:215], v[4:7]\n"
"v_exp_f32_e64 v150, -v150\n"
"v_mfma_f32_16x16x32_bf16 v[8:11], v[136:139], v[216:219], v[8:11]\n"
"v_exp_f32_e64 v151, -v151\n"
"v_mfma_f32_16x16x32_bf16 v[12:15], v[136:139], v[220:223], v[12:15]\n"
"v_mul_f32 v111, v79, v111\n"
"v_add_f32 v150, 1.0, v150\n"
"v_mfma_f32_16x16x32_bf16 v[16:19], v[140:143], v[208:211], v[16:19]\n"
"s_waitcnt vmcnt(14)\n"
"s_barrier\n"
"s_lshl_b32 s19, s63, 8\n"
"s_add_u32 s19, s19, 0\n"
"buffer_load_dword v178, off, s[48:51], s19 sc1\n"
"v_mfma_f32_16x16x32_bf16 v[20:23], v[140:143], v[212:215], v[20:23]\n"
"ds_read_b64_tr_b16 v[132:133], v162 offset:16384\n"
"ds_read_b64_tr_b16 v[134:135], v162 offset:24576\n"
"v_mfma_f32_16x16x32_bf16 v[24:27], v[140:143], v[216:219], v[24:27]\n"
"s_add_u32 m0, s65, 49152\n"
"s_add_u32 s66, s70, 2359552\n"
"buffer_load_dwordx4 v169, s[28:31], s66 offen lds\n"
"v_mfma_f32_16x16x32_bf16 v[28:31], v[140:143], v[220:223], v[28:31]\n"
"v_add_f32 v151, 1.0, v151\n"
"s_waitcnt lgkmcnt(2)\n"
"v_mfma_f32_16x16x32_bf16 v[32:35], v[144:147], v[208:211], v[32:35]\n"
"ds_read_b64_tr_b16 v[136:137], v163 offset:16384\n"
"ds_read_b64_tr_b16 v[138:139], v163 offset:24576\n"
"v_mfma_f32_16x16x32_bf16 v[36:39], v[144:147], v[212:215], v[36:39]\n"
"v_rcp_f32 v150, v150\n"
"v_mfma_f32_16x16x32_bf16 v[40:43], v[144:147], v[216:219], v[40:43]\n"
"v_rcp_f32 v151, v151\n"
"v_mfma_f32_16x16x32_bf16 v[44:47], v[144:147], v[220:223], v[44:47]\n"
"v_mul_f32 v110, v110, v150\n"
"v_mul_f32 v111, v111, v151\n"
"v_mfma_f32_16x16x32_bf16 v[48:51], v[128:131], v[208:211], v[48:51]\n"
"ds_read_b64_tr_b16 v[140:141], v162 offset:17408\n"
"ds_read_b64_tr_b16 v[142:143], v162 offset:25600\n"
"v_mfma_f32_16x16x32_bf16 v[52:55], v[128:131], v[212:215], v[52:55]\n"
"buffer_load_dwordx4 v170, s[36:39], s66 offen offset:1024 lds\n"
"v_cvt_pk_bf16_f32 v153, v110, v111\n"
"v_mfma_f32_16x16x32_bf16 v[56:59], v[128:131], v[216:219], v[56:59]\n"
"v_fma_f32 v148, v92, v92, s75\n"
"v_fma_f32 v149, v93, v93, s75\n"
"v_mfma_f32_16x16x32_bf16 v[60:63], v[128:131], v[220:223], v[60:63]\n"
"v_mul_f32 v148, v148, v92\n"
"v_mul_f32 v149, v149, v93\n"
"v_exp_f32_e64 v148, -v148\n"
"v_exp_f32_e64 v149, -v149\n"
"v_mul_f32 v124, v92, v124\n"
"v_mul_f32 v125, v93, v125\n"
"v_add_f32 v148, 1.0, v148\n"
"v_add_f32 v149, 1.0, v149\n"
"v_rcp_f32 v148, v148\n"
"v_rcp_f32 v149, v149\n"
"v_mul_f32 v124, v124, v148\n"
"v_mul_f32 v125, v125, v149\n"
"v_cvt_pk_bf16_f32 v154, v124, v125\n"
"v_fma_f32 v150, v94, v94, s75\n"
"v_fma_f32 v151, v95, v95, s75\n"
"v_mul_f32 v150, v150, v94\n"
"v_mul_f32 v151, v151, v95\n"
"v_exp_f32_e64 v150, -v150\n"
"v_exp_f32_e64 v151, -v151\n"
"v_mul_f32 v126, v94, v126\n"
"v_mul_f32 v127, v95, v127\n"
"v_add_f32 v150, 1.0, v150\n"
"v_add_f32 v151, 1.0, v151\n"
"v_rcp_f32 v150, v150\n"
"v_rcp_f32 v151, v151\n"
"v_mul_f32 v126, v126, v150\n"
"v_mul_f32 v127, v127, v151\n"
"v_cvt_pk_bf16_f32 v155, v126, v127\n"
"ds_write_b128 v156, v[152:155] offset:3072\n"
"buffer_store_dwordx4 v[152:155], v158, s[48:51], s17 offen offset:3072 sc1\n"
"s_waitcnt lgkmcnt(3)\n"
"v_mfma_f32_16x16x32_bf16 v[64:67], v[132:135], a[0:3], 0\n"
"ds_read_b64_tr_b16 v[144:145], v163 offset:17408\n"
"ds_read_b64_tr_b16 v[146:147], v163 offset:25600\n"
"v_mfma_f32_16x16x32_bf16 v[68:71], v[132:135], a[4:7], 0\n"
"v_mfma_f32_16x16x32_bf16 v[72:75], v[132:135], a[8:11], 0\n"
"v_mfma_f32_16x16x32_bf16 v[76:79], v[132:135], a[12:15], 0\n"
"v_mfma_f32_16x16x32_bf16 v[80:83], v[136:139], a[0:3], 0\n"
"ds_read_b64_tr_b16 v[128:129], v162 offset:18432\n"
"ds_read_b64_tr_b16 v[130:131], v162 offset:26624\n"
"v_mfma_f32_16x16x32_bf16 v[84:87], v[136:139], a[4:7], 0\n"
"buffer_load_dwordx4 v171, s[28:31], s66 offen offset:2048 lds\n"
"v_add_u32 v156, 114688, v177\n"
"v_mfma_f32_16x16x32_bf16 v[88:91], v[136:139], a[8:11], 0\n"
"s_lshl_b32 s17, s62, 13\n"
"s_add_u32 s17, s17, s27\n"
"s_add_u32 s17, s17, 16384\n"
"v_mfma_f32_16x16x32_bf16 v[92:95], v[136:139], a[12:15], 0\n"
"v_fma_f32 v148, v0, v0, s75\n"
"s_waitcnt lgkmcnt(2)\n"
"v_mfma_f32_16x16x32_bf16 v[96:99], v[140:143], a[0:3], 0\n"
"ds_read_b64_tr_b16 v[132:133], v163 offset:18432\n"
"ds_read_b64_tr_b16 v[134:135], v163 offset:26624\n"
"v_mfma_f32_16x16x32_bf16 v[100:103], v[140:143], a[4:7], 0\n"
"v_fma_f32 v149, v1, v1, s75\n"
"v_mul_f32 v148, v148, v0\n"
"v_mfma_f32_16x16x32_bf16 v[104:107], v[140:143], a[8:11], 0\n"
"v_mul_f32 v149, v149, v1\n"
"v_mul_f32 v32, v0, v32\n"
"v_mfma_f32_16x16x32_bf16 v[108:111], v[140:143], a[12:15], 0\n"
"v_exp_f32_e64 v148, -v148\n"
"v_mfma_f32_16x16x32_bf16 v[112:115], v[144:147], a[0:3], 0\n"
"ds_read_b64_tr_b16 v[136:137], v162 offset:19456\n"
"ds_read_b64_tr_b16 v[138:139], v162 offset:27648\n"
"v_mfma_f32_16x16x32_bf16 v[116:119], v[144:147], a[4:7], 0\n"
"buffer_load_dwordx4 v172, s[36:39], s66 offen offset:3072 lds\n"
"v_mul_f32 v33, v1, v33\n"
"v_mfma_f32_16x16x32_bf16 v[120:123], v[144:147], a[8:11], 0\n"
"v_exp_f32_e64 v149, -v149\n"
"v_mfma_f32_16x16x32_bf16 v[124:127], v[144:147], a[12:15], 0\n"
"v_add_f32 v148, 1.0, v148\n"
"s_waitcnt lgkmcnt(2)\n"
"v_mfma_f32_16x16x32_bf16 v[64:67], v[128:131], a[16:19], v[64:67]\n"
"ds_read_b64_tr_b16 v[140:141], v163 offset:19456\n"
"ds_read_b64_tr_b16 v[142:143], v163 offset:27648\n"
"v_mfma_f32_16x16x32_bf16 v[68:71], v[128:131], a[20:23], v[68:71]\n"
"v_add_f32 v149, 1.0, v149\n"
"v_fma_f32 v150, v2, v2, s75\n"
"v_mfma_f32_16x16x32_bf16 v[72:75], v[128:131], a[24:27], v[72:75]\n"
"v_rcp_f32 v148, v148\n"
"v_mfma_f32_16x16x32_bf16 v[76:79], v[128:131], a[28:31], v[76:79]\n"
"v_rcp_f32 v149, v149\n"
"v_mfma_f32_16x16x32_bf16 v[80:83], v[132:135], a[16:19], v[80:83]\n"
"s_waitcnt vmcnt(15)\n"
"s_barrier\n"
"v_mfma_f32_16x16x32_bf16 v[84:87], v[132:135], a[20:23], v[84:87]\n"
"ds_read_b64_tr_b16 v[144:145], v160\n"
"ds_read_b64_tr_b16 v[146:147], v160 offset:8192\n"
"v_mfma_f32_16x16x32_bf16 v[88:91], v[132:135], a[24:27], v[88:91]\n"
"s_add_u32 m0, s65, 65536\n"
"s_add_u32 s66, s70, 2949376\n"
"buffer_load_dwordx4 v169, s[28:31], s66 offen lds\n"
"v_mfma_f32_16x16x32_bf16 v[92:95], v[132:135], a[28:31], v[92:95]\n"
"v_mul_f32 v32, v32, v148\n"
"s_waitcnt lgkmcnt(2)\n"
"v_mfma_f32_16x16x32_bf16 v[96:99], v[136:139], a[16:19], v[96:99]\n"
"ds_read_b64_tr_b16 v[128:129], v161\n"
"ds_read_b64_tr_b16 v[130:131], v161 offset:8192\n"
"v_mfma_f32_16x16x32_bf16 v[100:103], v[136:139], a[20:23], v[100:103]\n"
"v_mul_f32 v33, v33, v149\n"
"v_cvt_pk_bf16_f32 v152, v32, v33\n"
"v_mfma_f32_16x16x32_bf16 v[104:107], v[136:139], a[24:27], v[104:107]\n"
"v_fma_f32 v151, v3, v3, s75\n"
"v_mul_f32 v150, v150, v2\n"
"v_mfma_f32_16x16x32_bf16 v[108:111], v[136:139], a[28:31], v[108:111]\n"
"v_mul_f32 v151, v151, v3\n"
"v_mul_f32 v34, v2, v34\n"
"v_mfma_f32_16x16x32_bf16 v[112:115], v[140:143], a[16:19], v[112:115]\n"
"ds_read_b64_tr_b16 v[132:133], v160 offset:1024\n"
"ds_read_b64_tr_b16 v[134:135], v160 offset:9216\n"
"v_mfma_f32_16x16x32_bf16 v[116:119], v[140:143], a[20:23], v[116:119]\n"
"buffer_load_dwordx4 v170, s[36:39], s66 offen offset:1024 lds\n"
"v_mul_f32 v35, v3, v35\n"
"v_mfma_f32_16x16x32_bf16 v[120:123], v[140:143], a[24:27], v[120:123]\n"
"v_exp_f32_e64 v150, -v150\n"
"v_mfma_f32_16x16x32_bf16 v[124:127], v[140:143], a[28:31], v[124:127]\n"
"v_add_f32 v150, 1.0, v150\n"
"s_waitcnt lgkmcnt(2)\n"
"v_mfma_f32_16x16x32_bf16 v[64:67], v[144:147], a[32:35], v[64:67]\n"
"ds_read_b64_tr_b16 v[136:137], v161 offset:1024\n"
"ds_read_b64_tr_b16 v[138:139], v161 offset:9216\n"
"v_mfma_f32_16x16x32_bf16 v[68:71], v[144:147], a[36:39], v[68:71]\n"
"v_exp_f32_e64 v151, -v151\n"
"v_mfma_f32_16x16x32_bf16 v[72:75], v[144:147], a[40:43], v[72:75]\n"
"v_add_f32 v151, 1.0, v151\n"
"v_fma_f32 v148, v16, v16, s75\n"
"v_mfma_f32_16x16x32_bf16 v[76:79], v[144:147], a[44:47], v[76:79]\n"
"v_rcp_f32 v150, v150\n"
"v_mfma_f32_16x16x32_bf16 v[80:83], v[128:131], a[32:35], v[80:83]\n"
"ds_read_b64_tr_b16 v[140:141], v160 offset:2048\n"
"ds_read_b64_tr_b16 v[142:143], v160 offset:10240\n"
"v_mfma_f32_16x16x32_bf16 v[84:87], v[128:131], a[36:39], v[84:87]\n"
"buffer_load_dwordx4 v171, s[28:31], s66 offen offset:2048 lds\n"
"v_mul_f32 v34, v34, v150\n"
"v_mfma_f32_16x16x32_bf16 v[88:91], v[128:131], a[40:43], v[88:91]\n"
"v_rcp_f32 v151, v151\n"
"v_mfma_f32_16x16x32_bf16 v[92:95], v[128:131], a[44:47], v[92:95]\n"
"v_mul_f32 v35, v35, v151\n"
"s_waitcnt lgkmcnt(2)\n"
"v_mfma_f32_16x16x32_bf16 v[96:99], v[132:135], a[32:35], v[96:99]\n"
"ds_read_b64_tr_b16 v[144:145], v161 offset:2048\n"
"ds_read_b64_tr_b16 v[146:147], v161 offset:10240\n"
"v_mfma_f32_16x16x32_bf16 v[100:103], v[132:135], a[36:39], v[100:103]\n"
"v_cvt_pk_bf16_f32 v153, v34, v35\n"
"v_fma_f32 v149, v17, v17, s75\n"
"v_mfma_f32_16x16x32_bf16 v[104:107], v[132:135], a[40:43], v[104:107]\n"
"v_mul_f32 v148, v148, v16\n"
"v_mul_f32 v149, v149, v17\n"
"v_mfma_f32_16x16x32_bf16 v[108:111], v[132:135], a[44:47], v[108:111]\n"
"v_exp_f32_e64 v148, -v148\n"
"v_mfma_f32_16x16x32_bf16 v[112:115], v[136:139], a[32:35], v[112:115]\n"
"ds_read_b64_tr_b16 v[128:129], v160 offset:3072\n"
"ds_read_b64_tr_b16 v[130:131], v160 offset:11264\n"
"v_mfma_f32_16x16x32_bf16 v[116:119], v[136:139], a[36:39], v[116:119]\n"
"buffer_load_dwordx4 v172, s[36:39], s66 offen offset:3072 lds\n"
"v_mul_f32 v48, v16, v48\n"
"v_mfma_f32_16x16x32_bf16 v[120:123], v[136:139], a[40:43], v[120:123]\n"
"v_exp_f32_e64 v149, -v149\n"
"v_mfma_f32_16x16x32_bf16 v[124:127], v[136:139], a[44:47], v[124:127]\n"
"v_mul_f32 v49, v17, v49\n"
"s_waitcnt lgkmcnt(2)\n"
"v_mfma_f32_16x16x32_bf16 v[64:67], v[140:143], a[48:51], v[64:67]\n"
"ds_read_b64_tr_b16 v[132:133], v161 offset:3072\n"
"ds_read_b64_tr_b16 v[134:135], v161 offset:11264\n"
"v_mfma_f32_16x16x32_bf16 v[68:71], v[140:143], a[52:55], v[68:71]\n"
"v_add_f32 v148, 1.0, v148\n"
"v_add_f32 v149, 1.0, v149\n"
"v_mfma_f32_16x16x32_bf16 v[72:75], v[140:143], a[56:59], v[72:75]\n"
"v_rcp_f32 v148, v148\n"
"v_mfma_f32_16x16x32_bf16 v[76:79], v[140:143], a[60:63], v[76:79]\n"
"v_rcp_f32 v149, v149\n"
"v_mfma_f32_16x16x32_bf16 v[80:83], v[144:147], a[48:51], v[80:83]\n"
"s_waitcnt vmcnt(15)\n"
"s_barrier\n"
"v_mfma_f32_16x16x32_bf16 v[84:87], v[144:147], a[52:55], v[84:87]\n"
"ds_read_b64_tr_b16 v[136:137], v160 offset:16384\n"
"ds_read_b64_tr_b16 v[138:139], v160 offset:24576\n"
"v_mfma_f32_16x16x32_bf16 v[88:91], v[144:147], a[56:59], v[88:91]\n"
"s_add_u32 m0, s65, 81920\n"
"s_add_u32 s66, s70, 3539200\n"
"buffer_load_dwordx4 v169, s[28:31], s66 offen lds\n"
"v_mfma_f32_16x16x32_bf16 v[92:95], v[144:147], a[60:63], v[92:95]\n"
"v_mul_f32 v48, v48, v148\n"
"s_waitcnt lgkmcnt(2)\n"
"v_mfma_f32_16x16x32_bf16 v[96:99], v[128:131], a[48:51], v[96:99]\n"
"ds_read_b64_tr_b16 v[140:141], v161 offset:16384\n"
"ds_read_b64_tr_b16 v[142:143], v161 offset:24576\n"
"v_mfma_f32_16x16x32_bf16 v[100:103], v[128:131], a[52:55], v[100:103]\n"
"v_mul_f32 v49, v49, v149\n"
"v_cvt_pk_bf16_f32 v154, v48, v49\n"
"v_mfma_f32_16x16x32_bf16 v[104:107], v[128:131], a[56:59], v[104:107]\n"
"v_fma_f32 v150, v18, v18, s75\n"
"v_fma_f32 v151, v19, v19, s75\n"
"v_mfma_f32_16x16x32_bf16 v[108:111], v[128:131], a[60:63], v[108:111]\n"
"v_mul_f32 v150, v150, v18\n"
"v_mul_f32 v151, v151, v19\n"
"v_mfma_f32_16x16x32_bf16 v[112:115], v[132:135], a[48:51], v[112:115]\n"
"ds_read_b64_tr_b16 v[144:145], v160 offset:17408\n"
"ds_read_b64_tr_b16 v[146:147], v160 offset:25600\n"
"v_mfma_f32_16x16x32_bf16 v[116:119], v[132:135], a[52:55], v[116:119]\n"
"buffer_load_dwordx4 v170, s[36:39], s66 offen offset:1024 lds\n"
"v_mul_f32 v50, v18, v50\n"
"v_mfma_f32_16x16x32_bf16 v[120:123], v[132:135], a[56:59], v[120:123]\n"
"v_exp_f32_e64 v150, -v150\n"
"v_mfma_f32_16x16x32_bf16 v[124:127], v[132:135], a[60:63], v[124:127]\n"
"v_mul_f32 v51, v19, v51\n"
"s_waitcnt lgkmcnt(2)\n"
"v_mfma_f32_16x16x32_bf16 v[64:67], v[136:139], a[64:67], v[64:67]\n"
"ds_read_b64_tr_b16 v[128:129], v161 offset:17408\n"
"ds_read_b64_tr_b16 v[130:131], v161 offset:25600\n"
"v_mfma_f32_16x16x32_bf16 v[68:71], v[136:139], a[68:71], v[68:71]\n"
"s_waitcnt vmcnt(11)\n"
"v_readfirstlane_b32 s18, v178\n"
"s_nop 1\n"
"s_cmp_ge_u32 s18, 16\n"
"s_cbranch_scc0 Lslow1_%=\n"
"Lback1_%=:\n"
"v_mfma_f32_16x16x32_bf16 v[72:75], v[136:139], a[72:75], v[72:75]\n"
"v_lshrrev_b32 v182, 4, v158\n"
"v_and_b32 v183, 15, v182\n"
"v_lshrrev_b32 v184, 4, v182\n"
"s_cmp_eq_u32 s99, 0\n"
"s_cselect_b64 s[10:11], -1, 0\n"
"v_cndmask_b32_e64 v182, 8, v183, s[10:11]\n"
"s_lshl_b32 s18, s60, 4\n"
"v_add_u32 v182, s18, v182\n"
"v_lshlrev_b32 v182, 4, v182\n"
"v_lshl_add_u32 v185, v184, 15, v182\n"
"v_add_u32 v183, s18, v183\n"
"v_lshlrev_b32 v186, s98, v183\n"
"v_mfma_f32_16x16x32_bf16 v[76:79], v[136:139], a[76:79], v[76:79]\n"
"s_add_u32 s18, s96, 0\n"
"buffer_load_dwordx4 v[224:227], v185, s[48:51], s18 offen sc1\n"
"v_mfma_f32_16x16x32_bf16 v[80:83], v[140:143], a[64:67], v[80:83]\n"
"ds_read_b64_tr_b16 v[132:133], v160 offset:18432\n"
"ds_read_b64_tr_b16 v[134:135], v160 offset:26624\n"
"v_mfma_f32_16x16x32_bf16 v[84:87], v[140:143], a[68:71], v[84:87]\n"
"buffer_load_dwordx4 v171, s[28:31], s66 offen offset:2048 lds\n"
"v_add_f32 v150, 1.0, v150\n"
"v_mfma_f32_16x16x32_bf16 v[88:91], v[140:143], a[72:75], v[88:91]\n"
"s_add_u32 s18, s96, 8192\n"
"buffer_load_dwordx4 v[228:231], v185, s[48:51], s18 offen sc1\n"
"v_mfma_f32_16x16x32_bf16 v[92:95], v[140:143], a[76:79], v[92:95]\n"
"s_waitcnt lgkmcnt(2)\n"
"v_mfma_f32_16x16x32_bf16 v[96:99], v[144:147], a[64:67], v[96:99]\n"
"ds_read_b64_tr_b16 v[136:137], v161 offset:18432\n"
"ds_read_b64_tr_b16 v[138:139], v161 offset:26624\n"
"v_mfma_f32_16x16x32_bf16 v[100:103], v[144:147], a[68:71], v[100:103]\n"
"s_add_u32 s18, s96, 16384\n"
"buffer_load_dwordx4 v[232:235], v185, s[48:51], s18 offen sc1\n"
"v_mfma_f32_16x16x32_bf16 v[104:107], v[144:147], a[72:75], v[104:107]\n"
"s_add_u32 s18, s96, 24576\n"
"buffer_load_dwordx4 v[236:239], v185, s[48:51], s18 offen sc1\n"
"v_mfma_f32_16x16x32_bf16 v[108:111], v[144:147], a[76:79], v[108:111]\n"
"v_exp_f32_e64 v151, -v151\n"
"v_mfma_f32_16x16x32_bf16 v[112:115], v[128:131], a[64:67], v[112:115]\n"
"ds_read_b64_tr_b16 v[140:141], v160 offset:19456\n"
"ds_read_b64_tr_b16 v[142:143], v160 offset:27648\n"
"v_mfma_f32_16x16x32_bf16 v[116:119], v[128:131], a[68:71], v[116:119]\n"
"buffer_load_dwordx4 v172, s[36:39], s66 offen offset:3072 lds\n"
"v_add_f32 v151, 1.0, v151\n"
"v_mfma_f32_16x16x32_bf16 v[120:123], v[128:131], a[72:75], v[120:123]\n"
"v_rcp_f32 v150, v150\n"
"v_mfma_f32_16x16x32_bf16 v[124:127], v[128:131], a[76:79], v[124:127]\n"
"v_mul_f32 v50, v50, v150\n"
"s_waitcnt lgkmcnt(2)\n"
"v_mfma_f32_16x16x32_bf16 v[64:67], v[132:135], a[80:83], v[64:67]\n"
"ds_read_b64_tr_b16 v[144:145], v161 offset:19456\n"
"ds_read_b64_tr_b16 v[146:147], v161 offset:27648\n"
"v_mfma_f32_16x16x32_bf16 v[68:71], v[132:135], a[84:87], v[68:71]\n"
"v_rcp_f32 v151, v151\n"
"v_mfma_f32_16x16x32_bf16 v[72:75], v[132:135], a[88:91], v[72:75]\n"
"v_mul_f32 v51, v51, v151\n"
"v_cvt_pk_bf16_f32 v155, v50, v51\n"
"v_mfma_f32_16x16x32_bf16 v[76:79], v[132:135], a[92:95], v[76:79]\n"
"ds_write_b128 v156, v[152:155]\n"
"buffer_store_dwordx4 v[152:155], v158, s[48:51], s17 offen sc1\n"
"v_mfma_f32_16x16x32_bf16 v[80:83], v[136:139], a[80:83], v[80:83]\n"
"s_barrier\n"
"v_mfma_f32_16x16x32_bf16 v[84:87], v[136:139], a[84:87], v[84:87]\n"
"ds_read_b64_tr_b16 v[128:129], v160 offset:32768\n"
"ds_read_b64_tr_b16 v[130:131], v160 offset:40960\n"
"v_mfma_f32_16x16x32_bf16 v[88:91], v[136:139], a[88:91], v[88:91]\n"
"s_add_u32 m0, s65, 0\n"
"s_add_u32 s66, s70, 4129024\n"
"buffer_load_dwordx4 v169, s[28:31], s66 offen lds\n"
"v_mfma_f32_16x16x32_bf16 v[92:95], v[136:139], a[92:95], v[92:95]\n"
"v_fma_f32 v148, v4, v4, s75\n"
"s_waitcnt lgkmcnt(3)\n"
"v_mfma_f32_16x16x32_bf16 v[96:99], v[140:143], a[80:83], v[96:99]\n"
"ds_read_b64_tr_b16 v[132:133], v161 offset:32768\n"
"ds_read_b64_tr_b16 v[134:135], v161 offset:40960\n"
"v_mfma_f32_16x16x32_bf16 v[100:103], v[140:143], a[84:87], v[100:103]\n"
"v_fma_f32 v149, v5, v5, s75\n"
"v_mul_f32 v148, v148, v4\n"
"v_mfma_f32_16x16x32_bf16 v[104:107], v[140:143], a[88:91], v[104:107]\n"
"v_mul_f32 v149, v149, v5\n"
"v_mul_f32 v36, v4, v36\n"
"v_mfma_f32_16x16x32_bf16 v[108:111], v[140:143], a[92:95], v[108:111]\n"
"v_exp_f32_e64 v148, -v148\n"
"v_mfma_f32_16x16x32_bf16 v[112:115], v[144:147], a[80:83], v[112:115]\n"
"ds_read_b64_tr_b16 v[136:137], v160 offset:33792\n"
"ds_read_b64_tr_b16 v[138:139], v160 offset:41984\n"
"v_mfma_f32_16x16x32_bf16 v[116:119], v[144:147], a[84:87], v[116:119]\n"
"buffer_load_dwordx4 v170, s[36:39], s66 offen offset:1024 lds\n"
"v_mul_f32 v37, v5, v37\n"
"v_mfma_f32_16x16x32_bf16 v[120:123], v[144:147], a[88:91], v[120:123]\n"
"v_exp_f32_e64 v149, -v149\n"
"v_mfma_f32_16x16x32_bf16 v[124:127], v[144:147], a[92:95], v[124:127]\n"
"v_add_f32 v148, 1.0, v148\n"
"s_waitcnt lgkmcnt(2)\n"
"v_mfma_f32_16x16x32_bf16 v[64:67], v[128:131], a[96:99], v[64:67]\n"
"ds_read_b64_tr_b16 v[140:141], v161 offset:33792\n"
"ds_read_b64_tr_b16 v[142:143], v161 offset:41984\n"
"v_mfma_f32_16x16x32_bf16 v[68:71], v[128:131], a[100:103], v[68:71]\n"
"v_add_f32 v149, 1.0, v149\n"
"v_fma_f32 v150, v6, v6, s75\n"
"v_mfma_f32_16x16x32_bf16 v[72:75], v[128:131], a[104:107], v[72:75]\n"
"v_rcp_f32 v148, v148\n"
"v_mfma_f32_16x16x32_bf16 v[76:79], v[128:131], a[108:111], v[76:79]\n"
"v_rcp_f32 v149, v149\n"
"v_mfma_f32_16x16x32_bf16 v[80:83], v[132:135], a[96:99], v[80:83]\n"
"ds_read_b64_tr_b16 v[144:145], v160 offset:34816\n"
"ds_read_b64_tr_b16 v[146:147], v160 offset:43008\n"
"v_mfma_f32_16x16x32_bf16 v[84:87], v[132:135], a[100:103], v[84:87]\n"
"buffer_load_dwordx4 v171, s[28:31], s66 offen offset:2048 lds\n"
"v_mul_f32 v36, v36, v148\n"
"v_mfma_f32_16x16x32_bf16 v[88:91], v[132:135], a[104:107], v[88:91]\n"
"v_mul_f32 v37, v37, v149\n"
"v_cvt_pk_bf16_f32 v152, v36, v37\n"
"v_mfma_f32_16x16x32_bf16 v[92:95], v[132:135], a[108:111], v[92:95]\n"
"v_fma_f32 v151, v7, v7, s75\n"
"s_waitcnt lgkmcnt(2)\n"
"v_mfma_f32_16x16x32_bf16 v[96:99], v[136:139], a[96:99], v[96:99]\n"
"ds_read_b64_tr_b16 v[128:129], v161 offset:34816\n"
"ds_read_b64_tr_b16 v[130:131], v161 offset:43008\n"
"v_mfma_f32_16x16x32_bf16 v[100:103], v[136:139], a[100:103], v[100:103]\n"
"v_mul_f32 v150, v150, v6\n"
"v_mul_f32 v151, v151, v7\n"
"v_mfma_f32_16x16x32_bf16 v[104:107], v[136:139], a[104:107], v[104:107]\n"
"v_exp_f32_e64 v150, -v150\n"
"v_mfma_f32_16x16x32_bf16 v[108:111], v[136:139], a[108:111], v[108:111]\n"
"v_exp_f32_e64 v151, -v151\n"
"v_mfma_f32_16x16x32_bf16 v[112:115], v[140:143], a[96:99], v[112:115]\n"
"ds_read_b64_tr_b16 v[132:133], v160 offset:35840\n"
"ds_read_b64_tr_b16 v[134:135], v160 offset:44032\n"
"v_mfma_f32_16x16x32_bf16 v[116:119], v[140:143], a[100:103], v[116:119]\n"
"buffer_load_dwordx4 v172, s[36:39], s66 offen offset:3072 lds\n"
"v_mul_f32 v38, v6, v38\n"
"v_mfma_f32_16x16x32_bf16 v[120:123], v[140:143], a[104:107], v[120:123]\n"
"v_mul_f32 v39, v7, v39\n"
"v_add_f32 v150, 1.0, v150\n"
"v_mfma_f32_16x16x32_bf16 v[124:127], v[140:143], a[108:111], v[124:127]\n"
"v_add_f32 v151, 1.0, v151\n"
"s_waitcnt lgkmcnt(2)\n"
"v_mfma_f32_16x16x32_bf16 v[64:67], v[144:147], a[112:115], v[64:67]\n"
"ds_read_b64_tr_b16 v[136:137], v161 offset:35840\n"
"ds_read_b64_tr_b16 v[138:139], v161 offset:44032\n"
"v_mfma_f32_16x16x32_bf16 v[68:71], v[144:147], a[116:119], v[68:71]\n"
"s_waitcnt vmcnt(6)\n"
"v_add_f32 v224, v224, v228\n"
"v_mfma_f32_16x16x32_bf16 v[72:75], v[144:147], a[120:123], v[72:75]\n"
"v_add_f32 v225, v225, v229\n"
"v_add_f32 v226, v226, v230\n"
"v_mfma_f32_16x16x32_bf16 v[76:79], v[144:147], a[124:127], v[76:79]\n"
"v_add_f32 v227, v227, v231\n"
"v_add_f32 v224, v224, v232\n"
"v_mfma_f32_16x16x32_bf16 v[80:83], v[128:131], a[112:115], v[80:83]\n"
"s_barrier\n"
"v_mfma_f32_16x16x32_bf16 v[84:87], v[128:131], a[116:119], v[84:87]\n"
"ds_read_b64_tr_b16 v[140:141], v160 offset:49152\n"
"ds_read_b64_tr_b16 v[142:143], v160 offset:57344\n"
"v_mfma_f32_16x16x32_bf16 v[88:91], v[128:131], a[120:123], v[88:91]\n"
"s_add_u32 m0, s65, 16384\n"
"s_add_u32 s66, s70, 4718848\n"
"buffer_load_dwordx4 v169, s[28:31], s66 offen lds\n"
"v_mfma_f32_16x16x32_bf16 v[92:95], v[128:131], a[124:127], v[92:95]\n"
"v_add_f32 v225, v225, v233\n"
"s_waitcnt lgkmcnt(2)\n"
"v_mfma_f32_16x16x32_bf16 v[96:99], v[132:135], a[112:115], v[96:99]\n"
"ds_read_b64_tr_b16 v[144:145], v161 offset:49152\n"
"ds_read_b64_tr_b16 v[146:147], v161 offset:57344\n"
"v_mfma_f32_16x16x32_bf16 v[100:103], v[132:135], a[116:119], v[100:103]\n"
"v_add_f32 v226, v226, v234\n"
"v_add_f32 v227, v227, v235\n"
"v_mfma_f32_16x16x32_bf16 v[104:107], v[132:135], a[120:123], v[104:107]\n"
"v_add_f32 v224, v224, v236\n"
"v_add_f32 v225, v225, v237\n"
"v_mfma_f32_16x16x32_bf16 v[108:111], v[132:135], a[124:127], v[108:111]\n"
"v_add_f32 v226, v226, v238\n"
"v_add_f32 v227, v227, v239\n"
"v_mfma_f32_16x16x32_bf16 v[112:115], v[136:139], a[112:115], v[112:115]\n"
"ds_read_b64_tr_b16 v[128:129], v160 offset:50176\n"
"ds_read_b64_tr_b16 v[130:131], v160 offset:58368\n"
"v_mfma_f32_16x16x32_bf16 v[116:119], v[136:139], a[116:119], v[116:119]\n"
"buffer_load_dwordx4 v170, s[36:39], s66 offen offset:1024 lds\n"
"v_mov_b32 v228, v224\n"
"v_mfma_f32_16x16x32_bf16 v[120:123], v[136:139], a[120:123], v[120:123]\n"
"v_mov_b32 v229, v225\n"
"v_mov_b32 v230, v226\n"
"v_mfma_f32_16x16x32_bf16 v[124:127], v[136:139], a[124:127], v[124:127]\n"
"v_mov_b32 v231, v227\n"
"s_waitcnt lgkmcnt(2)\n"
"v_mfma_f32_16x16x32_bf16 v[64:67], v[140:143], a[128:131], v[64:67]\n"
"ds_read_b64_tr_b16 v[132:133], v161 offset:50176\n"
"ds_read_b64_tr_b16 v[134:135], v161 offset:58368\n"
"v_mfma_f32_16x16x32_bf16 v[68:71], v[140:143], a[132:135], v[68:71]\n"
"v_permlane32_swap_b32 v224, v228\n"
"v_permlane32_swap_b32 v225, v229\n"
"v_mfma_f32_16x16x32_bf16 v[72:75], v[140:143], a[136:139], v[72:75]\n"
"v_permlane32_swap_b32 v226, v230\n"
"v_permlane32_swap_b32 v227, v231\n"
"v_mfma_f32_16x16x32_bf16 v[76:79], v[140:143], a[140:143], v[76:79]\n"
"v_add_f32 v224, v224, v228\n"
"v_add_f32 v225, v225, v229\n"
"v_mfma_f32_16x16x32_bf16 v[80:83], v[144:147], a[128:131], v[80:83]\n"
"ds_read_b64_tr_b16 v[136:137], v160 offset:51200\n"
"ds_read_b64_tr_b16 v[138:139], v160 offset:59392\n"
"v_mfma_f32_16x16x32_bf16 v[84:87], v[144:147], a[132:135], v[84:87]\n"
"buffer_load_dwordx4 v171, s[28:31], s66 offen offset:2048 lds\n"
"v_add_f32 v226, v226, v230\n"
"v_mfma_f32_16x16x32_bf16 v[88:91], v[144:147], a[136:139], v[88:91]\n"
"v_add_f32 v227, v227, v231\n"
"v_mov_b32 v228, v224\n"
"v_mfma_f32_16x16x32_bf16 v[92:95], v[144:147], a[140:143], v[92:95]\n"
"v_mov_b32 v229, v225\n"
"s_waitcnt lgkmcnt(2)\n"
"v_mfma_f32_16x16x32_bf16 v[96:99], v[128:131], a[128:131], v[96:99]\n"
"ds_read_b64_tr_b16 v[140:141], v161 offset:51200\n"
"ds_read_b64_tr_b16 v[142:143], v161 offset:59392\n"
"v_mfma_f32_16x16x32_bf16 v[100:103], v[128:131], a[132:135], v[100:103]\n"
"v_mov_b32 v230, v226\n"
"v_mov_b32 v231, v227\n"
"v_mfma_f32_16x16x32_bf16 v[104:107], v[128:131], a[136:139], v[104:107]\n"
"v_permlane16_swap_b32 v224, v228\n"
"v_permlane16_swap_b32 v225, v229\n"
"v_mfma_f32_16x16x32_bf16 v[108:111], v[128:131], a[140:143], v[108:111]\n"
"v_permlane16_swap_b32 v226, v230\n"
"v_permlane16_swap_b32 v227, v231\n"
"v_mfma_f32_16x16x32_bf16 v[112:115], v[132:135], a[128:131], v[112:115]\n"
"ds_read_b64_tr_b16 v[144:145], v160 offset:52224\n"
"ds_read_b64_tr_b16 v[146:147], v160 offset:60416\n"
"v_mfma_f32_16x16x32_bf16 v[116:119], v[132:135], a[132:135], v[116:119]\n"
"buffer_load_dwordx4 v172, s[36:39], s66 offen offset:3072 lds\n"
"v_add_f32 v224, v224, v228\n"
"v_mfma_f32_16x16x32_bf16 v[120:123], v[132:135], a[136:139], v[120:123]\n"
"v_add_f32 v225, v225, v229\n"
"v_add_f32 v226, v226, v230\n"
"v_mfma_f32_16x16x32_bf16 v[124:127], v[132:135], a[140:143], v[124:127]\n"
"v_add_f32 v227, v227, v231\n"
"s_waitcnt lgkmcnt(2)\n"
"v_mfma_f32_16x16x32_bf16 v[64:67], v[136:139], a[144:147], v[64:67]\n"
"ds_read_b64_tr_b16 v[128:129], v161 offset:52224\n"
"ds_read_b64_tr_b16 v[130:131], v161 offset:60416\n"
"v_mfma_f32_16x16x32_bf16 v[68:71], v[136:139], a[148:151], v[68:71]\n"
"v_cndmask_b32_e64 v224, 0, v224, s[86:87]\n"
"v_cndmask_b32_e64 v225, 0, v225, s[88:89]\n"
"v_mfma_f32_16x16x32_bf16 v[72:75], v[136:139], a[152:155], v[72:75]\n"
"v_cndmask_b32_e64 v226, 0, v226, s[90:91]\n"
"v_cndmask_b32_e64 v227, 0, v227, s[92:93]\n"
"v_mfma_f32_16x16x32_bf16 v[76:79], v[136:139], a[156:159], v[76:79]\n"
"v_cvt_pk_bf16_f32 v228, v224, v225\n"
"v_cvt_pk_bf16_f32 v229, v226, v227\n"
"v_mfma_f32_16x16x32_bf16 v[80:83], v[140:143], a[144:147], v[80:83]\n"
"s_barrier\n"
"v_mfma_f32_16x16x32_bf16 v[84:87], v[140:143], a[148:151], v[84:87]\n"
"ds_read_b64_tr_b16 v[132:133], v162\n"
"ds_read_b64_tr_b16 v[134:135], v162 offset:8192\n"
"v_mfma_f32_16x16x32_bf16 v[88:91], v[140:143], a[152:155], v[88:91]\n"
"s_add_u32 m0, s65, 32768\n"
"s_add_u32 s66, s70, 384\n"
"buffer_load_dwordx4 v169, s[28:31], s66 offen lds\n"
"v_mfma_f32_16x16x32_bf16 v[92:95], v[140:143], a[156:159], v[92:95]\n"
"buffer_store_dwordx2 v[228:229], v186, s[48:51], s97 offen sc1\n"
"s_waitcnt lgkmcnt(2)\n"
"v_mfma_f32_16x16x32_bf16 v[96:99], v[144:147], a[144:147], v[96:99]\n"
"ds_read_b64_tr_b16 v[136:137], v163\n"
"ds_read_b64_tr_b16 v[138:139], v163 offset:8192\n"
"v_mfma_f32_16x16x32_bf16 v[100:103], v[144:147], a[148:151], v[100:103]\n"
"v_rcp_f32 v150, v150\n"
"v_mfma_f32_16x16x32_bf16 v[104:107], v[144:147], a[152:155], v[104:107]\n"
"v_rcp_f32 v151, v151\n"
"v_mfma_f32_16x16x32_bf16 v[108:111], v[144:147], a[156:159], v[108:111]\n"
"v_mul_f32 v38, v38, v150\n"
"v_mul_f32 v39, v39, v151\n"
"v_mfma_f32_16x16x32_bf16 v[112:115], v[128:131], a[144:147], v[112:115]\n"
"ds_read_b64_tr_b16 v[140:141], v162 offset:1024\n"
"ds_read_b64_tr_b16 v[142:143], v162 offset:9216\n"
"v_mfma_f32_16x16x32_bf16 v[116:119], v[128:131], a[148:151], v[116:119]\n"
"buffer_load_dwordx4 v170, s[36:39], s66 offen offset:1024 lds\n"
"v_cvt_pk_bf16_f32 v153, v38, v39\n"
"v_mfma_f32_16x16x32_bf16 v[120:123], v[128:131], a[152:155], v[120:123]\n"
"v_fma_f32 v148, v20, v20, s75\n"
"v_fma_f32 v149, v21, v21, s75\n"
"v_mfma_f32_16x16x32_bf16 v[124:127], v[128:131], a[156:159], v[124:127]\n"
"v_mul_f32 v148, v148, v20\n"
"s_waitcnt lgkmcnt(2)\n"
"v_mfma_f32_16x16x32_bf16 v[64:67], v[132:135], a[160:163], v[64:67]\n"
"ds_read_b64_tr_b16 v[144:145], v163 offset:1024\n"
"ds_read_b64_tr_b16 v[146:147], v163 offset:9216\n"
"v_mfma_f32_16x16x32_bf16 v[68:71], v[132:135], a[164:167], v[68:71]\n"
"s_lshr_b32 s18, %[bid], 3\n"
"s_lshl_b32 s18, s18, 2\n"
"s_add_u32 s18, s18, s60\n"
"s_mul_i32 s18, s18, 41\n"
"s_add_u32 s18, s18, 22\n"
"v_lshrrev_b32 v182, 4, v158\n"
"v_cmp_gt_u32 vcc, 11, v182\n"
"v_add_u32 v184, s18, v182\n"
"v_lshlrev_b32 v184, 7, v184\n"
"v_mov_b32 v185, 2147483647\n"
"v_cndmask_b32 v184, v185, v184, vcc\n"
"s_lshr_b32 s18, s63, 1\n"
"s_mul_i32 s18, s18, 663552\n"
"s_add_u32 s18, s18, 3072\n"
"buffer_load_dword v186, v184, s[40:43], s18 offen\n"
"v_mfma_f32_16x16x32_bf16 v[72:75], v[132:135], a[168:171], v[72:75]\n"
"v_mul_f32 v149, v149, v21\n"
"v_mul_f32 v52, v20, v52\n"
"v_mfma_f32_16x16x32_bf16 v[76:79], v[132:135], a[172:175], v[76:79]\n"
"v_exp_f32_e64 v148, -v148\n"
"v_mfma_f32_16x16x32_bf16 v[80:83], v[136:139], a[160:163], v[80:83]\n"
"ds_read_b64_tr_b16 v[128:129], v162 offset:2048\n"
"ds_read_b64_tr_b16 v[130:131], v162 offset:10240\n"
"v_mfma_f32_16x16x32_bf16 v[84:87], v[136:139], a[164:167], v[84:87]\n"
"buffer_load_dwordx4 v171, s[28:31], s66 offen offset:2048 lds\n"
"v_mul_f32 v53, v21, v53\n"
"v_mfma_f32_16x16x32_bf16 v[88:91], v[136:139], a[168:171], v[88:91]\n"
"v_exp_f32_e64 v149, -v149\n"
"v_mfma_f32_16x16x32_bf16 v[92:95], v[136:139], a[172:175], v[92:95]\n"
"v_add_f32 v148, 1.0, v148\n"
"s_waitcnt lgkmcnt(2)\n"
"v_mfma_f32_16x16x32_bf16 v[96:99], v[140:143], a[160:163], v[96:99]\n"
"ds_read_b64_tr_b16 v[132:133], v163 offset:2048\n"
"ds_read_b64_tr_b16 v[134:135], v163 offset:10240\n"
"v_mfma_f32_16x16x32_bf16 v[100:103], v[140:143], a[164:167], v[100:103]\n"
"v_add_f32 v149, 1.0, v149\n"
"v_fma_f32 v150, v22, v22, s75\n"
"v_mfma_f32_16x16x32_bf16 v[104:107], v[140:143], a[168:171], v[104:107]\n"
"v_rcp_f32 v148, v148\n"
"v_mfma_f32_16x16x32_bf16 v[108:111], v[140:143], a[172:175], v[108:111]\n"
"v_rcp_f32 v149, v149\n"
"v_mfma_f32_16x16x32_bf16 v[112:115], v[144:147], a[160:163], v[112:115]\n"
"ds_read_b64_tr_b16 v[136:137], v162 offset:3072\n"
"ds_read_b64_tr_b16 v[138:139], v162 offset:11264\n"
"v_mfma_f32_16x16x32_bf16 v[116:119], v[144:147], a[164:167], v[116:119]\n"
"buffer_load_dwordx4 v172, s[36:39], s66 offen offset:3072 lds\n"
"v_mul_f32 v52, v52, v148\n"
"v_mfma_f32_16x16x32_bf16 v[120:123], v[144:147], a[168:171], v[120:123]\n"
"v_mul_f32 v53, v53, v149\n"
"v_cvt_pk_bf16_f32 v154, v52, v53\n"
"v_mfma_f32_16x16x32_bf16 v[124:127], v[144:147], a[172:175], v[124:127]\n"
"v_fma_f32 v151, v23, v23, s75\n"
"s_waitcnt lgkmcnt(2)\n"
"v_mfma_f32_16x16x32_bf16 v[64:67], v[128:131], a[176:179], v[64:67]\n"
"ds_read_b64_tr_b16 v[140:141], v163 offset:3072\n"
"ds_read_b64_tr_b16 v[142:143], v163 offset:11264\n"
"v_mfma_f32_16x16x32_bf16 v[68:71], v[128:131], a[180:183], v[68:71]\n"
"v_mul_f32 v150, v150, v22\n"
"v_mul_f32 v151, v151, v23\n"
"v_mfma_f32_16x16x32_bf16 v[72:75], v[128:131], a[184:187], v[72:75]\n"
"v_exp_f32_e64 v150, -v150\n"
"v_mfma_f32_16x16x32_bf16 v[76:79], v[128:131], a[188:191], v[76:79]\n"
"v_exp_f32_e64 v151, -v151\n"
"v_mfma_f32_16x16x32_bf16 v[80:83], v[132:135], a[176:179], v[80:83]\n"
"s_waitcnt vmcnt(15)\n"
"s_barrier\n"
"v_mfma_f32_16x16x32_bf16 v[84:87], v[132:135], a[180:183], v[84:87]\n"
"ds_read_b64_tr_b16 v[144:145], v162 offset:16384\n"
"ds_read_b64_tr_b16 v[146:147], v162 offset:24576\n"
"v_mfma_f32_16x16x32_bf16 v[88:91], v[132:135], a[184:187], v[88:91]\n"
"s_add_u32 m0, s65, 49152\n"
"s_add_u32 s66, s70, 590208\n"
"buffer_load_dwordx4 v169, s[28:31], s66 offen lds\n"
"v_mfma_f32_16x16x32_bf16 v[92:95], v[132:135], a[188:191], v[92:95]\n"
"v_mul_f32 v54, v22, v54\n"
"s_waitcnt lgkmcnt(2)\n"
"v_mfma_f32_16x16x32_bf16 v[96:99], v[136:139], a[176:179], v[96:99]\n"
"ds_read_b64_tr_b16 v[128:129], v163 offset:16384\n"
"ds_read_b64_tr_b16 v[130:131], v163 offset:24576\n"
"v_mfma_f32_16x16x32_bf16 v[100:103], v[136:139], a[180:183], v[100:103]\n"
"v_mul_f32 v55, v23, v55\n"
"v_add_f32 v150, 1.0, v150\n"
"v_mfma_f32_16x16x32_bf16 v[104:107], v[136:139], a[184:187], v[104:107]\n"
"v_add_f32 v151, 1.0, v151\n"
"v_mfma_f32_16x16x32_bf16 v[108:111], v[136:139], a[188:191], v[108:111]\n"
"v_rcp_f32 v150, v150\n"
"v_mfma_f32_16x16x32_bf16 v[112:115], v[140:143], a[176:179], v[112:115]\n"
"ds_read_b64_tr_b16 v[132:133], v162 offset:17408\n"
"ds_read_b64_tr_b16 v[134:135], v162 offset:25600\n"
"v_mfma_f32_16x16x32_bf16 v[116:119], v[140:143], a[180:183], v[116:119]\n"
"buffer_load_dwordx4 v170, s[36:39], s66 offen offset:1024 lds\n"
"v_mul_f32 v54, v54, v150\n"
"v_mfma_f32_16x16x32_bf16 v[120:123], v[140:143], a[184:187], v[120:123]\n"
"v_rcp_f32 v151, v151\n"
"v_mfma_f32_16x16x32_bf16 v[124:127], v[140:143], a[188:191], v[124:127]\n"
"v_mul_f32 v55, v55, v151\n"
"s_waitcnt lgkmcnt(2)\n"
"v_mfma_f32_16x16x32_bf16 v[64:67], v[144:147], a[192:195], v[64:67]\n"
"ds_read_b64_tr_b16 v[136:137], v163 offset:17408\n"
"ds_read_b64_tr_b16 v[138:139], v163 offset:25600\n"
"v_mfma_f32_16x16x32_bf16 v[68:71], v[144:147], a[196:199], v[68:71]\n"
"v_cvt_pk_bf16_f32 v155, v54, v55\n"
"ds_write_b128 v156, v[152:155] offset:1024\n"
"v_mfma_f32_16x16x32_bf16 v[72:75], v[144:147], a[200:203], v[72:75]\n"
"buffer_store_dwordx4 v[152:155], v158, s[48:51], s17 offen offset:1024 sc1\n"
"v_fma_f32 v148, v8, v8, s75\n"
"v_mfma_f32_16x16x32_bf16 v[76:79], v[144:147], a[204:207], v[76:79]\n"
"v_fma_f32 v149, v9, v9, s75\n"
"v_mul_f32 v148, v148, v8\n"
"v_mfma_f32_16x16x32_bf16 v[80:83], v[128:131], a[192:195], v[80:83]\n"
"ds_read_b64_tr_b16 v[140:141], v162 offset:18432\n"
"ds_read_b64_tr_b16 v[142:143], v162 offset:26624\n"
"v_mfma_f32_16x16x32_bf16 v[84:87], v[128:131], a[196:199], v[84:87]\n"
"buffer_load_dwordx4 v171, s[28:31], s66 offen offset:2048 lds\n"
"v_mul_f32 v149, v149, v9\n"
"v_mfma_f32_16x16x32_bf16 v[88:91], v[128:131], a[200:203], v[88:91]\n"
"v_exp_f32_e64 v148, -v148\n"
"v_mfma_f32_16x16x32_bf16 v[92:95], v[128:131], a[204:207], v[92:95]\n"
"v_mul_f32 v40, v8, v40\n"
"s_waitcnt lgkmcnt(3)\n"
"v_mfma_f32_16x16x32_bf16 v[96:99], v[132:135], a[192:195], v[96:99]\n"
"ds_read_b64_tr_b16 v[144:145], v163 offset:18432\n"
"ds_read_b64_tr_b16 v[146:147], v163 offset:26624\n"
"v_mfma_f32_16x16x32_bf16 v[100:103], v[132:135], a[196:199], v[100:103]\n"
"v_exp_f32_e64 v149, -v149\n"
"v_mfma_f32_16x16x32_bf16 v[104:107], v[132:135], a[200:203], v[104:107]\n"
"v_mul_f32 v41, v9, v41\n"
"v_add_f32 v148, 1.0, v148\n"
"v_mfma_f32_16x16x32_bf16 v[108:111], v[132:135], a[204:207], v[108:111]\n"
"v_add_f32 v149, 1.0, v149\n"
"v_fma_f32 v150, v10, v10, s75\n"
"v_mfma_f32_16x16x32_bf16 v[112:115], v[136:139], a[192:195], v[112:115]\n"
"ds_read_b64_tr_b16 v[128:129], v162 offset:19456\n"
"ds_read_b64_tr_b16 v[130:131], v162 offset:27648\n"
"v_mfma_f32_16x16x32_bf16 v[116:119], v[136:139], a[196:199], v[116:119]\n"
"buffer_load_dwordx4 v172, s[36:39], s66 offen offset:3072 lds\n"
"v_fma_f32 v151, v11, v11, s75\n"
"v_mfma_f32_16x16x32_bf16 v[120:123], v[136:139], a[200:203], v[120:123]\n"
"v_rcp_f32 v148, v148\n"
"v_mfma_f32_16x16x32_bf16 v[124:127], v[136:139], a[204:207], v[124:127]\n"
"v_mul_f32 v40, v40, v148\n"
"s_waitcnt lgkmcnt(2)\n"
"v_mfma_f32_16x16x32_bf16 v[64:67], v[140:143], a[208:211], v[64:67]\n"
"ds_read_b64_tr_b16 v[132:133], v163 offset:19456\n"
"ds_read_b64_tr_b16 v[134:135], v163 offset:27648\n"
"v_mfma_f32_16x16x32_bf16 v[68:71], v[140:143], a[212:215], v[68:71]\n"
"v_rcp_f32 v149, v149\n"
"v_mfma_f32_16x16x32_bf16 v[72:75], v[140:143], a[216:219], v[72:75]\n"
"v_mul_f32 v41, v41, v149\n"
"v_cvt_pk_bf16_f32 v152, v40, v41\n"
"v_mfma_f32_16x16x32_bf16 v[76:79], v[140:143], a[220:223], v[76:79]\n"
"v_mul_f32 v150, v150, v10\n"
"v_mul_f32 v151, v151, v11\n"
"v_mfma_f32_16x16x32_bf16 v[80:83], v[144:147], a[208:211], v[80:83]\n"
"s_waitcnt vmcnt(9)\n"
"s_barrier\n"
"s_lshl_b32 s68, s63, 8\n"
"s_add_u32 s68, s68, 4096\n"
"s_cmp_eq_u32 s60, 0\n"
"s_cbranch_scc0 Lsig2_%=\n"
"v_mov_b32 v180, 1\n"
"s_mov_b64 exec, 1\n"
"buffer_atomic_add v180, off, s[48:51], s68\n"
"s_mov_b64 exec, -1\n"
"Lsig2_%=:\n"
"v_mfma_f32_16x16x32_bf16 v[84:87], v[144:147], a[212:215], v[84:87]\n"
"ds_read_b64_tr_b16 v[136:137], v160\n"
"ds_read_b64_tr_b16 v[138:139], v160 offset:8192\n"
"v_mfma_f32_16x16x32_bf16 v[88:91], v[144:147], a[216:219], v[88:91]\n"
"s_add_u32 m0, s65, 65536\n"
"s_add_u32 s66, s70, 1180032\n"
"buffer_load_dwordx4 v169, s[28:31], s66 offen lds\n"
"v_mfma_f32_16x16x32_bf16 v[92:95], v[144:147], a[220:223], v[92:95]\n"
"v_mul_f32 v42, v10, v42\n"
"s_waitcnt lgkmcnt(2)\n"
"v_mfma_f32_16x16x32_bf16 v[96:99], v[128:131], a[208:211], v[96:99]\n"
"ds_read_b64_tr_b16 v[140:141], v161\n"
"ds_read_b64_tr_b16 v[142:143], v161 offset:8192\n"
"v_mfma_f32_16x16x32_bf16 v[100:103], v[128:131], a[212:215], v[100:103]\n"
"v_exp_f32_e64 v150, -v150\n"
"v_mfma_f32_16x16x32_bf16 v[104:107], v[128:131], a[216:219], v[104:107]\n"
"v_exp_f32_e64 v151, -v151\n"
"v_mfma_f32_16x16x32_bf16 v[108:111], v[128:131], a[220:223], v[108:111]\n"
"v_mul_f32 v43, v11, v43\n"
"v_add_f32 v150, 1.0, v150\n"
"v_mfma_f32_16x16x32_bf16 v[112:115], v[132:135], a[208:211], v[112:115]\n"
"ds_read_b64_tr_b16 v[144:145], v160 offset:1024\n"
"ds_read_b64_tr_b16 v[146:147], v160 offset:9216\n"
"v_mfma_f32_16x16x32_bf16 v[116:119], v[132:135], a[212:215], v[116:119]\n"
"buffer_load_dwordx4 v170, s[36:39], s66 offen offset:1024 lds\n"
"v_add_f32 v151, 1.0, v151\n"
"v_mfma_f32_16x16x32_bf16 v[120:123], v[132:135], a[216:219], v[120:123]\n"
"v_rcp_f32 v150, v150\n"
"v_mfma_f32_16x16x32_bf16 v[124:127], v[132:135], a[220:223], v[124:127]\n"
"v_mul_f32 v42, v42, v150\n"
"s_waitcnt lgkmcnt(2)\n"
"v_mfma_f32_16x16x32_bf16 v[64:67], v[136:139], a[224:227], v[64:67]\n"
"ds_read_b64_tr_b16 v[128:129], v161 offset:1024\n"
"ds_read_b64_tr_b16 v[130:131], v161 offset:9216\n"
"v_mfma_f32_16x16x32_bf16 v[68:71], v[136:139], a[228:231], v[68:71]\n"
"v_rcp_f32 v151, v151\n"
"v_mfma_f32_16x16x32_bf16 v[72:75], v[136:139], a[232:235], v[72:75]\n"
"v_mul_f32 v43, v43, v151\n"
"v_cvt_pk_bf16_f32 v153, v42, v43\n"
"v_mfma_f32_16x16x32_bf16 v[76:79], v[136:139], a[236:239], v[76:79]\n"
"v_fma_f32 v148, v24, v24, s75\n"
"v_fma_f32 v149, v25, v25, s75\n"
"v_mfma_f32_16x16x32_bf16 v[80:83], v[140:143], a[224:227], v[80:83]\n"
"ds_read_b64_tr_b16 v[132:133], v160 offset:2048\n"
"ds_read_b64_tr_b16 v[134:135], v160 offset:10240\n"
"v_mfma_f32_16x16x32_bf16 v[84:87], v[140:143], a[228:231], v[84:87]\n"
"buffer_load_dwordx4 v171, s[28:31], s66 offen offset:2048 lds\n"
"v_mul_f32 v148, v148, v24\n"
"v_mfma_f32_16x16x32_bf16 v[88:91], v[140:143], a[232:235], v[88:91]\n"
"v_mul_f32 v149, v149, v25\n"
"v_mul_f32 v56, v24, v56\n"
"v_mfma_f32_16x16x32_bf16 v[92:95], v[140:143], a[236:239], v[92:95]\n"
"v_mul_f32 v57, v25, v57\n"
"s_waitcnt lgkmcnt(2)\n"
"v_mfma_f32_16x16x32_bf16 v[96:99], v[144:147], a[224:227], v[96:99]\n"
"ds_read_b64_tr_b16 v[136:137], v161 offset:2048\n"
"ds_read_b64_tr_b16 v[138:139], v161 offset:10240\n"
"v_mfma_f32_16x16x32_bf16 v[100:103], v[144:147], a[228:231], v[100:103]\n"
"v_exp_f32_e64 v148, -v148\n"
"v_mfma_f32_16x16x32_bf16 v[104:107], v[144:147], a[232:235], v[104:107]\n"
"v_exp_f32_e64 v149, -v149\n"
"v_mfma_f32_16x16x32_bf16 v[108:111], v[144:147], a[236:239], v[108:111]\n"
"v_add_f32 v148, 1.0, v148\n"
"v_add_f32 v149, 1.0, v149\n"
"v_mfma_f32_16x16x32_bf16 v[112:115], v[128:131], a[224:227], v[112:115]\n"
"ds_read_b64_tr_b16 v[140:141], v160 offset:3072\n"
"ds_read_b64_tr_b16 v[142:143], v160 offset:11264\n"
"v_mfma_f32_16x16x32_bf16 v[116:119], v[128:131], a[228:231], v[116:119]\n"
"buffer_load_dwordx4 v172, s[36:39], s66 offen offset:3072 lds\n"
"v_fma_f32 v150, v26, v26, s75\n"
"v_mfma_f32_16x16x32_bf16 v[120:123], v[128:131], a[232:235], v[120:123]\n"
"v_rcp_f32 v148, v148\n"
"v_mfma_f32_16x16x32_bf16 v[124:127], v[128:131], a[236:239], v[124:127]\n"
"v_mul_f32 v56, v56, v148\n"
"s_waitcnt lgkmcnt(2)\n"
"v_mfma_f32_16x16x32_bf16 v[64:67], v[132:135], a[240:243], v[64:67]\n"
"ds_read_b64_tr_b16 v[144:145], v161 offset:3072\n"
"ds_read_b64_tr_b16 v[146:147], v161 offset:11264\n"
"v_mfma_f32_16x16x32_bf16 v[68:71], v[132:135], a[244:247], v[68:71]\n"
"v_rcp_f32 v149, v149\n"
"v_mfma_f32_16x16x32_bf16 v[72:75], v[132:135], a[248:251], v[72:75]\n"
"v_mul_f32 v57, v57, v149\n"
"v_cvt_pk_bf16_f32 v154, v56, v57\n"
"v_mfma_f32_16x16x32_bf16 v[76:79], v[132:135], a[252:255], v[76:79]\n"
"v_fma_f32 v151, v27, v27, s75\n"
"v_mul_f32 v150, v150, v26\n"
"v_mfma_f32_16x16x32_bf16 v[80:83], v[136:139], a[240:243], v[80:83]\n"
"s_barrier\n"
"v_mfma_f32_16x16x32_bf16 v[84:87], v[136:139], a[244:247], v[84:87]\n"
"ds_read_b64_tr_b16 v[128:129], v160 offset:16384\n"
"ds_read_b64_tr_b16 v[130:131], v160 offset:24576\n"
"v_mfma_f32_16x16x32_bf16 v[88:91], v[136:139], a[248:251], v[88:91]\n"
"s_add_u32 m0, s65, 81920\n"
"s_add_u32 s66, s70, 1769856\n"
"buffer_load_dwordx4 v169, s[28:31], s66 offen lds\n"
"v_mfma_f32_16x16x32_bf16 v[92:95], v[136:139], a[252:255], v[92:95]\n"
"v_mul_f32 v151, v151, v27\n"
"s_waitcnt lgkmcnt(2)\n"
"v_mfma_f32_16x16x32_bf16 v[96:99], v[140:143], a[240:243], v[96:99]\n"
"ds_read_b64_tr_b16 v[132:133], v161 offset:16384\n"
"ds_read_b64_tr_b16 v[134:135], v161 offset:24576\n"
"v_mfma_f32_16x16x32_bf16 v[100:103], v[140:143], a[244:247], v[100:103]\n"
"v_exp_f32_e64 v150, -v150\n"
"v_mfma_f32_16x16x32_bf16 v[104:107], v[140:143], a[248:251], v[104:107]\n"
"v_exp_f32_e64 v151, -v151\n"
"v_mfma_f32_16x16x32_bf16 v[108:111], v[140:143], a[252:255], v[108:111]\n"
"v_mul_f32 v58, v26, v58\n"
"v_mul_f32 v59, v27, v59\n"
"v_mfma_f32_16x16x32_bf16 v[112:115], v[144:147], a[240:243], v[112:115]\n"
"ds_read_b64_tr_b16 v[136:137], v160 offset:17408\n"
"ds_read_b64_tr_b16 v[138:139], v160 offset:25600\n"
"v_mfma_f32_16x16x32_bf16 v[116:119], v[144:147], a[244:247], v[116:119]\n"
"buffer_load_dwordx4 v170, s[36:39], s66 offen offset:1024 lds\n"
"v_add_f32 v150, 1.0, v150\n"
"v_mfma_f32_16x16x32_bf16 v[120:123], v[144:147], a[248:251], v[120:123]\n"
"v_add_f32 v151, 1.0, v151\n"
"v_mfma_f32_16x16x32_bf16 v[124:127], v[144:147], a[252:255], v[124:127]\n"
"s_waitcnt lgkmcnt(2)\n"
"v_mfma_f32_16x16x32_bf16 v[64:67], v[128:131], v[192:195], v[64:67]\n"
"ds_read_b64_tr_b16 v[140:141], v161 offset:17408\n"
"ds_read_b64_tr_b16 v[142:143], v161 offset:25600\n"
"v_mfma_f32_16x16x32_bf16 v[68:71], v[128:131], v[196:199], v[68:71]\n"
"v_rcp_f32 v150, v150\n"
"v_mfma_f32_16x16x32_bf16 v[72:75], v[128:131], v[200:203], v[72:75]\n"
"v_rcp_f32 v151, v151\n"
"v_mfma_f32_16x16x32_bf16 v[76:79], v[128:131], v[204:207], v[76:79]\n"
"v_mul_f32 v58, v58, v150\n"
"v_mul_f32 v59, v59, v151\n"
"v_mfma_f32_16x16x32_bf16 v[80:83], v[132:135], v[192:195], v[80:83]\n"
"ds_read_b64_tr_b16 v[144:145], v160 offset:18432\n"
"ds_read_b64_tr_b16 v[146:147], v160 offset:26624\n"
"v_mfma_f32_16x16x32_bf16 v[84:87], v[132:135], v[196:199], v[84:87]\n"
"buffer_load_dwordx4 v171, s[28:31], s66 offen offset:2048 lds\n"
"v_cvt_pk_bf16_f32 v155, v58, v59\n"
"v_mfma_f32_16x16x32_bf16 v[88:91], v[132:135], v[200:203], v[88:91]\n"
"ds_write_b128 v156, v[152:155] offset:2048\n"
"buffer_store_dwordx4 v[152:155], v158, s[48:51], s17 offen offset:2048 sc1\n"
"v_mfma_f32_16x16x32_bf16 v[92:95], v[132:135], v[204:207], v[92:95]\n"
"v_fma_f32 v148, v12, v12, s75\n"
"s_waitcnt lgkmcnt(3)\n"
"v_mfma_f32_16x16x32_bf16 v[96:99], v[136:139], v[192:195], v[96:99]\n"
"ds_read_b64_tr_b16 v[128:129], v161 offset:18432\n"
"ds_read_b64_tr_b16 v[130:131], v161 offset:26624\n"
"v_mfma_f32_16x16x32_bf16 v[100:103], v[136:139], v[196:199], v[100:103]\n"
"v_fma_f32 v149, v13, v13, s75\n"
"v_mul_f32 v148, v148, v12\n"
"v_mfma_f32_16x16x32_bf16 v[104:107], v[136:139], v[200:203], v[104:107]\n"
"v_mul_f32 v149, v149, v13\n"
"v_mul_f32 v44, v12, v44\n"
"v_mfma_f32_16x16x32_bf16 v[108:111], v[136:139], v[204:207], v[108:111]\n"
"v_exp_f32_e64 v148, -v148\n"
"v_mfma_f32_16x16x32_bf16 v[112:115], v[140:143], v[192:195], v[112:115]\n"
"ds_read_b64_tr_b16 v[132:133], v160 offset:19456\n"
"ds_read_b64_tr_b16 v[134:135], v160 offset:27648\n"
"v_mfma_f32_16x16x32_bf16 v[116:119], v[140:143], v[196:199], v[116:119]\n"
"buffer_load_dwordx4 v172, s[36:39], s66 offen offset:3072 lds\n"
"v_mul_f32 v45, v13, v45\n"
"v_mfma_f32_16x16x32_bf16 v[120:123], v[140:143], v[200:203], v[120:123]\n"
"v_exp_f32_e64 v149, -v149\n"
"v_mfma_f32_16x16x32_bf16 v[124:127], v[140:143], v[204:207], v[124:127]\n"
"v_add_f32 v148, 1.0, v148\n"
"s_waitcnt lgkmcnt(2)\n"
"v_mfma_f32_16x16x32_bf16 v[64:67], v[144:147], v[208:211], v[64:67]\n"
"ds_read_b64_tr_b16 v[136:137], v161 offset:19456\n"
"ds_read_b64_tr_b16 v[138:139], v161 offset:27648\n"
"v_mfma_f32_16x16x32_bf16 v[68:71], v[144:147], v[212:215], v[68:71]\n"
"v_add_f32 v149, 1.0, v149\n"
"v_fma_f32 v150, v14, v14, s75\n"
"v_mfma_f32_16x16x32_bf16 v[72:75], v[144:147], v[216:219], v[72:75]\n"
"v_rcp_f32 v148, v148\n"
"v_mfma_f32_16x16x32_bf16 v[76:79], v[144:147], v[220:223], v[76:79]\n"
"v_rcp_f32 v149, v149\n"
"v_mfma_f32_16x16x32_bf16 v[80:83], v[128:131], v[208:211], v[80:83]\n"
"s_waitcnt vmcnt(14)\n"
"s_barrier\n"
"s_lshl_b32 s19, s63, 8\n"
"s_add_u32 s19, s19, 4096\n"
"buffer_load_dword v178, off, s[48:51], s19 sc1\n"
"v_mfma_f32_16x16x32_bf16 v[84:87], v[128:131], v[212:215], v[84:87]\n"
"ds_read_b64_tr_b16 v[140:141], v160 offset:32768\n"
"ds_read_b64_tr_b16 v[142:143], v160 offset:40960\n"
"v_mfma_f32_16x16x32_bf16 v[88:91], v[128:131], v[216:219], v[88:91]\n"
"s_add_u32 m0, s65, 0\n"
"s_add_u32 s66, s70, 2359680\n"
"buffer_load_dwordx4 v169, s[28:31], s66 offen lds\n"
"v_mfma_f32_16x16x32_bf16 v[92:95], v[128:131], v[220:223], v[92:95]\n"
"v_mul_f32 v44, v44, v148\n"
"s_waitcnt lgkmcnt(2)\n"
"v_mfma_f32_16x16x32_bf16 v[96:99], v[132:135], v[208:211], v[96:99]\n"
"ds_read_b64_tr_b16 v[144:145], v161 offset:32768\n"
"ds_read_b64_tr_b16 v[146:147], v161 offset:40960\n"
"v_mfma_f32_16x16x32_bf16 v[100:103], v[132:135], v[212:215], v[100:103]\n"
"v_mul_f32 v45, v45, v149\n"
"v_cvt_pk_bf16_f32 v152, v44, v45\n"
"v_mfma_f32_16x16x32_bf16 v[104:107], v[132:135], v[216:219], v[104:107]\n"
"v_fma_f32 v151, v15, v15, s75\n"
"v_mul_f32 v150, v150, v14\n"
"v_mfma_f32_16x16x32_bf16 v[108:111], v[132:135], v[220:223], v[108:111]\n"
"v_mul_f32 v151, v151, v15\n"
"v_mul_f32 v46, v14, v46\n"
"v_mfma_f32_16x16x32_bf16 v[112:115], v[136:139], v[208:211], v[112:115]\n"
"ds_read_b64_tr_b16 v[128:129], v160 offset:33792\n"
"ds_read_b64_tr_b16 v[130:131], v160 offset:41984\n"
"v_mfma_f32_16x16x32_bf16 v[116:119], v[136:139], v[212:215], v[116:119]\n"
"buffer_load_dwordx4 v170, s[36:39], s66 offen offset:1024 lds\n"
"v_mul_f32 v47, v15, v47\n"
"v_mfma_f32_16x16x32_bf16 v[120:123], v[136:139], v[216:219], v[120:123]\n"
"v_exp_f32_e64 v150, -v150\n"
"v_mfma_f32_16x16x32_bf16 v[124:127], v[136:139], v[220:223], v[124:127]\n"
"v_add_f32 v150, 1.0, v150\n"
"v_add_u32 v173, 3072, v191\n"
"v_add_u32 v174, 2048, v191\n"
"v_add_u32 v175, 1024, v191\n"
"v_add_u32 v176, 0, v191\n"
"s_add_u32 s72, s72, s8\n"
"s_add_u32 s24, s24, s8\n"
"s_add_u32 s73, s73, s8\n"
"s_add_u32 s25, s25, s8\n"
"s_add_u32 s74, s74, s8\n"
"s_add_u32 s26, s26, s8\n"
"v_exp_f32_e64 v151, -v151\n"
"s_nop 0\n"
"v_add_f32 v151, 1.0, v151\n"
"v_rcp_f32 v150, v150\n"
"v_rcp_f32 v151, v151\n"
"v_mul_f32 v46, v46, v150\n"
"v_mul_f32 v47, v47, v151\n"
"v_cvt_pk_bf16_f32 v153, v46, v47\n"
"v_fma_f32 v148, v28, v28, s75\n"
"v_fma_f32 v149, v29, v29, s75\n"
"v_mul_f32 v148, v148, v28\n"
"v_mul_f32 v149, v149, v29\n"
"v_exp_f32_e64 v148, -v148\n"
"v_exp_f32_e64 v149, -v149\n"
"v_mul_f32 v60, v28, v60\n"
"v_mul_f32 v61, v29, v61\n"
"v_add_f32 v148, 1.0, v148\n"
"v_add_f32 v149, 1.0, v149\n"
"v_rcp_f32 v148, v148\n"
"v_rcp_f32 v149, v149\n"
"v_mul_f32 v60, v60, v148\n"
"v_mul_f32 v61, v61, v149\n"
"v_cvt_pk_bf16_f32 v154, v60, v61\n"
"v_fma_f32 v150, v30, v30, s75\n"
"v_fma_f32 v151, v31, v31, s75\n"
"v_mul_f32 v150, v150, v30\n"
"v_mul_f32 v151, v151, v31\n"
"v_exp_f32_e64 v150, -v150\n"
"v_exp_f32_e64 v151, -v151\n"
"v_mul_f32 v62, v30, v62\n"
"v_mul_f32 v63, v31, v63\n"
"v_add_f32 v150, 1.0, v150\n"
"v_add_f32 v151, 1.0, v151\n"
"v_rcp_f32 v150, v150\n"
"v_rcp_f32 v151, v151\n"
"v_mul_f32 v62, v62, v150\n"
"v_mul_f32 v63, v63, v151\n"
"v_cvt_pk_bf16_f32 v155, v62, v63\n"
"ds_write_b128 v156, v[152:155] offset:3072\n"
"buffer_store_dwordx4 v[152:155], v158, s[48:51], s17 offen offset:3072 sc1\n"
"s_waitcnt lgkmcnt(3)\n"
"v_mfma_f32_16x16x32_bf16 v[0:3], v[140:143], a[0:3], 0\n"
"ds_read_b64_tr_b16 v[132:133], v161 offset:33792\n"
"ds_read_b64_tr_b16 v[134:135], v161 offset:41984\n"
"v_mfma_f32_16x16x32_bf16 v[4:7], v[140:143], a[4:7], 0\n"
"v_mfma_f32_16x16x32_bf16 v[8:11], v[140:143], a[8:11], 0\n"
"v_mfma_f32_16x16x32_bf16 v[12:15], v[140:143], a[12:15], 0\n"
"v_mfma_f32_16x16x32_bf16 v[16:19], v[144:147], a[0:3], 0\n"
"ds_read_b64_tr_b16 v[136:137], v160 offset:34816\n"
"ds_read_b64_tr_b16 v[138:139], v160 offset:43008\n"
"v_mfma_f32_16x16x32_bf16 v[20:23], v[144:147], a[4:7], 0\n"
"buffer_load_dwordx4 v171, s[28:31], s66 offen offset:2048 lds\n"
"v_add_u32 v156, 98304, v177\n"
"v_mfma_f32_16x16x32_bf16 v[24:27], v[144:147], a[8:11], 0\n"
"s_lshl_b32 s17, s62, 13\n"
"s_add_u32 s17, s17, s27\n"
"s_add_u32 s17, s17, 32768\n"
"v_mfma_f32_16x16x32_bf16 v[28:31], v[144:147], a[12:15], 0\n"
"v_fma_f32 v148, v64, v64, s75\n"
"s_waitcnt lgkmcnt(2)\n"
"v_mfma_f32_16x16x32_bf16 v[32:35], v[128:131], a[0:3], 0\n"
"ds_read_b64_tr_b16 v[140:141], v161 offset:34816\n"
"ds_read_b64_tr_b16 v[142:143], v161 offset:43008\n"
"v_mfma_f32_16x16x32_bf16 v[36:39], v[128:131], a[4:7], 0\n"
"v_fma_f32 v149, v65, v65, s75\n"
"v_mul_f32 v148, v148, v64\n"
"v_mfma_f32_16x16x32_bf16 v[40:43], v[128:131], a[8:11], 0\n"
"v_mul_f32 v149, v149, v65\n"
"v_mul_f32 v96, v64, v96\n"
"v_mfma_f32_16x16x32_bf16 v[44:47], v[128:131], a[12:15], 0\n"
"v_exp_f32_e64 v148, -v148\n"
"v_mfma_f32_16x16x32_bf16 v[48:51], v[132:135], a[0:3], 0\n"
"ds_read_b64_tr_b16 v[144:145], v160 offset:35840\n"
"ds_read_b64_tr_b16 v[146:147], v160 offset:44032\n"
"v_mfma_f32_16x16x32_bf16 v[52:55], v[132:135], a[4:7], 0\n"
"buffer_load_dwordx4 v172, s[36:39], s66 offen offset:3072 lds\n"
"v_mul_f32 v97, v65, v97\n"
"v_mfma_f32_16x16x32_bf16 v[56:59], v[132:135], a[8:11], 0\n"
"v_exp_f32_e64 v149, -v149\n"
"v_mfma_f32_16x16x32_bf16 v[60:63], v[132:135], a[12:15], 0\n"
"v_add_f32 v148, 1.0, v148\n"
"s_waitcnt lgkmcnt(2)\n"
"v_mfma_f32_16x16x32_bf16 v[0:3], v[136:139], a[16:19], v[0:3]\n"
"ds_read_b64_tr_b16 v[128:129], v161 offset:35840\n"
"ds_read_b64_tr_b16 v[130:131], v161 offset:44032\n"
"v_mfma_f32_16x16x32_bf16 v[4:7], v[136:139], a[20:23], v[4:7]\n"
"v_add_f32 v149, 1.0, v149\n"
"v_fma_f32 v150, v66, v66, s75\n"
"v_mfma_f32_16x16x32_bf16 v[8:11], v[136:139], a[24:27], v[8:11]\n"
"v_rcp_f32 v148, v148\n"
"v_mfma_f32_16x16x32_bf16 v[12:15], v[136:139], a[28:31], v[12:15]\n"
"v_rcp_f32 v149, v149\n"
"v_mfma_f32_16x16x32_bf16 v[16:19], v[140:143], a[16:19], v[16:19]\n"
"s_waitcnt vmcnt(15)\n"
"s_barrier\n"
"v_mfma_f32_16x16x32_bf16 v[20:23], v[140:143], a[20:23], v[20:23]\n"
"ds_read_b64_tr_b16 v[132:133], v160 offset:49152\n"
"ds_read_b64_tr_b16 v[134:135], v160 offset:57344\n"
"v_mfma_f32_16x16x32_bf16 v[24:27], v[140:143], a[24:27], v[24:27]\n"
"s_add_u32 m0, s65, 16384\n"
"s_add_u32 s66, s70, 2949504\n"
"buffer_load_dwordx4 v169, s[28:31], s66 offen lds\n"
"v_mfma_f32_16x16x32_bf16 v[28:31], v[140:143], a[28:31], v[28:31]\n"
"v_mul_f32 v96, v96, v148\n"
"s_waitcnt lgkmcnt(2)\n"
"v_mfma_f32_16x16x32_bf16 v[32:35], v[144:147], a[16:19], v[32:35]\n"
"ds_read_b64_tr_b16 v[136:137], v161 offset:49152\n"
"ds_read_b64_tr_b16 v[138:139], v161 offset:57344\n"
"v_mfma_f32_16x16x32_bf16 v[36:39], v[144:147], a[20:23], v[36:39]\n"
"v_mul_f32 v97, v97, v149\n"
"v_cvt_pk_bf16_f32 v152, v96, v97\n"
"v_mfma_f32_16x16x32_bf16 v[40:43], v[144:147], a[24:27], v[40:43]\n"
"v_fma_f32 v151, v67, v67, s75\n"
"v_mul_f32 v150, v150, v66\n"
"v_mfma_f32_16x16x32_bf16 v[44:47], v[144:147], a[28:31], v[44:47]\n"
"v_mul_f32 v151, v151, v67\n"
"v_mul_f32 v98, v66, v98\n"
"v_mfma_f32_16x16x32_bf16 v[48:51], v[128:131], a[16:19], v[48:51]\n"
"ds_read_b64_tr_b16 v[140:141], v160 offset:50176\n"
"ds_read_b64_tr_b16 v[142:143], v160 offset:58368\n"
"v_mfma_f32_16x16x32_bf16 v[52:55], v[128:131], a[20:23], v[52:55]\n"
"buffer_load_dwordx4 v170, s[36:39], s66 offen offset:1024 lds\n"
"v_mul_f32 v99, v67, v99\n"
"v_mfma_f32_16x16x32_bf16 v[56:59], v[128:131], a[24:27], v[56:59]\n"
"v_exp_f32_e64 v150, -v150\n"
"v_mfma_f32_16x16x32_bf16 v[60:63], v[128:131], a[28:31], v[60:63]\n"
"v_add_f32 v150, 1.0, v150\n"
"s_waitcnt lgkmcnt(2)\n"
"v_mfma_f32_16x16x32_bf16 v[0:3], v[132:135], a[32:35], v[0:3]\n"
"ds_read_b64_tr_b16 v[144:145], v161 offset:50176\n"
"ds_read_b64_tr_b16 v[146:147], v161 offset:58368\n"
"v_mfma_f32_16x16x32_bf16 v[4:7], v[132:135], a[36:39], v[4:7]\n"
"v_exp_f32_e64 v151, -v151\n"
"v_mfma_f32_16x16x32_bf16 v[8:11], v[132:135], a[40:43], v[8:11]\n"
"v_add_f32 v151, 1.0, v151\n"
"v_fma_f32 v148, v80, v80, s75\n"
"v_mfma_f32_16x16x32_bf16 v[12:15], v[132:135], a[44:47], v[12:15]\n"
"v_rcp_f32 v150, v150\n"
"v_mfma_f32_16x16x32_bf16 v[16:19], v[136:139], a[32:35], v[16:19]\n"
"ds_read_b64_tr_b16 v[128:129], v160 offset:51200\n"
"ds_read_b64_tr_b16 v[130:131], v160 offset:59392\n"
"v_mfma_f32_16x16x32_bf16 v[20:23], v[136:139], a[36:39], v[20:23]\n"
"buffer_load_dwordx4 v171, s[28:31], s66 offen offset:2048 lds\n"
"v_mul_f32 v98, v98, v150\n"
"v_mfma_f32_16x16x32_bf16 v[24:27], v[136:139], a[40:43], v[24:27]\n"
"v_rcp_f32 v151, v151\n"
"v_mfma_f32_16x16x32_bf16 v[28:31], v[136:139], a[44:47], v[28:31]\n"
"v_mul_f32 v99, v99, v151\n"
"s_waitcnt lgkmcnt(2)\n"
"v_mfma_f32_16x16x32_bf16 v[32:35], v[140:143], a[32:35], v[32:35]\n"
"ds_read_b64_tr_b16 v[132:133], v161 offset:51200\n"
"ds_read_b64_tr_b16 v[134:135], v161 offset:59392\n"
"v_mfma_f32_16x16x32_bf16 v[36:39], v[140:143], a[36:39], v[36:39]\n"
"v_cvt_pk_bf16_f32 v153, v98, v99\n"
"v_fma_f32 v149, v81, v81, s75\n"
"v_mfma_f32_16x16x32_bf16 v[40:43], v[140:143], a[40:43], v[40:43]\n"
"v_mul_f32 v148, v148, v80\n"
"v_mul_f32 v149, v149, v81\n"
"v_mfma_f32_16x16x32_bf16 v[44:47], v[140:143], a[44:47], v[44:47]\n"
"v_exp_f32_e64 v148, -v148\n"
"v_mfma_f32_16x16x32_bf16 v[48:51], v[144:147], a[32:35], v[48:51]\n"
"ds_read_b64_tr_b16 v[136:137], v160 offset:52224\n"
"ds_read_b64_tr_b16 v[138:139], v160 offset:60416\n"
"v_mfma_f32_16x16x32_bf16 v[52:55], v[144:147], a[36:39], v[52:55]\n"
"buffer_load_dwordx4 v172, s[36:39], s66 offen offset:3072 lds\n"
"v_mul_f32 v112, v80, v112\n"
"v_mfma_f32_16x16x32_bf16 v[56:59], v[144:147], a[40:43], v[56:59]\n"
"v_exp_f32_e64 v149, -v149\n"
"v_mfma_f32_16x16x32_bf16 v[60:63], v[144:147], a[44:47], v[60:63]\n"
"v_mul_f32 v113, v81, v113\n"
"s_waitcnt lgkmcnt(2)\n"
"v_mfma_f32_16x16x32_bf16 v[0:3], v[128:131], a[48:51], v[0:3]\n"
"ds_read_b64_tr_b16 v[140:141], v161 offset:52224\n"
"ds_read_b64_tr_b16 v[142:143], v161 offset:60416\n"
"v_mfma_f32_16x16x32_bf16 v[4:7], v[128:131], a[52:55], v[4:7]\n"
"s_waitcnt vmcnt(9)\n"
"v_readfirstlane_b32 s18, v178\n"
"s_nop 1\n"
"s_cmp_ge_u32 s18, 16\n"
"s_cbranch_scc0 Lslow3_%=\n"
"Lback3_%=:\n"
"v_mfma_f32_16x16x32_bf16 v[8:11], v[128:131], a[56:59], v[8:11]\n"
"v_lshrrev_b32 v190, 1, v158\n"
"s_lshl_b32 s18, s63, 13\n"
"s_add_u32 s18, s18, 8912896\n"
"buffer_load_dwordx4 v[240:243], v158, s[48:51], s18 offen sc1\n"
"buffer_load_dwordx4 v[244:247], v158, s[48:51], s18 offen offset:1024 sc1\n"
"buffer_load_dwordx4 v[248:251], v158, s[48:51], s18 offen offset:2048 sc1\n"
"buffer_load_dwordx4 v[230:233], v158, s[48:51], s18 offen offset:3072 sc1\n"
"s_add_u32 s18, s18, 4096\n"
"buffer_load_dwordx2 v[228:229], v190, s[48:51], s18 offen sc1\n"
"buffer_load_dwordx2 v[234:235], v190, s[48:51], s18 offen offset:512 sc1\n"
"v_mov_b32 v226, 0\n"
"v_mov_b32 v227, 0\n"
"v_mfma_f32_16x16x32_bf16 v[12:15], v[128:131], a[60:63], v[12:15]\n"
"v_add_f32 v148, 1.0, v148\n"
"v_add_f32 v149, 1.0, v149\n"
"v_mfma_f32_16x16x32_bf16 v[16:19], v[132:135], a[48:51], v[16:19]\n"
"s_barrier\n"
"v_mfma_f32_16x16x32_bf16 v[20:23], v[132:135], a[52:55], v[20:23]\n"
"ds_read_b64_tr_b16 v[144:145], v162\n"
"ds_read_b64_tr_b16 v[146:147], v162 offset:8192\n"
"v_mfma_f32_16x16x32_bf16 v[24:27], v[132:135], a[56:59], v[24:27]\n"
"s_add_u32 m0, s65, 32768\n"
"s_add_u32 s66, s70, 3539328\n"
"buffer_load_dwordx4 v169, s[28:31], s66 offen lds\n"
"v_mfma_f32_16x16x32_bf16 v[28:31], v[132:135], a[60:63], v[28:31]\n"
"v_fma_f32 v150, v82, v82, s75\n"
"s_waitcnt lgkmcnt(2)\n"
"v_mfma_f32_16x16x32_bf16 v[32:35], v[136:139], a[48:51], v[32:35]\n"
"ds_read_b64_tr_b16 v[128:129], v163\n"
"ds_read_b64_tr_b16 v[130:131], v163 offset:8192\n"
"v_mfma_f32_16x16x32_bf16 v[36:39], v[136:139], a[52:55], v[36:39]\n"
"v_rcp_f32 v148, v148\n"
"v_mfma_f32_16x16x32_bf16 v[40:43], v[136:139], a[56:59], v[40:43]\n"
"v_rcp_f32 v149, v149\n"
"v_mfma_f32_16x16x32_bf16 v[44:47], v[136:139], a[60:63], v[44:47]\n"
"v_mul_f32 v112, v112, v148\n"
"v_mul_f32 v113, v113, v149\n"
"v_mfma_f32_16x16x32_bf16 v[48:51], v[140:143], a[48:51], v[48:51]\n"
"ds_read_b64_tr_b16 v[132:133], v162 offset:1024\n"
"ds_read_b64_tr_b16 v[134:135], v162 offset:9216\n"
"v_mfma_f32_16x16x32_bf16 v[52:55], v[140:143], a[52:55], v[52:55]\n"
"buffer_load_dwordx4 v170, s[36:39], s66 offen offset:1024 lds\n"
"v_cvt_pk_bf16_f32 v154, v112, v113\n"
"v_mfma_f32_16x16x32_bf16 v[56:59], v[140:143], a[56:59], v[56:59]\n"
"v_fma_f32 v151, v83, v83, s75\n"
"v_mul_f32 v150, v150, v82\n"
"v_mfma_f32_16x16x32_bf16 v[60:63], v[140:143], a[60:63], v[60:63]\n"
"v_mul_f32 v151, v151, v83\n"
"s_waitcnt lgkmcnt(2)\n"
"v_mfma_f32_16x16x32_bf16 v[0:3], v[144:147], a[64:67], v[0:3]\n"
"ds_read_b64_tr_b16 v[136:137], v163 offset:1024\n"
"ds_read_b64_tr_b16 v[138:139], v163 offset:9216\n"
"v_mfma_f32_16x16x32_bf16 v[4:7], v[144:147], a[68:71], v[4:7]\n"
"v_exp_f32_e64 v150, -v150\n"
"v_mfma_f32_16x16x32_bf16 v[8:11], v[144:147], a[72:75], v[8:11]\n"
"v_exp_f32_e64 v151, -v151\n"
"v_mfma_f32_16x16x32_bf16 v[12:15], v[144:147], a[76:79], v[12:15]\n"
"v_mul_f32 v114, v82, v114\n"
"v_mul_f32 v115, v83, v115\n"
"v_mfma_f32_16x16x32_bf16 v[16:19], v[128:131], a[64:67], v[16:19]\n"
"ds_read_b64_tr_b16 v[140:141], v162 offset:2048\n"
"ds_read_b64_tr_b16 v[142:143], v162 offset:10240\n"
"v_mfma_f32_16x16x32_bf16 v[20:23], v[128:131], a[68:71], v[20:23]\n"
"buffer_load_dwordx4 v171, s[28:31], s66 offen offset:2048 lds\n"
"v_add_f32 v150, 1.0, v150\n"
"v_mfma_f32_16x16x32_bf16 v[24:27], v[128:131], a[72:75], v[24:27]\n"
"v_add_f32 v151, 1.0, v151\n"
"v_mfma_f32_16x16x32_bf16 v[28:31], v[128:131], a[76:79], v[28:31]\n"
"s_waitcnt lgkmcnt(2)\n"
"v_mfma_f32_16x16x32_bf16 v[32:35], v[132:135], a[64:67], v[32:35]\n"
"ds_read_b64_tr_b16 v[144:145], v163 offset:2048\n"
"ds_read_b64_tr_b16 v[146:147], v163 offset:10240\n"
"v_mfma_f32_16x16x32_bf16 v[36:39], v[132:135], a[68:71], v[36:39]\n"
"v_rcp_f32 v150, v150\n"
"v_mfma_f32_16x16x32_bf16 v[40:43], v[132:135], a[72:75], v[40:43]\n"
"v_rcp_f32 v151, v151\n"
"v_mfma_f32_16x16x32_bf16 v[44:47], v[132:135], a[76:79], v[44:47]\n"
"v_mul_f32 v114, v114, v150\n"
"v_mul_f32 v115, v115, v151\n"
"v_mfma_f32_16x16x32_bf16 v[48:51], v[136:139], a[64:67], v[48:51]\n"
"ds_read_b64_tr_b16 v[128:129], v162 offset:3072\n"
"ds_read_b64_tr_b16 v[130:131], v162 offset:11264\n"
"v_mfma_f32_16x16x32_bf16 v[52:55], v[136:139], a[68:71], v[52:55]\n"
"buffer_load_dwordx4 v172, s[36:39], s66 offen offset:3072 lds\n"
"v_cvt_pk_bf16_f32 v155, v114, v115\n"
"v_mfma_f32_16x16x32_bf16 v[56:59], v[136:139], a[72:75], v[56:59]\n"
"ds_write_b128 v156, v[152:155]\n"
"buffer_store_dwordx4 v[152:155], v158, s[48:51], s17 offen sc1\n"
"v_mfma_f32_16x16x32_bf16 v[60:63], v[136:139], a[76:79], v[60:63]\n"
"v_fma_f32 v148, v68, v68, s75\n"
"s_waitcnt lgkmcnt(3)\n"
"v_mfma_f32_16x16x32_bf16 v[0:3], v[140:143], a[80:83], v[0:3]\n"
"ds_read_b64_tr_b16 v[132:133], v163 offset:3072\n"
"ds_read_b64_tr_b16 v[134:135], v163 offset:11264\n"
"v_mfma_f32_16x16x32_bf16 v[4:7], v[140:143], a[84:87], v[4:7]\n"
"v_fma_f32 v149, v69, v69, s75\n"
"v_mul_f32 v148, v148, v68\n"
"v_mfma_f32_16x16x32_bf16 v[8:11], v[140:143], a[88:91], v[8:11]\n"
"v_mul_f32 v149, v149, v69\n"
"v_mul_f32 v100, v68, v100\n"
"v_mfma_f32_16x16x32_bf16 v[12:15], v[140:143], a[92:95], v[12:15]\n"
"v_exp_f32_e64 v148, -v148\n"
"v_mfma_f32_16x16x32_bf16 v[16:19], v[144:147], a[80:83], v[16:19]\n"
"s_barrier\n"
"v_mfma_f32_16x16x32_bf16 v[20:23], v[144:147], a[84:87], v[20:23]\n"
"ds_read_b64_tr_b16 v[136:137], v162 offset:16384\n"
"ds_read_b64_tr_b16 v[138:139], v162 offset:24576\n"
"v_mfma_f32_16x16x32_bf16 v[24:27], v[144:147], a[88:91], v[24:27]\n"
"s_add_u32 m0, s65, 49152\n"
"s_add_u32 s66, s70, 4129152\n"
"buffer_load_dwordx4 v169, s[28:31], s66 offen lds\n"
"v_mfma_f32_16x16x32_bf16 v[28:31], v[144:147], a[92:95], v[28:31]\n"
"v_mul_f32 v101, v69, v101\n"
"s_waitcnt lgkmcnt(2)\n"
"v_mfma_f32_16x16x32_bf16 v[32:35], v[128:131], a[80:83], v[32:35]\n"
"ds_read_b64_tr_b16 v[140:141], v163 offset:16384\n"
"ds_read_b64_tr_b16 v[142:143], v163 offset:24576\n"
"v_mfma_f32_16x16x32_bf16 v[36:39], v[128:131], a[84:87], v[36:39]\n"
"v_exp_f32_e64 v149, -v149\n"
"v_mfma_f32_16x16x32_bf16 v[40:43], v[128:131], a[88:91], v[40:43]\n"
"v_add_f32 v148, 1.0, v148\n"
"v_add_f32 v149, 1.0, v149\n"
"v_mfma_f32_16x16x32_bf16 v[44:47], v[128:131], a[92:95], v[44:47]\n"
"v_rcp_f32 v148, v148\n"
"v_mfma_f32_16x16x32_bf16 v[48:51], v[132:135], a[80:83], v[48:51]\n"
"ds_read_b64_tr_b16 v[144:145], v162 offset:17408\n"
"ds_read_b64_tr_b16 v[146:147], v162 offset:25600\n"
"v_mfma_f32_16x16x32_bf16 v[52:55], v[132:135], a[84:87], v[52:55]\n"
"buffer_load_dwordx4 v170, s[36:39], s66 offen offset:1024 lds\n"
"v_mul_f32 v100, v100, v148\n"
"v_mfma_f32_16x16x32_bf16 v[56:59], v[132:135], a[88:91], v[56:59]\n"
"v_rcp_f32 v149, v149\n"
"v_mfma_f32_16x16x32_bf16 v[60:63], v[132:135], a[92:95], v[60:63]\n"
"v_mul_f32 v101, v101, v149\n"
"s_waitcnt lgkmcnt(2)\n"
"v_mfma_f32_16x16x32_bf16 v[0:3], v[136:139], a[96:99], v[0:3]\n"
"ds_read_b64_tr_b16 v[128:129], v163 offset:17408\n"
"ds_read_b64_tr_b16 v[130:131], v163 offset:25600\n"
"v_mfma_f32_16x16x32_bf16 v[4:7], v[136:139], a[100:103], v[4:7]\n"
"s_waitcnt vmcnt(7)\n"
"v_cvt_pk_bf16_f32 v152, v100, v101\n"
"v_mfma_f32_16x16x32_bf16 v[8:11], v[136:139], a[104:107], v[8:11]\n"
"v_lshrrev_b32 v181, 4, v158\n"
"v_lshrrev_b32 v190, 2, v181\n"
"v_lshlrev_b32 v190, 5, v190\n"
"v_and_b32 v181, 3, v181\n"
"v_lshl_add_u32 v190, v181, 3, v190\n"
"s_lshl_b32 s18, s60, 12\n"
"s_add_u32 s18, s18, 147456\n"
"s_add_u32 s18, s18, %[lds]\n"
"v_add_u32 v190, s18, v190\n"
"v_mfma_f32_16x16x32_bf16 v[12:15], v[136:139], a[108:111], v[12:15]\n"
"ds_read_b64_tr_b16 v[236:237], v190\n"
"ds_read_b64_tr_b16 v[238:239], v190 offset:512\n"
"v_mfma_f32_16x16x32_bf16 v[16:19], v[140:143], a[96:99], v[16:19]\n"
"ds_read_b64_tr_b16 v[132:133], v162 offset:18432\n"
"ds_read_b64_tr_b16 v[134:135], v162 offset:26624\n"
"v_mfma_f32_16x16x32_bf16 v[20:23], v[140:143], a[100:103], v[20:23]\n"
"buffer_load_dwordx4 v171, s[28:31], s66 offen offset:2048 lds\n"
"ds_read_b64_tr_b16 v[224:225], v190 offset:1024\n"
"v_mfma_f32_16x16x32_bf16 v[24:27], v[140:143], a[104:107], v[24:27]\n"
"s_waitcnt lgkmcnt(0)\n"
"v_fma_f32 v150, v70, v70, s75\n"
"v_mfma_f32_16x16x32_bf16 v[28:31], v[140:143], a[108:111], v[28:31]\n"
"v_fma_f32 v151, v71, v71, s75\n"
"v_mfma_f32_16x16x32_bf16 v[32:35], v[144:147], a[96:99], v[32:35]\n"
"ds_read_b64_tr_b16 v[136:137], v163 offset:18432\n"
"ds_read_b64_tr_b16 v[138:139], v163 offset:26624\n"
"v_mfma_f32_16x16x32_bf16 v[36:39], v[144:147], a[100:103], v[36:39]\n"
"v_mfma_f32_16x16x32_bf16 v[252:255], v[248:251], v[236:239], 0\n"
"v_mfma_f32_16x16x32_bf16 v[40:43], v[144:147], a[104:107], v[40:43]\n"
"v_mfma_f32_16x16x32_bf16 v[182:185], v[240:243], v[236:239], 0\n"
"v_mfma_f32_16x16x32_bf16 v[44:47], v[144:147], a[108:111], v[44:47]\n"
"v_mfma_f32_16x16x32_bf16 v[186:189], v[244:247], v[236:239], 0\n"
"v_mfma_f32_16x16x32_bf16 v[48:51], v[128:131], a[96:99], v[48:51]\n"
"ds_read_b64_tr_b16 v[140:141], v162 offset:19456\n"
"ds_read_b64_tr_b16 v[142:143], v162 offset:27648\n"
"v_mfma_f32_16x16x32_bf16 v[52:55], v[128:131], a[100:103], v[52:55]\n"
"buffer_load_dwordx4 v172, s[36:39], s66 offen offset:3072 lds\n"
"v_mul_f32 v150, v150, v70\n"
"v_mfma_f32_16x16x32_bf16 v[56:59], v[128:131], a[104:107], v[56:59]\n"
"v_mfma_f32_16x16x32_bf16 v[252:255], v[234:237], v[224:227], v[252:255]\n"
"v_mfma_f32_16x16x32_bf16 v[60:63], v[128:131], a[108:111], v[60:63]\n"
"v_mul_f32 v151, v151, v71\n"
"s_waitcnt lgkmcnt(2)\n"
"v_mfma_f32_16x16x32_bf16 v[0:3], v[132:135], a[112:115], v[0:3]\n"
"ds_read_b64_tr_b16 v[144:145], v163 offset:19456\n"
"ds_read_b64_tr_b16 v[146:147], v163 offset:27648\n"
"v_mfma_f32_16x16x32_bf16 v[4:7], v[132:135], a[116:119], v[4:7]\n"
"v_mfma_f32_16x16x32_bf16 v[186:189], v[230:233], v[224:227], v[186:189]\n"
"v_mfma_f32_16x16x32_bf16 v[8:11], v[132:135], a[120:123], v[8:11]\n"
"v_add_f32 v181, s78, v252\n"
"v_add_f32 v190, s78, v253\n"
"v_mfma_f32_16x16x32_bf16 v[12:15], v[132:135], a[124:127], v[12:15]\n"
"v_rcp_f32 v181, v181\n"
"v_mfma_f32_16x16x32_bf16 v[16:19], v[136:139], a[112:115], v[16:19]\n"
"s_barrier\n"
"v_mfma_f32_16x16x32_bf16 v[20:23], v[136:139], a[116:119], v[20:23]\n"
"ds_read_b64_tr_b16 v[128:129], v160\n"
"ds_read_b64_tr_b16 v[130:131], v160 offset:8192\n"
"v_mfma_f32_16x16x32_bf16 v[24:27], v[136:139], a[120:123], v[24:27]\n"
"s_add_u32 m0, s65, 65536\n"
"s_add_u32 s66, s70, 4718976\n"
"buffer_load_dwordx4 v169, s[28:31], s66 offen lds\n"
"v_mfma_f32_16x16x32_bf16 v[28:31], v[136:139], a[124:127], v[28:31]\n"
"v_mul_f32 v181, s5, v181\n"
"s_waitcnt lgkmcnt(2)\n"
"v_mfma_f32_16x16x32_bf16 v[32:35], v[140:143], a[112:115], v[32:35]\n"
"ds_read_b64_tr_b16 v[132:133], v161\n"
"ds_read_b64_tr_b16 v[134:135], v161 offset:8192\n"
"v_mfma_f32_16x16x32_bf16 v[36:39], v[140:143], a[116:119], v[36:39]\n"
"v_rcp_f32 v190, v190\n"
"v_mfma_f32_16x16x32_bf16 v[40:43], v[140:143], a[120:123], v[40:43]\n"
"v_mul_f32 v190, s5, v190\n"
"v_cndmask_b32_e64 v191, v190, v181, s[84:85]\n"
"v_mfma_f32_16x16x32_bf16 v[44:47], v[140:143], a[124:127], v[44:47]\n"
"v_mul_f32 v182, v182, v181\n"
"v_mul_f32 v183, v183, v181\n"
"v_mfma_f32_16x16x32_bf16 v[48:51], v[144:147], a[112:115], v[48:51]\n"
"ds_read_b64_tr_b16 v[136:137], v160 offset:1024\n"
"ds_read_b64_tr_b16 v[138:139], v160 offset:9216\n"
"v_mfma_f32_16x16x32_bf16 v[52:55], v[144:147], a[116:119], v[52:55]\n"
"buffer_load_dwordx4 v170, s[36:39], s66 offen offset:1024 lds\n"
"v_mul_f32 v184, v184, v181\n"
"v_mfma_f32_16x16x32_bf16 v[56:59], v[144:147], a[120:123], v[56:59]\n"
"v_mul_f32 v185, v185, v181\n"
"v_mul_f32 v186, v186, v191\n"
"v_mfma_f32_16x16x32_bf16 v[60:63], v[144:147], a[124:127], v[60:63]\n"
"v_mul_f32 v187, v187, v191\n"
"s_waitcnt lgkmcnt(2)\n"
"v_mfma_f32_16x16x32_bf16 v[0:3], v[128:131], a[128:131], v[0:3]\n"
"ds_read_b64_tr_b16 v[140:141], v161 offset:1024\n"
"ds_read_b64_tr_b16 v[142:143], v161 offset:9216\n"
"v_mfma_f32_16x16x32_bf16 v[4:7], v[128:131], a[132:135], v[4:7]\n"
"v_mul_f32 v188, v188, v190\n"
"v_mul_f32 v189, v189, v190\n"
"v_mfma_f32_16x16x32_bf16 v[8:11], v[128:131], a[136:139], v[8:11]\n"
"v_cvt_pk_bf16_f32 v252, v182, v183\n"
"v_cvt_pk_bf16_f32 v253, v184, v185\n"
"v_mfma_f32_16x16x32_bf16 v[12:15], v[128:131], a[140:143], v[12:15]\n"
"v_cvt_pk_bf16_f32 v254, v186, v187\n"
"v_cvt_pk_bf16_f32 v255, v188, v189\n"
"v_mfma_f32_16x16x32_bf16 v[16:19], v[132:135], a[128:131], v[16:19]\n"
"ds_read_b64_tr_b16 v[144:145], v160 offset:2048\n"
"ds_read_b64_tr_b16 v[146:147], v160 offset:10240\n"
"v_mfma_f32_16x16x32_bf16 v[20:23], v[132:135], a[132:135], v[20:23]\n"
"buffer_load_dwordx4 v171, s[28:31], s66 offen offset:2048 lds\n"
"v_mul_f32 v102, v70, v102\n"
"v_mfma_f32_16x16x32_bf16 v[24:27], v[132:135], a[136:139], v[24:27]\n"
"v_mfma_f32_16x16x32_bf16 v[182:185], v[226:229], v[236:239], 0\n"
"v_mfma_f32_16x16x32_bf16 v[28:31], v[132:135], a[140:143], v[28:31]\n"
"v_mul_f32 v103, v71, v103\n"
"s_waitcnt lgkmcnt(2)\n"
"v_mfma_f32_16x16x32_bf16 v[32:35], v[136:139], a[128:131], v[32:35]\n"
"ds_read_b64_tr_b16 v[128:129], v161 offset:2048\n"
"ds_read_b64_tr_b16 v[130:131], v161 offset:10240\n"
"v_mfma_f32_16x16x32_bf16 v[36:39], v[136:139], a[132:135], v[36:39]\n"
"v_mfma_f32_16x16x32_bf16 v[182:185], v[232:235], v[224:227], v[182:185]\n"
"v_mfma_f32_16x16x32_bf16 v[40:43], v[136:139], a[136:139], v[40:43]\n"
"s_lshl_b32 s18, s60, 12\n"
"s_add_u32 s18, s18, 147456\n"
"v_add_u32 v191, s18, v179\n"
"v_mfma_f32_16x16x32_bf16 v[44:47], v[136:139], a[140:143], v[44:47]\n"
"ds_write_b128 v191, v[252:255]\n"
"v_fma_f32 v148, v84, v84, s75\n"
"v_mfma_f32_16x16x32_bf16 v[48:51], v[140:143], a[128:131], v[48:51]\n"
"ds_read_b64_tr_b16 v[132:133], v160 offset:3072\n"
"ds_read_b64_tr_b16 v[134:135], v160 offset:11264\n"
"v_mfma_f32_16x16x32_bf16 v[52:55], v[140:143], a[132:135], v[52:55]\n"
"buffer_load_dwordx4 v172, s[36:39], s66 offen offset:3072 lds\n"
"v_fma_f32 v149, v85, v85, s75\n"
"v_mfma_f32_16x16x32_bf16 v[56:59], v[140:143], a[136:139], v[56:59]\n"
"s_lshl_b32 s18, s62, 11\n"
"s_add_u32 s18, s18, s27\n"
"s_add_u32 s18, s18, 73728\n"
"buffer_store_dwordx4 v[252:255], v158, s[48:51], s18 offen sc1\n"
"v_mfma_f32_16x16x32_bf16 v[60:63], v[140:143], a[140:143], v[60:63]\n"
"v_mul_f32 v182, v182, v190\n"
"s_waitcnt lgkmcnt(3)\n"
"v_mfma_f32_16x16x32_bf16 v[0:3], v[144:147], a[144:147], v[0:3]\n"
"ds_read_b64_tr_b16 v[136:137], v161 offset:3072\n"
"ds_read_b64_tr_b16 v[138:139], v161 offset:11264\n"
"v_mfma_f32_16x16x32_bf16 v[4:7], v[144:147], a[148:151], v[4:7]\n"
"v_mul_f32 v183, v183, v190\n"
"v_mul_f32 v184, v184, v190\n"
"v_mfma_f32_16x16x32_bf16 v[8:11], v[144:147], a[152:155], v[8:11]\n"
"v_mul_f32 v185, v185, v190\n"
"v_cvt_pk_bf16_f32 v186, v182, v183\n"
"v_mfma_f32_16x16x32_bf16 v[12:15], v[144:147], a[156:159], v[12:15]\n"
"v_cvt_pk_bf16_f32 v187, v184, v185\n"
"v_mov_b32 v188, 0\n"
"v_mfma_f32_16x16x32_bf16 v[16:19], v[128:131], a[144:147], v[16:19]\n"
"s_barrier\n"
"v_mfma_f32_16x16x32_bf16 v[20:23], v[128:131], a[148:151], v[20:23]\n"
"ds_read_b64_tr_b16 v[140:141], v160 offset:16384\n"
"ds_read_b64_tr_b16 v[142:143], v160 offset:24576\n"
"v_mfma_f32_16x16x32_bf16 v[24:27], v[128:131], a[152:155], v[24:27]\n"
"v_mov_b32 v189, 0\n"
"ds_write_b128 v191, v[186:189] offset:1024\n"
"v_mfma_f32_16x16x32_bf16 v[28:31], v[128:131], a[156:159], v[28:31]\n"
"v_mul_f32 v148, v148, v84\n"
"s_waitcnt lgkmcnt(3)\n"
"v_mfma_f32_16x16x32_bf16 v[32:35], v[132:135], a[144:147], v[32:35]\n"
"ds_read_b64_tr_b16 v[144:145], v161 offset:16384\n"
"ds_read_b64_tr_b16 v[146:147], v161 offset:24576\n"
"v_mfma_f32_16x16x32_bf16 v[36:39], v[132:135], a[148:151], v[36:39]\n"
"s_lshl_b32 s18, s62, 11\n"
"s_add_u32 s18, s18, s27\n"
"s_add_u32 s18, s18, 81920\n"
"buffer_store_dwordx4 v[186:189], v158, s[48:51], s18 offen sc1\n"
"v_mfma_f32_16x16x32_bf16 v[40:43], v[132:135], a[152:155], v[40:43]\n"
"v_lshrrev_b32 v181, 4, v158\n"
"v_lshrrev_b32 v190, 2, v181\n"
"v_lshlrev_b32 v190, 5, v190\n"
"v_and_b32 v181, 3, v181\n"
"v_lshl_add_u32 v190, v181, 3, v190\n"
"s_lshl_b32 s18, s60, 12\n"
"s_add_u32 s18, s18, 149504\n"
"s_add_u32 s18, s18, %[lds]\n"
"v_add_u32 v190, s18, v190\n"
"v_mfma_f32_16x16x32_bf16 v[44:47], v[132:135], a[156:159], v[44:47]\n"
"ds_read_b64_tr_b16 v[236:237], v190\n"
"ds_read_b64_tr_b16 v[238:239], v190 offset:512\n"
"v_mfma_f32_16x16x32_bf16 v[48:51], v[136:139], a[144:147], v[48:51]\n"
"ds_read_b64_tr_b16 v[128:129], v160 offset:17408\n"
"ds_read_b64_tr_b16 v[130:131], v160 offset:25600\n"
"v_mfma_f32_16x16x32_bf16 v[52:55], v[136:139], a[148:151], v[52:55]\n"
"ds_read_b64_tr_b16 v[224:225], v190 offset:1024\n"
"s_waitcnt lgkmcnt(0)\n"
"v_mfma_f32_16x16x32_bf16 v[56:59], v[136:139], a[152:155], v[56:59]\n"
"v_mfma_f32_16x16x32_bf16 v[252:255], v[248:251], v[236:239], 0\n"
"v_mfma_f32_16x16x32_bf16 v[60:63], v[136:139], a[156:159], v[60:63]\n"
"v_mul_f32 v149, v149, v85\n"
"v_mfma_f32_16x16x32_bf16 v[0:3], v[140:143], a[160:163], v[0:3]\n"
"ds_read_b64_tr_b16 v[132:133], v161 offset:17408\n"
"ds_read_b64_tr_b16 v[134:135], v161 offset:25600\n"
"v_mfma_f32_16x16x32_bf16 v[4:7], v[140:143], a[164:167], v[4:7]\n"
"v_mfma_f32_16x16x32_bf16 v[182:185], v[240:243], v[236:239], 0\n"
"v_mfma_f32_16x16x32_bf16 v[8:11], v[140:143], a[168:171], v[8:11]\n"
"v_mfma_f32_16x16x32_bf16 v[186:189], v[244:247], v[236:239], 0\n"
"v_mfma_f32_16x16x32_bf16 v[12:15], v[140:143], a[172:175], v[12:15]\n"
"v_mfma_f32_16x16x32_bf16 v[252:255], v[234:237], v[224:227], v[252:255]\n"
"v_mfma_f32_16x16x32_bf16 v[16:19], v[144:147], a[160:163], v[16:19]\n"
"ds_read_b64_tr_b16 v[136:137], v160 offset:18432\n"
"ds_read_b64_tr_b16 v[138:139], v160 offset:26624\n"
"v_mfma_f32_16x16x32_bf16 v[20:23], v[144:147], a[164:167], v[20:23]\n"
"v_mfma_f32_16x16x32_bf16 v[186:189], v[230:233], v[224:227], v[186:189]\n"
"v_mfma_f32_16x16x32_bf16 v[24:27], v[144:147], a[168:171], v[24:27]\n"
"s_nop 1\n"
"v_add_f32 v181, s78, v252\n"
"v_add_f32 v190, s78, v253\n"
"v_mfma_f32_16x16x32_bf16 v[28:31], v[144:147], a[172:175], v[28:31]\n"
"s_waitcnt lgkmcnt(2)\n"
"v_mfma_f32_16x16x32_bf16 v[32:35], v[128:131], a[160:163], v[32:35]\n"
"ds_read_b64_tr_b16 v[140:141], v161 offset:18432\n"
"ds_read_b64_tr_b16 v[142:143], v161 offset:26624\n"
"v_mfma_f32_16x16x32_bf16 v[36:39], v[128:131], a[164:167], v[36:39]\n"
"v_rcp_f32 v181, v181\n"
"v_mfma_f32_16x16x32_bf16 v[40:43], v[128:131], a[168:171], v[40:43]\n"
"v_rcp_f32 v190, v190\n"
"v_mfma_f32_16x16x32_bf16 v[44:47], v[128:131], a[172:175], v[44:47]\n"
"v_mul_f32 v181, s5, v181\n"
"v_mul_f32 v190, s5, v190\n"
"v_mfma_f32_16x16x32_bf16 v[48:51], v[132:135], a[160:163], v[48:51]\n"
"ds_read_b64_tr_b16 v[144:145], v160 offset:19456\n"
"ds_read_b64_tr_b16 v[146:147], v160 offset:27648\n"
"v_mfma_f32_16x16x32_bf16 v[52:55], v[132:135], a[164:167], v[52:55]\n"
"v_cndmask_b32_e64 v191, v190, v181, s[84:85]\n"
"v_mul_f32 v182, v182, v181\n"
"v_mfma_f32_16x16x32_bf16 v[56:59], v[132:135], a[168:171], v[56:59]\n"
"v_mul_f32 v183, v183, v181\n"
"v_mul_f32 v184, v184, v181\n"
"v_mfma_f32_16x16x32_bf16 v[60:63], v[132:135], a[172:175], v[60:63]\n"
"v_mul_f32 v185, v185, v181\n"
"s_waitcnt lgkmcnt(2)\n"
"v_mfma_f32_16x16x32_bf16 v[0:3], v[136:139], a[176:179], v[0:3]\n"
"ds_read_b64_tr_b16 v[128:129], v161 offset:19456\n"
"ds_read_b64_tr_b16 v[130:131], v161 offset:27648\n"
"v_mfma_f32_16x16x32_bf16 v[4:7], v[136:139], a[180:183], v[4:7]\n"
"v_mul_f32 v186, v186, v191\n"
"v_mul_f32 v187, v187, v191\n"
"v_mfma_f32_16x16x32_bf16 v[8:11], v[136:139], a[184:187], v[8:11]\n"
"v_mul_f32 v188, v188, v190\n"
"v_mul_f32 v189, v189, v190\n"
"v_mfma_f32_16x16x32_bf16 v[12:15], v[136:139], a[188:191], v[12:15]\n"
"v_cvt_pk_bf16_f32 v252, v182, v183\n"
"v_cvt_pk_bf16_f32 v253, v184, v185\n"
"v_mfma_f32_16x16x32_bf16 v[16:19], v[140:143], a[176:179], v[16:19]\n"
"s_waitcnt vmcnt(11)\n"
"s_barrier\n"
"v_mfma_f32_16x16x32_bf16 v[20:23], v[140:143], a[180:183], v[20:23]\n"
"ds_read_b64_tr_b16 v[132:133], v160 offset:32768\n"
"ds_read_b64_tr_b16 v[134:135], v160 offset:40960\n"
"v_mfma_f32_16x16x32_bf16 v[24:27], v[140:143], a[184:187], v[24:27]\n"
"s_add_u32 m0, s65, 0\n"
"s_add_u32 s66, s9, 3072\n"
"buffer_load_dwordx4 v166, s[20:23], s66 offen sc1 lds\n"
"v_mfma_f32_16x16x32_bf16 v[28:31], v[140:143], a[188:191], v[28:31]\n"
"v_cvt_pk_bf16_f32 v254, v186, v187\n"
"s_waitcnt lgkmcnt(2)\n"
"v_mfma_f32_16x16x32_bf16 v[32:35], v[144:147], a[176:179], v[32:35]\n"
"ds_read_b64_tr_b16 v[136:137], v161 offset:32768\n"
"ds_read_b64_tr_b16 v[138:139], v161 offset:40960\n"
"v_mfma_f32_16x16x32_bf16 v[36:39], v[144:147], a[180:183], v[36:39]\n"
"v_cvt_pk_bf16_f32 v255, v188, v189\n"
"v_mfma_f32_16x16x32_bf16 v[40:43], v[144:147], a[184:187], v[40:43]\n"
"v_mfma_f32_16x16x32_bf16 v[182:185], v[226:229], v[236:239], 0\n"
"v_mfma_f32_16x16x32_bf16 v[44:47], v[144:147], a[188:191], v[44:47]\n"
"v_mfma_f32_16x16x32_bf16 v[182:185], v[232:235], v[224:227], v[182:185]\n"
"v_mfma_f32_16x16x32_bf16 v[48:51], v[128:131], a[176:179], v[48:51]\n"
"ds_read_b64_tr_b16 v[140:141], v160 offset:33792\n"
"ds_read_b64_tr_b16 v[142:143], v160 offset:41984\n"
"v_mfma_f32_16x16x32_bf16 v[52:55], v[128:131], a[180:183], v[52:55]\n"
"s_add_u32 s66, s9, 6144\n"
"buffer_load_dwordx4 v166, s[20:23], s66 offen offset:1024 sc1 lds\n"
"v_mfma_f32_16x16x32_bf16 v[56:59], v[128:131], a[184:187], v[56:59]\n"
"s_lshl_b32 s18, s60, 12\n"
"s_add_u32 s18, s18, 149504\n"
"v_add_u32 v191, s18, v179\n"
"v_mfma_f32_16x16x32_bf16 v[60:63], v[128:131], a[188:191], v[60:63]\n"
"ds_write_b128 v191, v[252:255]\n"
"s_waitcnt lgkmcnt(3)\n"
"v_mfma_f32_16x16x32_bf16 v[0:3], v[132:135], a[192:195], v[0:3]\n"
"ds_read_b64_tr_b16 v[144:145], v161 offset:33792\n"
"ds_read_b64_tr_b16 v[146:147], v161 offset:41984\n"
"v_mfma_f32_16x16x32_bf16 v[4:7], v[132:135], a[196:199], v[4:7]\n"
"s_lshl_b32 s18, s62, 11\n"
"s_add_u32 s18, s18, s27\n"
"s_add_u32 s18, s18, 73728\n"
"buffer_store_dwordx4 v[252:255], v158, s[48:51], s18 offen offset:1024 sc1\n"
"v_mfma_f32_16x16x32_bf16 v[8:11], v[132:135], a[200:203], v[8:11]\n"
"v_mul_f32 v182, v182, v190\n"
"v_mul_f32 v183, v183, v190\n"
"v_mfma_f32_16x16x32_bf16 v[12:15], v[132:135], a[204:207], v[12:15]\n"
"v_mul_f32 v184, v184, v190\n"
"v_mul_f32 v185, v185, v190\n"
"v_mfma_f32_16x16x32_bf16 v[16:19], v[136:139], a[192:195], v[16:19]\n"
"ds_read_b64_tr_b16 v[128:129], v160 offset:34816\n"
"ds_read_b64_tr_b16 v[130:131], v160 offset:43008\n"
"v_mfma_f32_16x16x32_bf16 v[20:23], v[136:139], a[196:199], v[20:23]\n"
"s_add_u32 s66, s9, 9216\n"
"buffer_load_dwordx4 v166, s[20:23], s66 offen offset:2048 sc1 lds\n"
"v_mfma_f32_16x16x32_bf16 v[24:27], v[136:139], a[200:203], v[24:27]\n"
"v_cvt_pk_bf16_f32 v186, v182, v183\n"
"v_cvt_pk_bf16_f32 v187, v184, v185\n"
"v_mfma_f32_16x16x32_bf16 v[28:31], v[136:139], a[204:207], v[28:31]\n"
"v_mov_b32 v188, 0\n"
"s_waitcnt lgkmcnt(2)\n"
"v_mfma_f32_16x16x32_bf16 v[32:35], v[140:143], a[192:195], v[32:35]\n"
"ds_read_b64_tr_b16 v[132:133], v161 offset:34816\n"
"ds_read_b64_tr_b16 v[134:135], v161 offset:43008\n"
"v_mfma_f32_16x16x32_bf16 v[36:39], v[140:143], a[196:199], v[36:39]\n"
"v_mov_b32 v189, 0\n"
"ds_write_b128 v191, v[186:189] offset:1024\n"
"v_mfma_f32_16x16x32_bf16 v[40:43], v[140:143], a[200:203], v[40:43]\n"
"s_lshl_b32 s18, s62, 11\n"
"s_add_u32 s18, s18, s27\n"
"s_add_u32 s18, s18, 81920\n"
"buffer_store_dwordx4 v[186:189], v158, s[48:51], s18 offen offset:1024 sc1\n"
"v_mfma_f32_16x16x32_bf16 v[44:47], v[140:143], a[204:207], v[44:47]\n"
"s_lshr_b32 s18, %[bid], 3\n"
"s_lshl_b32 s18, s18, 2\n"
"s_add_u32 s18, s18, s60\n"
"s_mul_i32 s18, s18, 41\n"
"s_add_u32 s18, s18, 33\n"
"v_lshrrev_b32 v182, 4, v158\n"
"v_cmp_gt_u32 vcc, 11, v182\n"
"v_add_u32 v184, s18, v182\n"
"v_lshlrev_b32 v184, 7, v184\n"
"v_mov_b32 v185, 2147483647\n"
"v_cndmask_b32 v184, v185, v184, vcc\n"
"s_lshr_b32 s18, s63, 1\n"
"s_mul_i32 s18, s18, 663552\n"
"s_add_u32 s18, s18, 3072\n"
"buffer_load_dword v186, v184, s[40:43], s18 offen\n"
"v_mfma_f32_16x16x32_bf16 v[48:51], v[144:147], a[192:195], v[48:51]\n"
"ds_read_b64_tr_b16 v[136:137], v160 offset:35840\n"
"ds_read_b64_tr_b16 v[138:139], v160 offset:44032\n"
"v_mfma_f32_16x16x32_bf16 v[52:55], v[144:147], a[196:199], v[52:55]\n"
"s_add_u32 s66, s9, 12288\n"
"buffer_load_dwordx4 v166, s[20:23], s66 offen offset:3072 sc1 lds\n"
"v_mfma_f32_16x16x32_bf16 v[56:59], v[144:147], a[200:203], v[56:59]\n"
"s_lshr_b32 s18, %[bid], 3\n"
"s_lshl_b32 s18, s18, 2\n"
"s_add_u32 s18, s18, s60\n"
"s_mul_i32 s18, s18, 6\n"
"s_add_u32 s18, s18, 0\n"
"v_lshrrev_b32 v182, 4, v158\n"
"v_cmp_gt_u32 vcc, 6, v182\n"
"v_add_u32 v184, s18, v182\n"
"v_lshlrev_b32 v184, 7, v184\n"
"v_mov_b32 v185, 2147483647\n"
"v_cndmask_b32 v184, v185, v184, vcc\n"
"s_lshr_b32 s18, s63, 1\n"
"s_mul_i32 s18, s18, 82944\n"
"s_add_u32 s18, s18, 3072\n"
"buffer_load_dword v186, v184, s[44:47], s18 offen\n"
"v_mfma_f32_16x16x32_bf16 v[60:63], v[144:147], a[204:207], v[60:63]\n"
"s_waitcnt lgkmcnt(3)\n"
"v_mfma_f32_16x16x32_bf16 v[0:3], v[128:131], a[208:211], v[0:3]\n"
"ds_read_b64_tr_b16 v[140:141], v161 offset:35840\n"
"ds_read_b64_tr_b16 v[142:143], v161 offset:44032\n"
"v_mfma_f32_16x16x32_bf16 v[4:7], v[128:131], a[212:215], v[4:7]\n"
"v_exp_f32_e64 v150, -v150\n"
"v_mfma_f32_16x16x32_bf16 v[8:11], v[128:131], a[216:219], v[8:11]\n"
"v_exp_f32_e64 v151, -v151\n"
"v_mfma_f32_16x16x32_bf16 v[12:15], v[128:131], a[220:223], v[12:15]\n"
"v_add_f32 v150, 1.0, v150\n"
"v_add_f32 v151, 1.0, v151\n"
"v_mfma_f32_16x16x32_bf16 v[16:19], v[132:135], a[208:211], v[16:19]\n"
"s_waitcnt vmcnt(14)\n"
"s_barrier\n"
"v_mfma_f32_16x16x32_bf16 v[20:23], v[132:135], a[212:215], v[20:23]\n"
"ds_read_b64_tr_b16 v[144:145], v160 offset:49152\n"
"ds_read_b64_tr_b16 v[146:147], v160 offset:57344\n"
"v_mfma_f32_16x16x32_bf16 v[24:27], v[132:135], a[216:219], v[24:27]\n"
"s_add_u32 m0, s65, 16384\n"
"s_add_u32 s66, s9, 19456\n"
"buffer_load_dwordx4 v166, s[20:23], s66 offen sc1 lds\n"
"v_mfma_f32_16x16x32_bf16 v[28:31], v[132:135], a[220:223], v[28:31]\n"
"v_mul_f32 v116, v84, v116\n"
"s_waitcnt lgkmcnt(2)\n"
"v_mfma_f32_16x16x32_bf16 v[32:35], v[136:139], a[208:211], v[32:35]\n"
"ds_read_b64_tr_b16 v[128:129], v161 offset:49152\n"
"ds_read_b64_tr_b16 v[130:131], v161 offset:57344\n"
"v_mfma_f32_16x16x32_bf16 v[36:39], v[136:139], a[212:215], v[36:39]\n"
"v_rcp_f32 v150, v150\n"
"v_mfma_f32_16x16x32_bf16 v[40:43], v[136:139], a[216:219], v[40:43]\n"
"v_rcp_f32 v151, v151\n"
"v_mfma_f32_16x16x32_bf16 v[44:47], v[136:139], a[220:223], v[44:47]\n"
"v_mul_f32 v102, v102, v150\n"
"v_mul_f32 v103, v103, v151\n"
"v_mfma_f32_16x16x32_bf16 v[48:51], v[140:143], a[208:211], v[48:51]\n"
"ds_read_b64_tr_b16 v[132:133], v160 offset:50176\n"
"ds_read_b64_tr_b16 v[134:135], v160 offset:58368\n"
"v_mfma_f32_16x16x32_bf16 v[52:55], v[140:143], a[212:215], v[52:55]\n"
"s_add_u32 s66, s9, 22528\n"
"buffer_load_dwordx4 v166, s[20:23], s66 offen offset:1024 sc1 lds\n"
"v_mfma_f32_16x16x32_bf16 v[56:59], v[140:143], a[216:219], v[56:59]\n"
"v_cvt_pk_bf16_f32 v153, v102, v103\n"
"v_mul_f32 v117, v85, v117\n"
"v_mfma_f32_16x16x32_bf16 v[60:63], v[140:143], a[220:223], v[60:63]\n"
"v_fma_f32 v150, v86, v86, s75\n"
"s_waitcnt lgkmcnt(2)\n"
"v_mfma_f32_16x16x32_bf16 v[0:3], v[144:147], a[224:227], v[0:3]\n"
"ds_read_b64_tr_b16 v[136:137], v161 offset:50176\n"
"ds_read_b64_tr_b16 v[138:139], v161 offset:58368\n"
"v_mfma_f32_16x16x32_bf16 v[4:7], v[144:147], a[228:231], v[4:7]\n"
"v_exp_f32_e64 v148, -v148\n"
"v_mfma_f32_16x16x32_bf16 v[8:11], v[144:147], a[232:235], v[8:11]\n"
"v_exp_f32_e64 v149, -v149\n"
"v_mfma_f32_16x16x32_bf16 v[12:15], v[144:147], a[236:239], v[12:15]\n"
"v_add_f32 v148, 1.0, v148\n"
"v_add_f32 v149, 1.0, v149\n"
"v_mfma_f32_16x16x32_bf16 v[16:19], v[128:131], a[224:227], v[16:19]\n"
"ds_read_b64_tr_b16 v[140:141], v160 offset:51200\n"
"ds_read_b64_tr_b16 v[142:143], v160 offset:59392\n"
"v_mfma_f32_16x16x32_bf16 v[20:23], v[128:131], a[228:231], v[20:23]\n"
"s_add_u32 s66, s9, 25600\n"
"buffer_load_dwordx4 v166, s[20:23], s66 offen offset:2048 sc1 lds\n"
"v_mfma_f32_16x16x32_bf16 v[24:27], v[128:131], a[232:235], v[24:27]\n"
"v_rcp_f32 v148, v148\n"
"v_mfma_f32_16x16x32_bf16 v[28:31], v[128:131], a[236:239], v[28:31]\n"
"v_mul_f32 v116, v116, v148\n"
"s_waitcnt lgkmcnt(2)\n"
"v_mfma_f32_16x16x32_bf16 v[32:35], v[132:135], a[224:227], v[32:35]\n"
"ds_read_b64_tr_b16 v[144:145], v161 offset:51200\n"
"ds_read_b64_tr_b16 v[146:147], v161 offset:59392\n"
"v_mfma_f32_16x16x32_bf16 v[36:39], v[132:135], a[228:231], v[36:39]\n"
"v_rcp_f32 v149, v149\n"
"v_mfma_f32_16x16x32_bf16 v[40:43], v[132:135], a[232:235], v[40:43]\n"
"v_mul_f32 v117, v117, v149\n"
"v_cvt_pk_bf16_f32 v154, v116, v117\n"
"v_mfma_f32_16x16x32_bf16 v[44:47], v[132:135], a[236:239], v[44:47]\n"
"v_fma_f32 v151, v87, v87, s75\n"
"v_mul_f32 v150, v150, v86\n"
"v_mfma_f32_16x16x32_bf16 v[48:51], v[136:139], a[224:227], v[48:51]\n"
"ds_read_b64_tr_b16 v[128:129], v160 offset:52224\n"
"ds_read_b64_tr_b16 v[130:131], v160 offset:60416\n"
"v_mfma_f32_16x16x32_bf16 v[52:55], v[136:139], a[228:231], v[52:55]\n"
"s_add_u32 s66, s9, 28672\n"
"buffer_load_dwordx4 v166, s[20:23], s66 offen offset:3072 sc1 lds\n"
"v_mfma_f32_16x16x32_bf16 v[56:59], v[136:139], a[232:235], v[56:59]\n"
"v_mul_f32 v151, v151, v87\n"
"v_mul_f32 v118, v86, v118\n"
"v_mfma_f32_16x16x32_bf16 v[60:63], v[136:139], a[236:239], v[60:63]\n"
"v_mul_f32 v119, v87, v119\n"
"s_waitcnt lgkmcnt(2)\n"
"v_mfma_f32_16x16x32_bf16 v[0:3], v[140:143], a[240:243], v[0:3]\n"
"ds_read_b64_tr_b16 v[132:133], v161 offset:52224\n"
"ds_read_b64_tr_b16 v[134:135], v161 offset:60416\n"
"v_mfma_f32_16x16x32_bf16 v[4:7], v[140:143], a[244:247], v[4:7]\n"
"v_exp_f32_e64 v150, -v150\n"
"v_mfma_f32_16x16x32_bf16 v[8:11], v[140:143], a[248:251], v[8:11]\n"
"v_exp_f32_e64 v151, -v151\n"
"v_mfma_f32_16x16x32_bf16 v[12:15], v[140:143], a[252:255], v[12:15]\n"
"v_add_f32 v150, 1.0, v150\n"
"v_add_f32 v151, 1.0, v151\n"
"v_mfma_f32_16x16x32_bf16 v[16:19], v[144:147], a[240:243], v[16:19]\n"
"s_waitcnt vmcnt(14)\n"
"s_barrier\n"
"v_mfma_f32_16x16x32_bf16 v[20:23], v[144:147], a[244:247], v[20:23]\n"
"ds_read_b64_tr_b16 v[136:137], v162\n"
"ds_read_b64_tr_b16 v[138:139], v162 offset:8192\n"
"v_mfma_f32_16x16x32_bf16 v[24:27], v[144:147], a[248:251], v[24:27]\n"
"s_add_u32 m0, s65, 32768\n"
"s_add_u32 s66, s9, 35840\n"
"buffer_load_dwordx4 v166, s[20:23], s66 offen sc1 lds\n"
"v_mfma_f32_16x16x32_bf16 v[28:31], v[144:147], a[252:255], v[28:31]\n"
"s_waitcnt lgkmcnt(2)\n"
"v_mfma_f32_16x16x32_bf16 v[32:35], v[128:131], a[240:243], v[32:35]\n"
"ds_read_b64_tr_b16 v[140:141], v163\n"
"ds_read_b64_tr_b16 v[142:143], v163 offset:8192\n"
"v_mfma_f32_16x16x32_bf16 v[36:39], v[128:131], a[244:247], v[36:39]\n"
"v_rcp_f32 v150, v150\n"
"v_mfma_f32_16x16x32_bf16 v[40:43], v[128:131], a[248:251], v[40:43]\n"
"v_rcp_f32 v151, v151\n"
"v_mfma_f32_16x16x32_bf16 v[44:47], v[128:131], a[252:255], v[44:47]\n"
"v_mul_f32 v118, v118, v150\n"
"v_mul_f32 v119, v119, v151\n"
"v_mfma_f32_16x16x32_bf16 v[48:51], v[132:135], a[240:243], v[48:51]\n"
"ds_read_b64_tr_b16 v[144:145], v162 offset:1024\n"
"ds_read_b64_tr_b16 v[146:147], v162 offset:9216\n"
"v_mfma_f32_16x16x32_bf16 v[52:55], v[132:135], a[244:247], v[52:55]\n"
"s_add_u32 s66, s9, 38912\n"
"buffer_load_dwordx4 v166, s[20:23], s66 offen offset:1024 sc1 lds\n"
"v_mfma_f32_16x16x32_bf16 v[56:59], v[132:135], a[248:251], v[56:59]\n"
"v_cvt_pk_bf16_f32 v155, v118, v119\n"
"ds_write_b128 v156, v[152:155] offset:1024\n"
"v_mfma_f32_16x16x32_bf16 v[60:63], v[132:135], a[252:255], v[60:63]\n"
"buffer_store_dwordx4 v[152:155], v158, s[48:51], s17 offen offset:1024 sc1\n"
"s_waitcnt lgkmcnt(3)\n"
"v_mfma_f32_16x16x32_bf16 v[0:3], v[136:139], v[192:195], v[0:3]\n"
"ds_read_b64_tr_b16 v[128:129], v163 offset:1024\n"
"ds_read_b64_tr_b16 v[130:131], v163 offset:9216\n"
"v_mfma_f32_16x16x32_bf16 v[4:7], v[136:139], v[196:199], v[4:7]\n"
"v_fma_f32 v148, v72, v72, s75\n"
"v_fma_f32 v149, v73, v73, s75\n"
"v_mfma_f32_16x16x32_bf16 v[8:11], v[136:139], v[200:203], v[8:11]\n"
"v_mul_f32 v148, v148, v72\n"
"v_mul_f32 v149, v149, v73\n"
"v_mfma_f32_16x16x32_bf16 v[12:15], v[136:139], v[204:207], v[12:15]\n"
"v_exp_f32_e64 v148, -v148\n"
"v_mfma_f32_16x16x32_bf16 v[16:19], v[140:143], v[192:195], v[16:19]\n"
"ds_read_b64_tr_b16 v[132:133], v162 offset:2048\n"
"ds_read_b64_tr_b16 v[134:135], v162 offset:10240\n"
"v_mfma_f32_16x16x32_bf16 v[20:23], v[140:143], v[196:199], v[20:23]\n"
"s_add_u32 s66, s9, 41984\n"
"buffer_load_dwordx4 v166, s[20:23], s66 offen offset:2048 sc1 lds\n"
"v_mfma_f32_16x16x32_bf16 v[24:27], v[140:143], v[200:203], v[24:27]\n"
"v_exp_f32_e64 v149, -v149\n"
"v_mfma_f32_16x16x32_bf16 v[28:31], v[140:143], v[204:207], v[28:31]\n"
"v_mul_f32 v104, v72, v104\n"
"s_waitcnt lgkmcnt(2)\n"
"v_mfma_f32_16x16x32_bf16 v[32:35], v[144:147], v[192:195], v[32:35]\n"
"ds_read_b64_tr_b16 v[136:137], v163 offset:2048\n"
"ds_read_b64_tr_b16 v[138:139], v163 offset:10240\n"
"v_mfma_f32_16x16x32_bf16 v[36:39], v[144:147], v[196:199], v[36:39]\n"
"v_mul_f32 v105, v73, v105\n"
"v_add_f32 v148, 1.0, v148\n"
"v_mfma_f32_16x16x32_bf16 v[40:43], v[144:147], v[200:203], v[40:43]\n"
"v_add_f32 v149, 1.0, v149\n"
"v_fma_f32 v150, v74, v74, s75\n"
"v_mfma_f32_16x16x32_bf16 v[44:47], v[144:147], v[204:207], v[44:47]\n"
"v_rcp_f32 v148, v148\n"
"v_mfma_f32_16x16x32_bf16 v[48:51], v[128:131], v[192:195], v[48:51]\n"
"ds_read_b64_tr_b16 v[140:141], v162 offset:3072\n"
"ds_read_b64_tr_b16 v[142:143], v162 offset:11264\n"
"v_mfma_f32_16x16x32_bf16 v[52:55], v[128:131], v[196:199], v[52:55]\n"
"s_add_u32 s66, s9, 45056\n"
"buffer_load_dwordx4 v166, s[20:23], s66 offen offset:3072 sc1 lds\n"
"v_mfma_f32_16x16x32_bf16 v[56:59], v[128:131], v[200:203], v[56:59]\n"
"v_rcp_f32 v149, v149\n"
"v_mfma_f32_16x16x32_bf16 v[60:63], v[128:131], v[204:207], v[60:63]\n"
"v_mul_f32 v104, v104, v148\n"
"s_waitcnt lgkmcnt(2)\n"
"v_mfma_f32_16x16x32_bf16 v[0:3], v[132:135], v[208:211], v[0:3]\n"
"ds_read_b64_tr_b16 v[144:145], v163 offset:3072\n"
"ds_read_b64_tr_b16 v[146:147], v163 offset:11264\n"
"v_mfma_f32_16x16x32_bf16 v[4:7], v[132:135], v[212:215], v[4:7]\n"
"v_mul_f32 v105, v105, v149\n"
"v_cvt_pk_bf16_f32 v152, v104, v105\n"
"v_mfma_f32_16x16x32_bf16 v[8:11], v[132:135], v[216:219], v[8:11]\n"
"v_fma_f32 v151, v75, v75, s75\n"
"v_mul_f32 v150, v150, v74\n"
"v_mfma_f32_16x16x32_bf16 v[12:15], v[132:135], v[220:223], v[12:15]\n"
"v_mul_f32 v151, v151, v75\n"
"v_mul_f32 v106, v74, v106\n"
"v_mfma_f32_16x16x32_bf16 v[16:19], v[136:139], v[208:211], v[16:19]\n"
"s_waitcnt vmcnt(10)\n"
"s_barrier\n"
"v_mfma_f32_16x16x32_bf16 v[20:23], v[136:139], v[212:215], v[20:23]\n"
"ds_read_b64_tr_b16 v[128:129], v160\n"
"ds_read_b64_tr_b16 v[130:131], v160 offset:8192\n"
"v_mfma_f32_16x16x32_bf16 v[24:27], v[136:139], v[216:219], v[24:27]\n"
"s_add_u32 m0, s65, 49152\n"
"s_add_u32 s66, s9, 52224\n"
"buffer_load_dwordx4 v166, s[20:23], s66 offen sc1 lds\n"
"v_mfma_f32_16x16x32_bf16 v[28:31], v[136:139], v[220:223], v[28:31]\n"
"v_mul_f32 v107, v75, v107\n"
"s_waitcnt lgkmcnt(2)\n"
"v_mfma_f32_16x16x32_bf16 v[32:35], v[140:143], v[208:211], v[32:35]\n"
"ds_read_b64_tr_b16 v[132:133], v161\n"
"ds_read_b64_tr_b16 v[134:135], v161 offset:8192\n"
"v_mfma_f32_16x16x32_bf16 v[36:39], v[140:143], v[212:215], v[36:39]\n"
"v_exp_f32_e64 v150, -v150\n"
"v_mfma_f32_16x16x32_bf16 v[40:43], v[140:143], v[216:219], v[40:43]\n"
"v_exp_f32_e64 v151, -v151\n"
"v_mfma_f32_16x16x32_bf16 v[44:47], v[140:143], v[220:223], v[44:47]\n"
"v_add_f32 v150, 1.0, v150\n"
"v_add_f32 v151, 1.0, v151\n"
"v_mfma_f32_16x16x32_bf16 v[48:51], v[144:147], v[208:211], v[48:51]\n"
"ds_read_b64_tr_b16 v[136:137], v160 offset:1024\n"
"ds_read_b64_tr_b16 v[138:139], v160 offset:9216\n"
"v_mfma_f32_16x16x32_bf16 v[52:55], v[144:147], v[212:215], v[52:55]\n"
"s_add_u32 s66, s9, 55296\n"
"buffer_load_dwordx4 v166, s[20:23], s66 offen offset:1024 sc1 lds\n"
"v_mfma_f32_16x16x32_bf16 v[56:59], v[144:147], v[216:219], v[56:59]\n"
"v_rcp_f32 v150, v150\n"
"v_mfma_f32_16x16x32_bf16 v[60:63], v[144:147], v[220:223], v[60:63]\n"
"v_mul_f32 v106, v106, v150\n"
"v_rcp_f32 v151, v151\n"
"s_nop 0\n"
"v_mul_f32 v107, v107, v151\n"
"v_cvt_pk_bf16_f32 v153, v106, v107\n"
"v_fma_f32 v148, v88, v88, s75\n"
"v_fma_f32 v149, v89, v89, s75\n"
"v_mul_f32 v148, v148, v88\n"
"v_mul_f32 v149, v149, v89\n"
"v_exp_f32_e64 v148, -v148\n"
"v_exp_f32_e64 v149, -v149\n"
"v_mul_f32 v120, v88, v120\n"
"v_mul_f32 v121, v89, v121\n"
"v_add_f32 v148, 1.0, v148\n"
"v_add_f32 v149, 1.0, v149\n"
"v_rcp_f32 v148, v148\n"
"v_rcp_f32 v149, v149\n"
"v_mul_f32 v120, v120, v148\n"
"v_mul_f32 v121, v121, v149\n"
"v_cvt_pk_bf16_f32 v154, v120, v121\n"
"v_fma_f32 v150, v90, v90, s75\n"
"v_fma_f32 v151, v91, v91, s75\n"
"v_mul_f32 v150, v150, v90\n"
"v_mul_f32 v151, v151, v91\n"
"v_exp_f32_e64 v150, -v150\n"
"v_exp_f32_e64 v151, -v151\n"
"v_mul_f32 v122, v90, v122\n"
"v_mul_f32 v123, v91, v123\n"
"v_add_f32 v150, 1.0, v150\n"
"v_add_f32 v151, 1.0, v151\n"
"v_rcp_f32 v150, v150\n"
"v_rcp_f32 v151, v151\n"
"v_mul_f32 v122, v122, v150\n"
"v_mul_f32 v123, v123, v151\n"
"v_cvt_pk_bf16_f32 v155, v122, v123\n"
"ds_write_b128 v156, v[152:155] offset:2048\n"
"buffer_store_dwordx4 v[152:155], v158, s[48:51], s17 offen offset:2048 sc1\n"
"v_fma_f32 v148, v76, v76, s75\n"
"v_fma_f32 v149, v77, v77, s75\n"
"v_mul_f32 v148, v148, v76\n"
"v_mul_f32 v149, v149, v77\n"
"v_exp_f32_e64 v148, -v148\n"
"v_exp_f32_e64 v149, -v149\n"
"v_mul_f32 v108, v76, v108\n"
"v_mul_f32 v109, v77, v109\n"
"v_add_f32 v148, 1.0, v148\n"
"v_add_f32 v149, 1.0, v149\n"
"v_rcp_f32 v148, v148\n"
"v_rcp_f32 v149, v149\n"
"v_mul_f32 v108, v108, v148\n"
"v_mul_f32 v109, v109, v149\n"
"v_cvt_pk_bf16_f32 v152, v108, v109\n"
"v_fma_f32 v150, v78, v78, s75\n"
"v_fma_f32 v151, v79, v79, s75\n"
"v_mul_f32 v150, v150, v78\n"
"v_mul_f32 v151, v151, v79\n"
"v_exp_f32_e64 v150, -v150\n"
"v_exp_f32_e64 v151, -v151\n"
"v_mul_f32 v110, v78, v110\n"
"v_mul_f32 v111, v79, v111\n"
"v_add_f32 v150, 1.0, v150\n"
"v_add_f32 v151, 1.0, v151\n"
"v_rcp_f32 v150, v150\n"
"v_rcp_f32 v151, v151\n"
"v_mul_f32 v110, v110, v150\n"
"v_mul_f32 v111, v111, v151\n"
"v_cvt_pk_bf16_f32 v153, v110, v111\n"
"v_fma_f32 v148, v92, v92, s75\n"
"v_fma_f32 v149, v93, v93, s75\n"
"v_mul_f32 v148, v148, v92\n"
"v_mul_f32 v149, v149, v93\n"
"v_exp_f32_e64 v148, -v148\n"
"v_exp_f32_e64 v149, -v149\n"
"v_mul_f32 v124, v92, v124\n"
"v_mul_f32 v125, v93, v125\n"
"v_add_f32 v148, 1.0, v148\n"
"v_add_f32 v149, 1.0, v149\n"
"v_rcp_f32 v148, v148\n"
"v_rcp_f32 v149, v149\n"
"v_mul_f32 v124, v124, v148\n"
"v_mul_f32 v125, v125, v149\n"
"v_cvt_pk_bf16_f32 v154, v124, v125\n"
"v_fma_f32 v150, v94, v94, s75\n"
"v_fma_f32 v151, v95, v95, s75\n"
"v_mul_f32 v150, v150, v94\n"
"v_mul_f32 v151, v151, v95\n"
"v_exp_f32_e64 v150, -v150\n"
"v_exp_f32_e64 v151, -v151\n"
"v_mul_f32 v126, v94, v126\n"
"v_mul_f32 v127, v95, v127\n"
"v_add_f32 v150, 1.0, v150\n"
"v_add_f32 v151, 1.0, v151\n"
"v_rcp_f32 v150, v150\n"
"v_rcp_f32 v151, v151\n"
"v_mul_f32 v126, v126, v150\n"
"v_mul_f32 v127, v127, v151\n"
"v_cvt_pk_bf16_f32 v155, v126, v127\n"
"ds_write_b128 v156, v[152:155] offset:3072\n"
"buffer_store_dwordx4 v[152:155], v158, s[48:51], s17 offen offset:3072 sc1\n"
"s_waitcnt lgkmcnt(4)\n"
"v_mfma_f32_16x16x32_bf16 v[64:67], v[128:131], a[0:3], 0\n"
"ds_read_b64_tr_b16 v[140:141], v161 offset:1024\n"
"ds_read_b64_tr_b16 v[142:143], v161 offset:9216\n"
"v_mfma_f32_16x16x32_bf16 v[68:71], v[128:131], a[4:7], 0\n"
"v_mfma_f32_16x16x32_bf16 v[72:75], v[128:131], a[8:11], 0\n"
"v_mfma_f32_16x16x32_bf16 v[76:79], v[128:131], a[12:15], 0\n"
"v_mfma_f32_16x16x32_bf16 v[80:83], v[132:135], a[0:3], 0\n"
"ds_read_b64_tr_b16 v[144:145], v160 offset:2048\n"
"ds_read_b64_tr_b16 v[146:147], v160 offset:10240\n"
"v_mfma_f32_16x16x32_bf16 v[84:87], v[132:135], a[4:7], 0\n"
"s_add_u32 s66, s9, 58368\n"
"buffer_load_dwordx4 v166, s[20:23], s66 offen offset:2048 sc1 lds\n"
"v_mfma_f32_16x16x32_bf16 v[88:91], v[132:135], a[8:11], 0\n"
"v_add_u32 v156, 81920, v177\n"
"v_mfma_f32_16x16x32_bf16 v[92:95], v[132:135], a[12:15], 0\n"
"s_waitcnt lgkmcnt(2)\n"
"v_mfma_f32_16x16x32_bf16 v[64:67], v[136:139], a[16:19], v[64:67]\n"
"ds_read_b64_tr_b16 v[128:129], v161 offset:2048\n"
"ds_read_b64_tr_b16 v[130:131], v161 offset:10240\n"
"v_mfma_f32_16x16x32_bf16 v[68:71], v[136:139], a[20:23], v[68:71]\n"
"s_lshl_b32 s17, s62, 13\n"
"s_add_u32 s17, s17, s27\n"
"s_add_u32 s17, s17, 49152\n"
"v_mfma_f32_16x16x32_bf16 v[72:75], v[136:139], a[24:27], v[72:75]\n"
"v_fma_f32 v148, v0, v0, s75\n"
"v_fma_f32 v149, v1, v1, s75\n"
"v_mfma_f32_16x16x32_bf16 v[76:79], v[136:139], a[28:31], v[76:79]\n"
"v_mul_f32 v148, v148, v0\n"
"v_mul_f32 v149, v149, v1\n"
"v_mfma_f32_16x16x32_bf16 v[80:83], v[140:143], a[16:19], v[80:83]\n"
"ds_read_b64_tr_b16 v[132:133], v160 offset:3072\n"
"ds_read_b64_tr_b16 v[134:135], v160 offset:11264\n"
"v_mfma_f32_16x16x32_bf16 v[84:87], v[140:143], a[20:23], v[84:87]\n"
"s_add_u32 s66, s9, 61440\n"
"buffer_load_dwordx4 v166, s[20:23], s66 offen offset:3072 sc1 lds\n"
"v_mfma_f32_16x16x32_bf16 v[88:91], v[140:143], a[24:27], v[88:91]\n"
"v_exp_f32_e64 v148, -v148\n"
"v_mfma_f32_16x16x32_bf16 v[92:95], v[140:143], a[28:31], v[92:95]\n"
"v_mul_f32 v32, v0, v32\n"
"s_waitcnt lgkmcnt(2)\n"
"v_mfma_f32_16x16x32_bf16 v[64:67], v[144:147], a[32:35], v[64:67]\n"
"ds_read_b64_tr_b16 v[136:137], v161 offset:3072\n"
"ds_read_b64_tr_b16 v[138:139], v161 offset:11264\n"
"v_mfma_f32_16x16x32_bf16 v[68:71], v[144:147], a[36:39], v[68:71]\n"
"v_exp_f32_e64 v149, -v149\n"
"v_mfma_f32_16x16x32_bf16 v[72:75], v[144:147], a[40:43], v[72:75]\n"
"v_mul_f32 v33, v1, v33\n"
"v_add_f32 v148, 1.0, v148\n"
"v_mfma_f32_16x16x32_bf16 v[76:79], v[144:147], a[44:47], v[76:79]\n"
"v_add_f32 v149, 1.0, v149\n"
"v_fma_f32 v150, v2, v2, s75\n"
"v_mfma_f32_16x16x32_bf16 v[80:83], v[128:131], a[32:35], v[80:83]\n"
"s_waitcnt vmcnt(11)\n"
"s_barrier\n"
"v_mfma_f32_16x16x32_bf16 v[84:87], v[128:131], a[36:39], v[84:87]\n"
"ds_read_b64_tr_b16 v[140:141], v160 offset:16384\n"
"ds_read_b64_tr_b16 v[142:143], v160 offset:24576\n"
"v_mfma_f32_16x16x32_bf16 v[88:91], v[128:131], a[40:43], v[88:91]\n"
"v_rcp_f32 v148, v148\n"
"v_mfma_f32_16x16x32_bf16 v[92:95], v[128:131], a[44:47], v[92:95]\n"
"v_mul_f32 v32, v32, v148\n"
"s_waitcnt lgkmcnt(2)\n"
"v_mfma_f32_16x16x32_bf16 v[64:67], v[132:135], a[48:51], v[64:67]\n"
"ds_read_b64_tr_b16 v[144:145], v161 offset:16384\n"
"ds_read_b64_tr_b16 v[146:147], v161 offset:24576\n"
"v_mfma_f32_16x16x32_bf16 v[68:71], v[132:135], a[52:55], v[68:71]\n"
"v_rcp_f32 v149, v149\n"
"v_mfma_f32_16x16x32_bf16 v[72:75], v[132:135], a[56:59], v[72:75]\n"
"v_mul_f32 v33, v33, v149\n"
"v_cvt_pk_bf16_f32 v152, v32, v33\n"
"v_mfma_f32_16x16x32_bf16 v[76:79], v[132:135], a[60:63], v[76:79]\n"
"v_fma_f32 v151, v3, v3, s75\n"
"v_mul_f32 v150, v150, v2\n"
"v_mfma_f32_16x16x32_bf16 v[80:83], v[136:139], a[48:51], v[80:83]\n"
"ds_read_b64_tr_b16 v[128:129], v160 offset:17408\n"
"ds_read_b64_tr_b16 v[130:131], v160 offset:25600\n"
"v_mfma_f32_16x16x32_bf16 v[84:87], v[136:139], a[52:55], v[84:87]\n"
"v_mul_f32 v151, v151, v3\n"
"v_mul_f32 v34, v2, v34\n"
"v_mfma_f32_16x16x32_bf16 v[88:91], v[136:139], a[56:59], v[88:91]\n"
"v_exp_f32_e64 v150, -v150\n"
"v_mfma_f32_16x16x32_bf16 v[92:95], v[136:139], a[60:63], v[92:95]\n"
"v_mul_f32 v35, v3, v35\n"
"s_waitcnt lgkmcnt(2)\n"
"v_mfma_f32_16x16x32_bf16 v[64:67], v[140:143], a[64:67], v[64:67]\n"
"ds_read_b64_tr_b16 v[132:133], v161 offset:17408\n"
"ds_read_b64_tr_b16 v[134:135], v161 offset:25600\n"
"v_mfma_f32_16x16x32_bf16 v[68:71], v[140:143], a[68:71], v[68:71]\n"
"v_exp_f32_e64 v151, -v151\n"
"v_mfma_f32_16x16x32_bf16 v[72:75], v[140:143], a[72:75], v[72:75]\n"
"v_add_f32 v150, 1.0, v150\n"
"v_add_f32 v151, 1.0, v151\n"
"v_mfma_f32_16x16x32_bf16 v[76:79], v[140:143], a[76:79], v[76:79]\n"
"v_rcp_f32 v150, v150\n"
"v_mfma_f32_16x16x32_bf16 v[80:83], v[144:147], a[64:67], v[80:83]\n"
"s_lshl_b32 s68, s61, 12\n"
"s_add_u32 s68, s68, 131072\n"
"v_add_u32 v180, s68, v179\n"
"v_mfma_f32_16x16x32_bf16 v[84:87], v[144:147], a[68:71], v[84:87]\n"
"ds_read_b128 a[0:3], v180\n"
"ds_read_b128 a[4:7], v180 offset:1024\n"
"v_mfma_f32_16x16x32_bf16 v[88:91], v[144:147], a[72:75], v[88:91]\n"
"ds_read_b128 a[8:11], v180 offset:2048\n"
"ds_read_b128 a[12:15], v180 offset:3072\n"
"v_mfma_f32_16x16x32_bf16 v[92:95], v[144:147], a[76:79], v[92:95]\n"
"ds_read_b64_tr_b16 v[136:137], v160 offset:18432\n"
"s_waitcnt lgkmcnt(5)\n"
"v_mfma_f32_16x16x32_bf16 v[64:67], v[128:131], a[80:83], v[64:67]\n"
"ds_read_b64_tr_b16 v[138:139], v160 offset:26624\n"
"ds_read_b64_tr_b16 v[140:141], v161 offset:18432\n"
"v_mfma_f32_16x16x32_bf16 v[68:71], v[128:131], a[84:87], v[68:71]\n"
"ds_read_b64_tr_b16 v[142:143], v161 offset:26624\n"
"v_mul_f32 v34, v34, v150\n"
"v_mfma_f32_16x16x32_bf16 v[72:75], v[128:131], a[88:91], v[72:75]\n"
"v_rcp_f32 v151, v151\n"
"v_mfma_f32_16x16x32_bf16 v[76:79], v[128:131], a[92:95], v[76:79]\n"
"v_mul_f32 v35, v35, v151\n"
"v_cvt_pk_bf16_f32 v153, v34, v35\n"
"v_mfma_f32_16x16x32_bf16 v[80:83], v[132:135], a[80:83], v[80:83]\n"
"s_lshl_b32 s68, s61, 12\n"
"s_add_u32 s68, s68, 139264\n"
"v_add_u32 v180, s68, v179\n"
"v_mfma_f32_16x16x32_bf16 v[84:87], v[132:135], a[84:87], v[84:87]\n"
"ds_read_b128 a[16:19], v180\n"
"ds_read_b128 a[20:23], v180 offset:1024\n"
"v_mfma_f32_16x16x32_bf16 v[88:91], v[132:135], a[88:91], v[88:91]\n"
"ds_read_b128 a[24:27], v180 offset:2048\n"
"ds_read_b128 a[28:31], v180 offset:3072\n"
"v_mfma_f32_16x16x32_bf16 v[92:95], v[132:135], a[92:95], v[92:95]\n"
"ds_read_b64_tr_b16 v[144:145], v160 offset:19456\n"
"s_waitcnt lgkmcnt(5)\n"
"v_mfma_f32_16x16x32_bf16 v[64:67], v[136:139], a[96:99], v[64:67]\n"
"ds_read_b64_tr_b16 v[146:147], v160 offset:27648\n"
"ds_read_b64_tr_b16 v[128:129], v161 offset:19456\n"
"v_mfma_f32_16x16x32_bf16 v[68:71], v[136:139], a[100:103], v[68:71]\n"
"ds_read_b64_tr_b16 v[130:131], v161 offset:27648\n"
"v_fma_f32 v148, v16, v16, s75\n"
"v_mfma_f32_16x16x32_bf16 v[72:75], v[136:139], a[104:107], v[72:75]\n"
"v_fma_f32 v149, v17, v17, s75\n"
"v_mul_f32 v148, v148, v16\n"
"v_mfma_f32_16x16x32_bf16 v[76:79], v[136:139], a[108:111], v[76:79]\n"
"v_mul_f32 v149, v149, v17\n"
"v_mul_f32 v48, v16, v48\n"
"v_mfma_f32_16x16x32_bf16 v[80:83], v[140:143], a[96:99], v[80:83]\n"
"s_waitcnt vmcnt(2)\n"
"s_barrier\n"
"s_lshl_b32 s68, s64, 4\n"
"s_add_u32 s68, s68, s63\n"
"s_lshl_b32 s68, s68, 7\n"
"s_add_u32 s68, s68, 16384\n"
"s_cmp_eq_u32 s60, 0\n"
"s_cbranch_scc0 Lsig4_%=\n"
"v_mov_b32 v180, 1\n"
"s_mov_b64 exec, 1\n"
"buffer_atomic_add v180, off, s[48:51], s68\n"
"s_mov_b64 exec, -1\n"
"Lsig4_%=:\n"
"v_mfma_f32_16x16x32_bf16 v[84:87], v[140:143], a[100:103], v[84:87]\n"
"s_lshl_b32 s68, s61, 12\n"
"s_add_u32 s68, s68, 114688\n"
"v_add_u32 v180, s68, v179\n"
"v_mfma_f32_16x16x32_bf16 v[88:91], v[140:143], a[104:107], v[88:91]\n"
"ds_read_b128 a[32:35], v180\n"
"ds_read_b128 a[36:39], v180 offset:1024\n"
"v_mfma_f32_16x16x32_bf16 v[92:95], v[140:143], a[108:111], v[92:95]\n"
"ds_read_b128 a[40:43], v180 offset:2048\n"
"s_waitcnt lgkmcnt(3)\n"
"v_mfma_f32_16x16x32_bf16 v[64:67], v[144:147], a[112:115], v[64:67]\n"
"ds_read_b128 a[44:47], v180 offset:3072\n"
"ds_read_b64_tr_b16 v[132:133], v160 offset:32768\n"
"v_mfma_f32_16x16x32_bf16 v[68:71], v[144:147], a[116:119], v[68:71]\n"
"ds_read_b64_tr_b16 v[134:135], v160 offset:40960\n"
"ds_read_b64_tr_b16 v[136:137], v161 offset:32768\n"
"v_mfma_f32_16x16x32_bf16 v[72:75], v[144:147], a[120:123], v[72:75]\n"
"ds_read_b64_tr_b16 v[138:139], v161 offset:40960\n"
"s_add_u32 m0, s65, 0\n"
"s_add_u32 s66, s9, 68608\n"
"buffer_load_dwordx4 v166, s[20:23], s66 offen sc1 lds\n"
"v_mfma_f32_16x16x32_bf16 v[76:79], v[144:147], a[124:127], v[76:79]\n"
"v_exp_f32_e64 v148, -v148\n"
"v_mfma_f32_16x16x32_bf16 v[80:83], v[128:131], a[112:115], v[80:83]\n"
"s_lshl_b32 s68, s61, 12\n"
"s_add_u32 s68, s68, 122880\n"
"v_add_u32 v180, s68, v179\n"
"v_mfma_f32_16x16x32_bf16 v[84:87], v[128:131], a[116:119], v[84:87]\n"
"ds_read_b128 a[48:51], v180\n"
"ds_read_b128 a[52:55], v180 offset:1024\n"
"v_mfma_f32_16x16x32_bf16 v[88:91], v[128:131], a[120:123], v[88:91]\n"
"ds_read_b128 a[56:59], v180 offset:2048\n"
"ds_read_b128 a[60:63], v180 offset:3072\n"
"v_mfma_f32_16x16x32_bf16 v[92:95], v[128:131], a[124:127], v[92:95]\n"
"ds_read_b64_tr_b16 v[140:141], v160 offset:33792\n"
"s_waitcnt lgkmcnt(5)\n"
"v_mfma_f32_16x16x32_bf16 v[64:67], v[132:135], a[128:131], v[64:67]\n"
"ds_read_b64_tr_b16 v[142:143], v160 offset:41984\n"
"ds_read_b64_tr_b16 v[144:145], v161 offset:33792\n"
"v_mfma_f32_16x16x32_bf16 v[68:71], v[132:135], a[132:135], v[68:71]\n"
"ds_read_b64_tr_b16 v[146:147], v161 offset:41984\n"
"s_add_u32 s66, s9, 71680\n"
"buffer_load_dwordx4 v166, s[20:23], s66 offen offset:1024 sc1 lds\n"
"v_mfma_f32_16x16x32_bf16 v[72:75], v[132:135], a[136:139], v[72:75]\n"
"v_exp_f32_e64 v149, -v149\n"
"v_mfma_f32_16x16x32_bf16 v[76:79], v[132:135], a[140:143], v[76:79]\n"
"v_mul_f32 v49, v17, v49\n"
"v_add_f32 v148, 1.0, v148\n"
"v_mfma_f32_16x16x32_bf16 v[80:83], v[136:139], a[128:131], v[80:83]\n"
"s_lshl_b32 s68, s61, 12\n"
"s_add_u32 s68, s68, 98304\n"
"v_add_u32 v180, s68, v179\n"
"v_mfma_f32_16x16x32_bf16 v[84:87], v[136:139], a[132:135], v[84:87]\n"
"ds_read_b128 a[64:67], v180\n"
"ds_read_b128 a[68:71], v180 offset:1024\n"
"v_mfma_f32_16x16x32_bf16 v[88:91], v[136:139], a[136:139], v[88:91]\n"
"ds_read_b128 a[72:75], v180 offset:2048\n"
"ds_read_b128 a[76:79], v180 offset:3072\n"
"v_mfma_f32_16x16x32_bf16 v[92:95], v[136:139], a[140:143], v[92:95]\n"
"ds_read_b64_tr_b16 v[128:129], v160 offset:34816\n"
"s_waitcnt lgkmcnt(5)\n"
"v_mfma_f32_16x16x32_bf16 v[64:67], v[140:143], a[144:147], v[64:67]\n"
"ds_read_b64_tr_b16 v[130:131], v160 offset:43008\n"
"ds_read_b64_tr_b16 v[132:133], v161 offset:34816\n"
"v_mfma_f32_16x16x32_bf16 v[68:71], v[140:143], a[148:151], v[68:71]\n"
"ds_read_b64_tr_b16 v[134:135], v161 offset:43008\n"
"v_add_f32 v149, 1.0, v149\n"
"v_mfma_f32_16x16x32_bf16 v[72:75], v[140:143], a[152:155], v[72:75]\n"
"v_rcp_f32 v148, v148\n"
"v_mfma_f32_16x16x32_bf16 v[76:79], v[140:143], a[156:159], v[76:79]\n"
"v_rcp_f32 v149, v149\n"
"v_mfma_f32_16x16x32_bf16 v[80:83], v[144:147], a[144:147], v[80:83]\n"
"s_lshl_b32 s68, s61, 12\n"
"s_add_u32 s68, s68, 106496\n"
"v_add_u32 v180, s68, v179\n"
"v_mfma_f32_16x16x32_bf16 v[84:87], v[144:147], a[148:151], v[84:87]\n"
"ds_read_b128 a[80:83], v180\n"
"ds_read_b128 a[84:87], v180 offset:1024\n"
"v_mfma_f32_16x16x32_bf16 v[88:91], v[144:147], a[152:155], v[88:91]\n"
"ds_read_b128 a[88:91], v180 offset:2048\n"
"ds_read_b128 a[92:95], v180 offset:3072\n"
"v_mfma_f32_16x16x32_bf16 v[92:95], v[144:147], a[156:159], v[92:95]\n"
"ds_read_b64_tr_b16 v[136:137], v160 offset:35840\n"
"s_waitcnt lgkmcnt(5)\n"
"v_mfma_f32_16x16x32_bf16 v[64:67], v[128:131], a[160:163], v[64:67]\n"
"ds_read_b64_tr_b16 v[138:139], v160 offset:44032\n"
"ds_read_b64_tr_b16 v[140:141], v161 offset:35840\n"
"v_mfma_f32_16x16x32_bf16 v[68:71], v[128:131], a[164:167], v[68:71]\n"
"ds_read_b64_tr_b16 v[142:143], v161 offset:44032\n"
"v_mul_f32 v48, v48, v148\n"
"v_mfma_f32_16x16x32_bf16 v[72:75], v[128:131], a[168:171], v[72:75]\n"
"v_mul_f32 v49, v49, v149\n"
"v_cvt_pk_bf16_f32 v154, v48, v49\n"
"v_mfma_f32_16x16x32_bf16 v[76:79], v[128:131], a[172:175], v[76:79]\n"
"v_fma_f32 v150, v18, v18, s75\n"
"v_fma_f32 v151, v19, v19, s75\n"
"v_mfma_f32_16x16x32_bf16 v[80:83], v[132:135], a[160:163], v[80:83]\n"
"s_waitcnt vmcnt(2)\n"
"s_barrier\n"
"s_lshl_b32 s68, s64, 4\n"
"s_add_u32 s68, s68, s63\n"
"s_xor_b32 s68, s68, 1\n"
"s_lshl_b32 s68, s68, 7\n"
"s_add_u32 s19, s68, 16384\n"
"buffer_load_dword v178, off, s[48:51], s19 sc1\n"
"v_mfma_f32_16x16x32_bf16 v[84:87], v[132:135], a[164:167], v[84:87]\n"
"s_lshl_b32 s68, s61, 13\n"
"s_add_u32 s68, s68, 147456\n"
"v_add_u32 v180, s68, v179\n"
"v_mfma_f32_16x16x32_bf16 v[88:91], v[132:135], a[168:171], v[88:91]\n"
"ds_read_b128 a[96:99], v180\n"
"ds_read_b128 a[100:103], v180 offset:2048\n"
"v_mfma_f32_16x16x32_bf16 v[92:95], v[132:135], a[172:175], v[92:95]\n"
"ds_read_b128 a[104:107], v180 offset:4096\n"
"s_waitcnt lgkmcnt(3)\n"
"v_mfma_f32_16x16x32_bf16 v[64:67], v[136:139], a[176:179], v[64:67]\n"
"ds_read_b128 a[108:111], v180 offset:6144\n"
"ds_read_b64_tr_b16 v[144:145], v160 offset:49152\n"
"v_mfma_f32_16x16x32_bf16 v[68:71], v[136:139], a[180:183], v[68:71]\n"
"ds_read_b64_tr_b16 v[146:147], v160 offset:57344\n"
"ds_read_b64_tr_b16 v[128:129], v161 offset:49152\n"
"v_mfma_f32_16x16x32_bf16 v[72:75], v[136:139], a[184:187], v[72:75]\n"
"ds_read_b64_tr_b16 v[130:131], v161 offset:57344\n"
"s_add_u32 m0, s65, 16384\n"
"s_add_u32 s66, s72, 0\n"
"buffer_load_dwordx4 v173, s[40:43], s66 offen lds\n"
"v_mfma_f32_16x16x32_bf16 v[76:79], v[136:139], a[188:191], v[76:79]\n"
"v_mul_f32 v150, v150, v18\n"
"v_mul_f32 v151, v151, v19\n"
"v_mfma_f32_16x16x32_bf16 v[80:83], v[140:143], a[176:179], v[80:83]\n"
"s_lshl_b32 s68, s61, 13\n"
"s_add_u32 s68, s68, 148480\n"
"v_add_u32 v180, s68, v179\n"
"v_mfma_f32_16x16x32_bf16 v[84:87], v[140:143], a[180:183], v[84:87]\n"
"ds_read_b128 a[112:115], v180\n"
"ds_read_b128 a[116:119], v180 offset:2048\n"
"v_mfma_f32_16x16x32_bf16 v[88:91], v[140:143], a[184:187], v[88:91]\n"
"ds_read_b128 a[120:123], v180 offset:4096\n"
"ds_read_b128 a[124:127], v180 offset:6144\n"
"v_mfma_f32_16x16x32_bf16 v[92:95], v[140:143], a[188:191], v[92:95]\n"
"ds_read_b64_tr_b16 v[132:133], v160 offset:50176\n"
"s_waitcnt lgkmcnt(5)\n"
"v_mfma_f32_16x16x32_bf16 v[64:67], v[144:147], a[192:195], v[64:67]\n"
"ds_read_b64_tr_b16 v[134:135], v160 offset:58368\n"
"ds_read_b64_tr_b16 v[136:137], v161 offset:50176\n"
"v_mfma_f32_16x16x32_bf16 v[68:71], v[144:147], a[196:199], v[68:71]\n"
"ds_read_b64_tr_b16 v[138:139], v161 offset:58368\n"
"s_add_u32 s66, s72, 36864\n"
"buffer_load_dwordx4 v174, s[40:43], s66 offen offset:1024 lds\n"
"v_mfma_f32_16x16x32_bf16 v[72:75], v[144:147], a[200:203], v[72:75]\n"
"v_exp_f32_e64 v150, -v150\n"
"v_mfma_f32_16x16x32_bf16 v[76:79], v[144:147], a[204:207], v[76:79]\n"
"v_exp_f32_e64 v151, -v151\n"
"v_mfma_f32_16x16x32_bf16 v[80:83], v[128:131], a[192:195], v[80:83]\n"
"ds_read_b64_tr_b16 v[140:141], v160 offset:51200\n"
"ds_read_b64_tr_b16 v[142:143], v160 offset:59392\n"
"v_mfma_f32_16x16x32_bf16 v[84:87], v[128:131], a[196:199], v[84:87]\n"
"v_mul_f32 v50, v18, v50\n"
"v_mul_f32 v51, v19, v51\n"
"v_mfma_f32_16x16x32_bf16 v[88:91], v[128:131], a[200:203], v[88:91]\n"
"v_add_f32 v150, 1.0, v150\n"
"v_add_f32 v151, 1.0, v151\n"
"v_mfma_f32_16x16x32_bf16 v[92:95], v[128:131], a[204:207], v[92:95]\n"
"s_waitcnt lgkmcnt(2)\n"
"v_mfma_f32_16x16x32_bf16 v[64:67], v[132:135], a[208:211], v[64:67]\n"
"ds_read_b64_tr_b16 v[144:145], v161 offset:51200\n"
"ds_read_b64_tr_b16 v[146:147], v161 offset:59392\n"
"v_mfma_f32_16x16x32_bf16 v[68:71], v[132:135], a[212:215], v[68:71]\n"
"s_add_u32 s66, s72, 73728\n"
"buffer_load_dwordx4 v175, s[40:43], s66 offen offset:2048 lds\n"
"v_mfma_f32_16x16x32_bf16 v[72:75], v[132:135], a[216:219], v[72:75]\n"
"v_rcp_f32 v150, v150\n"
"v_mfma_f32_16x16x32_bf16 v[76:79], v[132:135], a[220:223], v[76:79]\n"
"v_rcp_f32 v151, v151\n"
"v_mfma_f32_16x16x32_bf16 v[80:83], v[136:139], a[208:211], v[80:83]\n"
"ds_read_b64_tr_b16 v[128:129], v160 offset:52224\n"
"ds_read_b64_tr_b16 v[130:131], v160 offset:60416\n"
"v_mfma_f32_16x16x32_bf16 v[84:87], v[136:139], a[212:215], v[84:87]\n"
"v_mul_f32 v50, v50, v150\n"
"v_mul_f32 v51, v51, v151\n"
"v_mfma_f32_16x16x32_bf16 v[88:91], v[136:139], a[216:219], v[88:91]\n"
"v_cvt_pk_bf16_f32 v155, v50, v51\n"
"ds_write_b128 v156, v[152:155]\n"
"v_mfma_f32_16x16x32_bf16 v[92:95], v[136:139], a[220:223], v[92:95]\n"
"buffer_store_dwordx4 v[152:155], v158, s[48:51], s17 offen sc1\n"
"s_waitcnt lgkmcnt(3)\n"
"v_mfma_f32_16x16x32_bf16 v[64:67], v[140:143], a[224:227], v[64:67]\n"
"ds_read_b64_tr_b16 v[132:133], v161 offset:52224\n"
"ds_read_b64_tr_b16 v[134:135], v161 offset:60416\n"
"v_mfma_f32_16x16x32_bf16 v[68:71], v[140:143], a[228:231], v[68:71]\n"
"s_add_u32 s66, s72, 110592\n"
"buffer_load_dwordx4 v176, s[40:43], s66 offen offset:3072 lds\n"
"v_mfma_f32_16x16x32_bf16 v[72:75], v[140:143], a[232:235], v[72:75]\n"
"v_fma_f32 v148, v4, v4, s75\n"
"v_fma_f32 v149, v5, v5, s75\n"
"v_mfma_f32_16x16x32_bf16 v[76:79], v[140:143], a[236:239], v[76:79]\n"
"v_mul_f32 v148, v148, v4\n"
"v_mul_f32 v149, v149, v5\n"
"v_mfma_f32_16x16x32_bf16 v[80:83], v[144:147], a[224:227], v[80:83]\n"
"s_waitcnt vmcnt(6)\n"
"s_barrier\n"
"s_waitcnt vmcnt(5)\n"
"v_readfirstlane_b32 s18, v178\n"
"s_nop 1\n"
"s_cmp_ge_u32 s18, 1\n"
"s_cbranch_scc0 Lslow5_%=\n"
"Lback5_%=:\n"
"v_mfma_f32_16x16x32_bf16 v[84:87], v[144:147], a[228:231], v[84:87]\n"
"ds_read_b64_tr_b16 v[136:137], v160\n"
"ds_read_b64_tr_b16 v[138:139], v160 offset:8192\n"
"v_mfma_f32_16x16x32_bf16 v[88:91], v[144:147], a[232:235], v[88:91]\n"
"s_add_u32 m0, s65, 32768\n"
"s_add_u32 s66, s72, 147456\n"
"buffer_load_dwordx4 v173, s[40:43], s66 offen lds\n"
"v_mfma_f32_16x16x32_bf16 v[92:95], v[144:147], a[236:239], v[92:95]\n"
"s_add_u32 s66, s72, 184320\n"
"buffer_load_dwordx4 v174, s[40:43], s66 offen offset:1024 lds\n"
"s_waitcnt lgkmcnt(2)\n"
"v_mfma_f32_16x16x32_bf16 v[64:67], v[128:131], a[240:243], v[64:67]\n"
"ds_read_b64_tr_b16 v[140:141], v161\n"
"ds_read_b64_tr_b16 v[142:143], v161 offset:8192\n"
"v_mfma_f32_16x16x32_bf16 v[68:71], v[128:131], a[244:247], v[68:71]\n"
"s_add_u32 s66, s74, 0\n"
"buffer_load_dwordx4 v175, s[44:47], s66 offen offset:2048 lds\n"
"v_mfma_f32_16x16x32_bf16 v[72:75], v[128:131], a[248:251], v[72:75]\n"
"s_add_u32 s66, s74, 23040\n"
"buffer_load_dwordx4 v176, s[44:47], s66 offen offset:3072 lds\n"
"v_mfma_f32_16x16x32_bf16 v[76:79], v[128:131], a[252:255], v[76:79]\n"
"v_exp_f32_e64 v148, -v148\n"
"v_mfma_f32_16x16x32_bf16 v[80:83], v[132:135], a[240:243], v[80:83]\n"
"ds_read_b64_tr_b16 v[144:145], v160 offset:1024\n"
"ds_read_b64_tr_b16 v[146:147], v160 offset:9216\n"
"v_mfma_f32_16x16x32_bf16 v[84:87], v[132:135], a[244:247], v[84:87]\n"
"s_add_u32 m0, s65, 147456\n"
"s_add_u32 s66, s72, 128\n"
"buffer_load_dwordx4 v173, s[40:43], s66 offen lds\n"
"v_mfma_f32_16x16x32_bf16 v[88:91], v[132:135], a[248:251], v[88:91]\n"
"v_exp_f32_e64 v149, -v149\n"
"v_mfma_f32_16x16x32_bf16 v[92:95], v[132:135], a[252:255], v[92:95]\n"
"v_mul_f32 v36, v4, v36\n"
"s_waitcnt lgkmcnt(2)\n"
"v_mfma_f32_16x16x32_bf16 v[64:67], v[136:139], v[192:195], v[64:67]\n"
"ds_read_b64_tr_b16 v[128:129], v161 offset:1024\n"
"ds_read_b64_tr_b16 v[130:131], v161 offset:9216\n"
"v_mfma_f32_16x16x32_bf16 v[68:71], v[136:139], v[196:199], v[68:71]\n"
"s_add_u32 s66, s72, 36992\n"
"buffer_load_dwordx4 v174, s[40:43], s66 offen offset:1024 lds\n"
"v_mfma_f32_16x16x32_bf16 v[72:75], v[136:139], v[200:203], v[72:75]\n"
"v_mul_f32 v37, v5, v37\n"
"v_add_f32 v148, 1.0, v148\n"
"v_mfma_f32_16x16x32_bf16 v[76:79], v[136:139], v[204:207], v[76:79]\n"
"v_add_f32 v149, 1.0, v149\n"
"v_fma_f32 v150, v6, v6, s75\n"
"v_mfma_f32_16x16x32_bf16 v[80:83], v[140:143], v[192:195], v[80:83]\n"
"s_waitcnt vmcnt(6)\n"
"s_barrier\n"
"v_mfma_f32_16x16x32_bf16 v[84:87], v[140:143], v[196:199], v[84:87]\n"
"ds_read_b64_tr_b16 v[132:133], v160 offset:16384\n"
"ds_read_b64_tr_b16 v[134:135], v160 offset:24576\n"
"v_mfma_f32_16x16x32_bf16 v[88:91], v[140:143], v[200:203], v[88:91]\n"
"s_add_u32 s66, s72, 73856\n"
"buffer_load_dwordx4 v175, s[40:43], s66 offen offset:2048 lds\n"
"v_mfma_f32_16x16x32_bf16 v[92:95], v[140:143], v[204:207], v[92:95]\n"
"s_add_u32 s66, s72, 110720\n"
"buffer_load_dwordx4 v176, s[40:43], s66 offen offset:3072 lds\n"
"s_waitcnt lgkmcnt(2)\n"
"v_mfma_f32_16x16x32_bf16 v[64:67], v[144:147], v[208:211], v[64:67]\n"
"ds_read_b64_tr_b16 v[136:137], v161 offset:16384\n"
"ds_read_b64_tr_b16 v[138:139], v161 offset:24576\n"
"v_mfma_f32_16x16x32_bf16 v[68:71], v[144:147], v[212:215], v[68:71]\n"
"s_add_u32 m0, s65, 98304\n"
"s_add_u32 s66, s6, 4294964224\n"
"buffer_load_dwordx4 v166, s[48:51], s66 offen sc1 lds\n"
"v_mfma_f32_16x16x32_bf16 v[72:75], v[144:147], v[216:219], v[72:75]\n"
"v_rcp_f32 v148, v148\n"
"v_mfma_f32_16x16x32_bf16 v[76:79], v[144:147], v[220:223], v[76:79]\n"
"v_rcp_f32 v149, v149\n"
"v_mfma_f32_16x16x32_bf16 v[80:83], v[128:131], v[208:211], v[80:83]\n"
"ds_read_b64_tr_b16 v[140:141], v160 offset:17408\n"
"ds_read_b64_tr_b16 v[142:143], v160 offset:25600\n"
"v_mfma_f32_16x16x32_bf16 v[84:87], v[128:131], v[212:215], v[84:87]\n"
"s_add_u32 s66, s6, 0\n"
"buffer_load_dwordx4 v166, s[48:51], s66 offen offset:1024 sc1 lds\n"
"v_mfma_f32_16x16x32_bf16 v[88:91], v[128:131], v[216:219], v[88:91]\n"
"v_mul_f32 v36, v36, v148\n"
"v_mul_f32 v37, v37, v149\n"
"v_mfma_f32_16x16x32_bf16 v[92:95], v[128:131], v[220:223], v[92:95]\n"
"v_cvt_pk_bf16_f32 v152, v36, v37\n"
"s_waitcnt lgkmcnt(2)\n"
"v_mfma_f32_16x16x32_bf16 v[96:99], v[132:135], a[0:3], 0\n"
"ds_read_b64_tr_b16 v[144:145], v161 offset:17408\n"
"ds_read_b64_tr_b16 v[146:147], v161 offset:25600\n"
"v_mfma_f32_16x16x32_bf16 v[100:103], v[132:135], a[4:7], 0\n"
"s_add_u32 s66, s6, 3072\n"
"buffer_load_dwordx4 v166, s[48:51], s66 offen offset:2048 sc1 lds\n"
"v_mfma_f32_16x16x32_bf16 v[104:107], v[132:135], a[8:11], 0\n"
"v_fma_f32 v151, v7, v7, s75\n"
"v_mul_f32 v150, v150, v6\n"
"v_mfma_f32_16x16x32_bf16 v[108:111], v[132:135], a[12:15], 0\n"
"v_mul_f32 v151, v151, v7\n"
"v_mul_f32 v38, v6, v38\n"
"v_mfma_f32_16x16x32_bf16 v[112:115], v[136:139], a[0:3], 0\n"
"ds_read_b64_tr_b16 v[128:129], v160 offset:18432\n"
"ds_read_b64_tr_b16 v[130:131], v160 offset:26624\n"
"v_mfma_f32_16x16x32_bf16 v[116:119], v[136:139], a[4:7], 0\n"
"s_add_u32 s66, s6, 6144\n"
"buffer_load_dwordx4 v166, s[48:51], s66 offen offset:3072 sc1 lds\n"
"v_mfma_f32_16x16x32_bf16 v[120:123], v[136:139], a[8:11], 0\n"
"v_exp_f32_e64 v150, -v150\n"
"v_mfma_f32_16x16x32_bf16 v[124:127], v[136:139], a[12:15], 0\n"
"v_mul_f32 v39, v7, v39\n"
"s_waitcnt lgkmcnt(2)\n"
"v_mfma_f32_16x16x32_bf16 v[96:99], v[140:143], a[16:19], v[96:99]\n"
"ds_read_b64_tr_b16 v[132:133], v161 offset:18432\n"
"ds_read_b64_tr_b16 v[134:135], v161 offset:26624\n"
"v_mfma_f32_16x16x32_bf16 v[100:103], v[140:143], a[20:23], v[100:103]\n"
"v_exp_f32_e64 v151, -v151\n"
"v_mfma_f32_16x16x32_bf16 v[104:107], v[140:143], a[24:27], v[104:107]\n"
"v_add_f32 v150, 1.0, v150\n"
"v_add_f32 v151, 1.0, v151\n"
"v_mfma_f32_16x16x32_bf16 v[108:111], v[140:143], a[28:31], v[108:111]\n"
"v_rcp_f32 v150, v150\n"
"v_mfma_f32_16x16x32_bf16 v[112:115], v[144:147], a[16:19], v[112:115]\n"
"ds_read_b64_tr_b16 v[136:137], v160 offset:19456\n"
"ds_read_b64_tr_b16 v[138:139], v160 offset:27648\n"
"v_mfma_f32_16x16x32_bf16 v[116:119], v[144:147], a[20:23], v[116:119]\n"
"s_add_u32 m0, s65, 49152\n"
"s_add_u32 s66, s72, 147584\n"
"buffer_load_dwordx4 v173, s[40:43], s66 offen lds\n"
"v_mfma_f32_16x16x32_bf16 v[120:123], v[144:147], a[24:27], v[120:123]\n"
"v_rcp_f32 v151, v151\n"
"v_mfma_f32_16x16x32_bf16 v[124:127], v[144:147], a[28:31], v[124:127]\n"
"v_mul_f32 v38, v38, v150\n"
"s_waitcnt lgkmcnt(2)\n"
"v_mfma_f32_16x16x32_bf16 v[96:99], v[128:131], a[32:35], v[96:99]\n"
"ds_read_b64_tr_b16 v[140:141], v161 offset:19456\n"
"ds_read_b64_tr_b16 v[142:143], v161 offset:27648\n"
"v_mfma_f32_16x16x32_bf16 v[100:103], v[128:131], a[36:39], v[100:103]\n"
"v_mul_f32 v39, v39, v151\n"
"v_cvt_pk_bf16_f32 v153, v38, v39\n"
"v_mfma_f32_16x16x32_bf16 v[104:107], v[128:131], a[40:43], v[104:107]\n"
"v_fma_f32 v148, v20, v20, s75\n"
"v_fma_f32 v149, v21, v21, s75\n"
"v_mfma_f32_16x16x32_bf16 v[108:111], v[128:131], a[44:47], v[108:111]\n"
"v_mul_f32 v148, v148, v20\n"
"v_mul_f32 v149, v149, v21\n"
"v_mfma_f32_16x16x32_bf16 v[112:115], v[132:135], a[32:35], v[112:115]\n"
"s_waitcnt vmcnt(9)\n"
"s_barrier\n"
"v_mfma_f32_16x16x32_bf16 v[116:119], v[132:135], a[36:39], v[116:119]\n"
"ds_read_b64_tr_b16 v[144:145], v160 offset:32768\n"
"ds_read_b64_tr_b16 v[146:147], v160 offset:40960\n"
"v_mfma_f32_16x16x32_bf16 v[120:123], v[132:135], a[40:43], v[120:123]\n"
"s_add_u32 s66, s72, 184448\n"
"buffer_load_dwordx4 v174, s[40:43], s66 offen offset:1024 lds\n"
"v_mfma_f32_16x16x32_bf16 v[124:127], v[132:135], a[44:47], v[124:127]\n"
"s_add_u32 s66, s74, 128\n"
"buffer_load_dwordx4 v175, s[44:47], s66 offen offset:2048 lds\n"
"s_waitcnt lgkmcnt(2)\n"
"v_mfma_f32_16x16x32_bf16 v[96:99], v[136:139], a[48:51], v[96:99]\n"
"ds_read_b64_tr_b16 v[128:129], v161 offset:32768\n"
"ds_read_b64_tr_b16 v[130:131], v161 offset:40960\n"
"v_mfma_f32_16x16x32_bf16 v[100:103], v[136:139], a[52:55], v[100:103]\n"
"s_add_u32 s66, s74, 23168\n"
"buffer_load_dwordx4 v176, s[44:47], s66 offen offset:3072 lds\n"
"v_mfma_f32_16x16x32_bf16 v[104:107], v[136:139], a[56:59], v[104:107]\n"
"v_exp_f32_e64 v148, -v148\n"
"v_mfma_f32_16x16x32_bf16 v[108:111], v[136:139], a[60:63], v[108:111]\n"
"v_exp_f32_e64 v149, -v149\n"
"v_mfma_f32_16x16x32_bf16 v[112:115], v[140:143], a[48:51], v[112:115]\n"
"ds_read_b64_tr_b16 v[132:133], v160 offset:33792\n"
"ds_read_b64_tr_b16 v[134:135], v160 offset:41984\n"
"v_mfma_f32_16x16x32_bf16 v[116:119], v[140:143], a[52:55], v[116:119]\n"
"s_add_u32 m0, s65, 0\n"
"s_add_u32 s66, s72, 221184\n"
"buffer_load_dwordx4 v173, s[40:43], s66 offen lds\n"
"v_mfma_f32_16x16x32_bf16 v[120:123], v[140:143], a[56:59], v[120:123]\n"
"v_mul_f32 v52, v20, v52\n"
"v_mul_f32 v53, v21, v53\n"
"v_mfma_f32_16x16x32_bf16 v[124:127], v[140:143], a[60:63], v[124:127]\n"
"v_add_f32 v148, 1.0, v148\n"
"s_waitcnt lgkmcnt(2)\n"
"v_mfma_f32_16x16x32_bf16 v[96:99], v[144:147], a[64:67], v[96:99]\n"
"ds_read_b64_tr_b16 v[136:137], v161 offset:33792\n"
"ds_read_b64_tr_b16 v[138:139], v161 offset:41984\n"
"v_mfma_f32_16x16x32_bf16 v[100:103], v[144:147], a[68:71], v[100:103]\n"
"s_add_u32 s66, s72, 258048\n"
"buffer_load_dwordx4 v174, s[40:43], s66 offen offset:1024 lds\n"
"v_mfma_f32_16x16x32_bf16 v[104:107], v[144:147], a[72:75], v[104:107]\n"
"v_add_f32 v149, 1.0, v149\n"
"v_fma_f32 v150, v22, v22, s75\n"
"v_mfma_f32_16x16x32_bf16 v[108:111], v[144:147], a[76:79], v[108:111]\n"
"v_rcp_f32 v148, v148\n"
"v_mfma_f32_16x16x32_bf16 v[112:115], v[128:131], a[64:67], v[112:115]\n"
"ds_read_b64_tr_b16 v[140:141], v160 offset:34816\n"
"ds_read_b64_tr_b16 v[142:143], v160 offset:43008\n"
"v_mfma_f32_16x16x32_bf16 v[116:119], v[128:131], a[68:71], v[116:119]\n"
"s_add_u32 s66, s73, 0\n"
"buffer_load_dwordx4 v175, s[40:43], s66 offen offset:2048 lds\n"
"v_mfma_f32_16x16x32_bf16 v[120:123], v[128:131], a[72:75], v[120:123]\n"
"v_rcp_f32 v149, v149\n"
"v_mfma_f32_16x16x32_bf16 v[124:127], v[128:131], a[76:79], v[124:127]\n"
"v_mul_f32 v52, v52, v148\n"
"s_waitcnt lgkmcnt(2)\n"
"v_mfma_f32_16x16x32_bf16 v[96:99], v[132:135], a[80:83], v[96:99]\n"
"ds_read_b64_tr_b16 v[144:145], v161 offset:34816\n"
"ds_read_b64_tr_b16 v[146:147], v161 offset:43008\n"
"v_mfma_f32_16x16x32_bf16 v[100:103], v[132:135], a[84:87], v[100:103]\n"
"s_add_u32 s66, s72, 221312\n"
"buffer_load_dwordx4 v176, s[40:43], s66 offen offset:3072 lds\n"
"v_mfma_f32_16x16x32_bf16 v[104:107], v[132:135], a[88:91], v[104:107]\n"
"v_mul_f32 v53, v53, v149\n"
"v_cvt_pk_bf16_f32 v154, v52, v53\n"
"v_mfma_f32_16x16x32_bf16 v[108:111], v[132:135], a[92:95], v[108:111]\n"
"v_fma_f32 v151, v23, v23, s75\n"
"v_mul_f32 v150, v150, v22\n"
"v_mfma_f32_16x16x32_bf16 v[112:115], v[136:139], a[80:83], v[112:115]\n"
"ds_read_b64_tr_b16 v[128:129], v160 offset:35840\n"
"ds_read_b64_tr_b16 v[130:131], v160 offset:44032\n"
"v_mfma_f32_16x16x32_bf16 v[116:119], v[136:139], a[84:87], v[116:119]\n"
"v_mul_f32 v151, v151, v23\n"
"v_mul_f32 v54, v22, v54\n"
"v_mfma_f32_16x16x32_bf16 v[120:123], v[136:139], a[88:91], v[120:123]\n"
"v_exp_f32_e64 v150, -v150\n"
"v_mfma_f32_16x16x32_bf16 v[124:127], v[136:139], a[92:95], v[124:127]\n"
"v_mul_f32 v55, v23, v55\n"
"s_waitcnt lgkmcnt(2)\n"
"v_mfma_f32_16x16x32_bf16 v[96:99], v[140:143], a[96:99], v[96:99]\n"
"ds_read_b64_tr_b16 v[132:133], v161 offset:35840\n"
"ds_read_b64_tr_b16 v[134:135], v161 offset:44032\n"
"v_mfma_f32_16x16x32_bf16 v[100:103], v[140:143], a[100:103], v[100:103]\n"
"s_add_u32 m0, s65, 114688\n"
"s_add_u32 s66, s6, 13312\n"
"buffer_load_dwordx4 v166, s[48:51], s66 offen sc1 lds\n"
"v_mfma_f32_16x16x32_bf16 v[104:107], v[140:143], a[104:107], v[104:107]\n"
"v_exp_f32_e64 v151, -v151\n"
"v_mfma_f32_16x16x32_bf16 v[108:111], v[140:143], a[108:111], v[108:111]\n"
"v_add_f32 v150, 1.0, v150\n"
"v_add_f32 v151, 1.0, v151\n"
"v_mfma_f32_16x16x32_bf16 v[112:115], v[144:147], a[96:99], v[112:115]\n"
"s_waitcnt vmcnt(9)\n"
"s_barrier\n"
"v_mfma_f32_16x16x32_bf16 v[116:119], v[144:147], a[100:103], v[116:119]\n"
"v_add_u32 v180, 98304, v167\n"
"ds_read_b128 a[128:131], v180\n"
"v_mfma_f32_16x16x32_bf16 v[120:123], v[144:147], a[104:107], v[120:123]\n"
"ds_read_b128 a[132:135], v180 offset:4096\n"
"ds_read_b128 a[136:139], v180 offset:8192\n"
"v_mfma_f32_16x16x32_bf16 v[124:127], v[144:147], a[108:111], v[124:127]\n"
"ds_read_b128 a[140:143], v180 offset:12288\n"
"s_waitcnt lgkmcnt(4)\n"
"v_mfma_f32_16x16x32_bf16 v[96:99], v[128:131], a[112:115], v[96:99]\n"
"ds_read_b64_tr_b16 v[136:137], v164 offset:16384\n"
"ds_read_b64_tr_b16 v[138:139], v164 offset:24576\n"
"v_mfma_f32_16x16x32_bf16 v[100:103], v[128:131], a[116:119], v[100:103]\n"
"v_add_u32 v180, 98304, v167\n"
"ds_read_b128 a[144:147], v180 offset:2048\n"
"v_mfma_f32_16x16x32_bf16 v[104:107], v[128:131], a[120:123], v[104:107]\n"
"ds_read_b128 a[148:151], v180 offset:6144\n"
"ds_read_b128 a[152:155], v180 offset:10240\n"
"v_mfma_f32_16x16x32_bf16 v[108:111], v[128:131], a[124:127], v[108:111]\n"
"ds_read_b128 a[156:159], v180 offset:14336\n"
"ds_read_b64_tr_b16 v[140:141], v165 offset:16384\n"
"v_mfma_f32_16x16x32_bf16 v[112:115], v[132:135], a[112:115], v[112:115]\n"
"ds_read_b64_tr_b16 v[142:143], v165 offset:24576\n"
"ds_read_b64_tr_b16 v[144:145], v164 offset:17408\n"
"v_mfma_f32_16x16x32_bf16 v[116:119], v[132:135], a[116:119], v[116:119]\n"
"ds_read_b64_tr_b16 v[146:147], v164 offset:25600\n"
"s_add_u32 s66, s6, 16384\n"
"buffer_load_dwordx4 v166, s[48:51], s66 offen offset:1024 sc1 lds\n"
"v_mfma_f32_16x16x32_bf16 v[120:123], v[132:135], a[120:123], v[120:123]\n"
"v_rcp_f32 v150, v150\n"
"v_mfma_f32_16x16x32_bf16 v[124:127], v[132:135], a[124:127], v[124:127]\n"
"v_mul_f32 v54, v54, v150\n"
"s_waitcnt lgkmcnt(2)\n"
"v_mfma_f32_16x16x32_bf16 v[192:195], v[136:139], a[0:3], 0\n"
"ds_read_b64_tr_b16 v[128:129], v165 offset:17408\n"
"ds_read_b64_tr_b16 v[130:131], v165 offset:25600\n"
"v_mfma_f32_16x16x32_bf16 v[196:199], v[136:139], a[4:7], 0\n"
"s_add_u32 s66, s6, 19456\n"
"buffer_load_dwordx4 v166, s[48:51], s66 offen offset:2048 sc1 lds\n"
"v_mfma_f32_16x16x32_bf16 v[200:203], v[136:139], a[8:11], 0\n"
"v_rcp_f32 v151, v151\n"
"v_mfma_f32_16x16x32_bf16 v[204:207], v[136:139], a[12:15], 0\n"
"v_mul_f32 v55, v55, v151\n"
"v_cvt_pk_bf16_f32 v155, v54, v55\n"
"v_mfma_f32_16x16x32_bf16 v[208:211], v[140:143], a[0:3], 0\n"
"ds_read_b64_tr_b16 v[132:133], v164 offset:18432\n"
"ds_read_b64_tr_b16 v[134:135], v164 offset:26624\n"
"v_mfma_f32_16x16x32_bf16 v[212:215], v[140:143], a[4:7], 0\n"
"s_add_u32 s66, s6, 22528\n"
"buffer_load_dwordx4 v166, s[48:51], s66 offen offset:3072 sc1 lds\n"
"v_mfma_f32_16x16x32_bf16 v[216:219], v[140:143], a[8:11], 0\n"
"ds_write_b128 v156, v[152:155] offset:1024\n"
"buffer_store_dwordx4 v[152:155], v158, s[48:51], s17 offen offset:1024 sc1\n"
"v_mfma_f32_16x16x32_bf16 v[220:223], v[140:143], a[12:15], 0\n"
"v_fma_f32 v148, v8, v8, s75\n"
"s_waitcnt lgkmcnt(3)\n"
"v_mfma_f32_16x16x32_bf16 v[192:195], v[144:147], a[16:19], v[192:195]\n"
"ds_read_b64_tr_b16 v[136:137], v165 offset:18432\n"
"ds_read_b64_tr_b16 v[138:139], v165 offset:26624\n"
"v_mfma_f32_16x16x32_bf16 v[196:199], v[144:147], a[20:23], v[196:199]\n"
"v_fma_f32 v149, v9, v9, s75\n"
"v_mul_f32 v148, v148, v8\n"
"v_mfma_f32_16x16x32_bf16 v[200:203], v[144:147], a[24:27], v[200:203]\n"
"v_mul_f32 v149, v149, v9\n"
"v_mul_f32 v40, v8, v40\n"
"v_mfma_f32_16x16x32_bf16 v[204:207], v[144:147], a[28:31], v[204:207]\n"
"v_exp_f32_e64 v148, -v148\n"
"v_mfma_f32_16x16x32_bf16 v[208:211], v[128:131], a[16:19], v[208:211]\n"
"ds_read_b64_tr_b16 v[140:141], v164 offset:19456\n"
"ds_read_b64_tr_b16 v[142:143], v164 offset:27648\n"
"v_mfma_f32_16x16x32_bf16 v[212:215], v[128:131], a[20:23], v[212:215]\n"
"s_add_u32 m0, s65, 16384\n"
"s_add_u32 s66, s72, 258176\n"
"buffer_load_dwordx4 v173, s[40:43], s66 offen lds\n"
"v_mfma_f32_16x16x32_bf16 v[216:219], v[128:131], a[24:27], v[216:219]\n"
"v_exp_f32_e64 v149, -v149\n"
"v_mfma_f32_16x16x32_bf16 v[220:223], v[128:131], a[28:31], v[220:223]\n"
"v_mul_f32 v41, v9, v41\n"
"s_waitcnt lgkmcnt(2)\n"
"v_mfma_f32_16x16x32_bf16 v[192:195], v[132:135], a[32:35], v[192:195]\n"
"ds_read_b64_tr_b16 v[144:145], v165 offset:19456\n"
"ds_read_b64_tr_b16 v[146:147], v165 offset:27648\n"
"v_mfma_f32_16x16x32_bf16 v[196:199], v[132:135], a[36:39], v[196:199]\n"
"v_add_f32 v148, 1.0, v148\n"
"v_add_f32 v149, 1.0, v149\n"
"v_mfma_f32_16x16x32_bf16 v[200:203], v[132:135], a[40:43], v[200:203]\n"
"v_rcp_f32 v148, v148\n"
"v_mfma_f32_16x16x32_bf16 v[204:207], v[132:135], a[44:47], v[204:207]\n"
"v_rcp_f32 v149, v149\n"
"v_mfma_f32_16x16x32_bf16 v[208:211], v[136:139], a[32:35], v[208:211]\n"
"s_waitcnt vmcnt(10)\n"
"s_barrier\n"
"v_mfma_f32_16x16x32_bf16 v[212:215], v[136:139], a[36:39], v[212:215]\n"
"ds_read_b64_tr_b16 v[128:129], v160 offset:49152\n"
"ds_read_b64_tr_b16 v[130:131], v160 offset:57344\n"
"v_mfma_f32_16x16x32_bf16 v[216:219], v[136:139], a[40:43], v[216:219]\n"
"s_add_u32 s66, s73, 128\n"
"buffer_load_dwordx4 v174, s[40:43], s66 offen offset:1024 lds\n"
"v_mfma_f32_16x16x32_bf16 v[220:223], v[136:139], a[44:47], v[220:223]\n"
"s_add_u32 s66, s72, 256\n"
"buffer_load_dwordx4 v175, s[40:43], s66 offen offset:2048 lds\n"
"s_waitcnt lgkmcnt(2)\n"
"v_mfma_f32_16x16x32_bf16 v[192:195], v[140:143], a[48:51], v[192:195]\n"
"ds_read_b64_tr_b16 v[132:133], v161 offset:49152\n"
"ds_read_b64_tr_b16 v[134:135], v161 offset:57344\n"
"v_mfma_f32_16x16x32_bf16 v[196:199], v[140:143], a[52:55], v[196:199]\n"
"s_add_u32 s66, s72, 37120\n"
"buffer_load_dwordx4 v176, s[40:43], s66 offen offset:3072 lds\n"
"v_mfma_f32_16x16x32_bf16 v[200:203], v[140:143], a[56:59], v[200:203]\n"
"v_mul_f32 v40, v40, v148\n"
"v_mul_f32 v41, v41, v149\n"
"v_mfma_f32_16x16x32_bf16 v[204:207], v[140:143], a[60:63], v[204:207]\n"
"v_cvt_pk_bf16_f32 v152, v40, v41\n"
"v_fma_f32 v150, v10, v10, s75\n"
"v_mfma_f32_16x16x32_bf16 v[208:211], v[144:147], a[48:51], v[208:211]\n"
"ds_read_b64_tr_b16 v[136:137], v160 offset:50176\n"
"ds_read_b64_tr_b16 v[138:139], v160 offset:58368\n"
"v_mfma_f32_16x16x32_bf16 v[212:215], v[144:147], a[52:55], v[212:215]\n"
"s_add_u32 m0, s65, 32768\n"
"s_add_u32 s66, s72, 73984\n"
"buffer_load_dwordx4 v173, s[40:43], s66 offen lds\n"
"v_mfma_f32_16x16x32_bf16 v[216:219], v[144:147], a[56:59], v[216:219]\n"
"v_fma_f32 v151, v11, v11, s75\n"
"v_mul_f32 v150, v150, v10\n"
"v_mfma_f32_16x16x32_bf16 v[220:223], v[144:147], a[60:63], v[220:223]\n"
"v_mul_f32 v151, v151, v11\n"
"s_waitcnt lgkmcnt(2)\n"
"v_mfma_f32_16x16x32_bf16 v[192:195], v[128:131], a[64:67], v[192:195]\n"
"ds_read_b64_tr_b16 v[140:141], v161 offset:50176\n"
"ds_read_b64_tr_b16 v[142:143], v161 offset:58368\n"
"v_mfma_f32_16x16x32_bf16 v[196:199], v[128:131], a[68:71], v[196:199]\n"
"s_add_u32 s66, s72, 110848\n"
"buffer_load_dwordx4 v174, s[40:43], s66 offen offset:1024 lds\n"
"v_mfma_f32_16x16x32_bf16 v[200:203], v[128:131], a[72:75], v[200:203]\n"
"v_exp_f32_e64 v150, -v150\n"
"v_mfma_f32_16x16x32_bf16 v[204:207], v[128:131], a[76:79], v[204:207]\n"
"v_exp_f32_e64 v151, -v151\n"
"v_mfma_f32_16x16x32_bf16 v[208:211], v[132:135], a[64:67], v[208:211]\n"
"ds_read_b64_tr_b16 v[144:145], v160 offset:51200\n"
"ds_read_b64_tr_b16 v[146:147], v160 offset:59392\n"
"v_mfma_f32_16x16x32_bf16 v[212:215], v[132:135], a[68:71], v[212:215]\n"
"s_add_u32 s66, s72, 147712\n"
"buffer_load_dwordx4 v175, s[40:43], s66 offen offset:2048 lds\n"
"v_mfma_f32_16x16x32_bf16 v[216:219], v[132:135], a[72:75], v[216:219]\n"
"v_mul_f32 v42, v10, v42\n"
"v_mul_f32 v43, v11, v43\n"
"v_mfma_f32_16x16x32_bf16 v[220:223], v[132:135], a[76:79], v[220:223]\n"
"v_add_f32 v150, 1.0, v150\n"
"s_waitcnt lgkmcnt(2)\n"
"v_mfma_f32_16x16x32_bf16 v[192:195], v[136:139], a[80:83], v[192:195]\n"
"ds_read_b64_tr_b16 v[128:129], v161 offset:51200\n"
"ds_read_b64_tr_b16 v[130:131], v161 offset:59392\n"
"v_mfma_f32_16x16x32_bf16 v[196:199], v[136:139], a[84:87], v[196:199]\n"
"s_add_u32 s66, s72, 184576\n"
"buffer_load_dwordx4 v176, s[40:43], s66 offen offset:3072 lds\n"
"v_mfma_f32_16x16x32_bf16 v[200:203], v[136:139], a[88:91], v[200:203]\n"
"v_add_f32 v151, 1.0, v151\n"
"v_fma_f32 v148, v24, v24, s75\n"
"v_mfma_f32_16x16x32_bf16 v[204:207], v[136:139], a[92:95], v[204:207]\n"
"v_rcp_f32 v150, v150\n"
"v_mfma_f32_16x16x32_bf16 v[208:211], v[140:143], a[80:83], v[208:211]\n"
"ds_read_b64_tr_b16 v[132:133], v160 offset:52224\n"
"ds_read_b64_tr_b16 v[134:135], v160 offset:60416\n"
"v_mfma_f32_16x16x32_bf16 v[212:215], v[140:143], a[84:87], v[212:215]\n"
"v_rcp_f32 v151, v151\n"
"v_mfma_f32_16x16x32_bf16 v[216:219], v[140:143], a[88:91], v[216:219]\n"
"v_mul_f32 v42, v42, v150\n"
"v_mul_f32 v43, v43, v151\n"
"v_mfma_f32_16x16x32_bf16 v[220:223], v[140:143], a[92:95], v[220:223]\n"
"v_cvt_pk_bf16_f32 v153, v42, v43\n"
"s_waitcnt lgkmcnt(2)\n"
"v_mfma_f32_16x16x32_bf16 v[192:195], v[144:147], a[96:99], v[192:195]\n"
"ds_read_b64_tr_b16 v[136:137], v161 offset:52224\n"
"ds_read_b64_tr_b16 v[138:139], v161 offset:60416\n"
"v_mfma_f32_16x16x32_bf16 v[196:199], v[144:147], a[100:103], v[196:199]\n"
"s_add_u32 m0, s65, 131072\n"
"s_add_u32 s66, s6, 29696\n"
"buffer_load_dwordx4 v166, s[48:51], s66 offen sc1 lds\n"
"v_mfma_f32_16x16x32_bf16 v[200:203], v[144:147], a[104:107], v[200:203]\n"
"v_fma_f32 v149, v25, v25, s75\n"
"v_mul_f32 v148, v148, v24\n"
"v_mfma_f32_16x16x32_bf16 v[204:207], v[144:147], a[108:111], v[204:207]\n"
"v_mul_f32 v149, v149, v25\n"
"v_mul_f32 v56, v24, v56\n"
"v_mfma_f32_16x16x32_bf16 v[208:211], v[128:131], a[96:99], v[208:211]\n"
"v_exp_f32_e64 v148, -v148\n"
"v_exp_f32_e64 v149, -v149\n"
"v_mul_f32 v57, v25, v57\n"
"v_add_f32 v148, 1.0, v148\n"
"v_add_f32 v149, 1.0, v149\n"
"v_rcp_f32 v148, v148\n"
"v_rcp_f32 v149, v149\n"
"v_mul_f32 v56, v56, v148\n"
"v_mul_f32 v57, v57, v149\n"
"v_cvt_pk_bf16_f32 v154, v56, v57\n"
"v_fma_f32 v150, v26, v26, s75\n"
"v_fma_f32 v151, v27, v27, s75\n"
"v_mul_f32 v150, v150, v26\n"
"v_mul_f32 v151, v151, v27\n"
"v_exp_f32_e64 v150, -v150\n"
"v_exp_f32_e64 v151, -v151\n"
"v_mul_f32 v58, v26, v58\n"
"v_mul_f32 v59, v27, v59\n"
"v_add_f32 v150, 1.0, v150\n"
"v_add_f32 v151, 1.0, v151\n"
"v_rcp_f32 v150, v150\n"
"v_rcp_f32 v151, v151\n"
"v_mul_f32 v58, v58, v150\n"
"v_mul_f32 v59, v59, v151\n"
"v_cvt_pk_bf16_f32 v155, v58, v59\n"
"ds_write_b128 v156, v[152:155] offset:2048\n"
"buffer_store_dwordx4 v[152:155], v158, s[48:51], s17 offen offset:2048 sc1\n"
"v_fma_f32 v148, v12, v12, s75\n"
"v_fma_f32 v149, v13, v13, s75\n"
"v_mul_f32 v148, v148, v12\n"
"v_mul_f32 v149, v149, v13\n"
"v_exp_f32_e64 v148, -v148\n"
"v_exp_f32_e64 v149, -v149\n"
"v_mul_f32 v44, v12, v44\n"
"v_mul_f32 v45, v13, v45\n"
"v_add_f32 v148, 1.0, v148\n"
"v_add_f32 v149, 1.0, v149\n"
"v_rcp_f32 v148, v148\n"
"v_rcp_f32 v149, v149\n"
"v_mul_f32 v44, v44, v148\n"
"v_mul_f32 v45, v45, v149\n"
"v_cvt_pk_bf16_f32 v152, v44, v45\n"
"v_fma_f32 v150, v14, v14, s75\n"
"v_fma_f32 v151, v15, v15, s75\n"
"v_mul_f32 v150, v150, v14\n"
"v_mul_f32 v151, v151, v15\n"
"v_exp_f32_e64 v150, -v150\n"
"v_exp_f32_e64 v151, -v151\n"
"v_mul_f32 v46, v14, v46\n"
"v_mul_f32 v47, v15, v47\n"
"v_add_f32 v150, 1.0, v150\n"
"v_add_f32 v151, 1.0, v151\n"
"v_rcp_f32 v150, v150\n"
"v_rcp_f32 v151, v151\n"
"v_mul_f32 v46, v46, v150\n"
"v_mul_f32 v47, v47, v151\n"
"v_cvt_pk_bf16_f32 v153, v46, v47\n"
"v_fma_f32 v148, v28, v28, s75\n"
"v_fma_f32 v149, v29, v29, s75\n"
"v_mul_f32 v148, v148, v28\n"
"v_mul_f32 v149, v149, v29\n"
"v_exp_f32_e64 v148, -v148\n"
"v_exp_f32_e64 v149, -v149\n"
"v_mul_f32 v60, v28, v60\n"
"v_mul_f32 v61, v29, v61\n"
"v_add_f32 v148, 1.0, v148\n"
"v_add_f32 v149, 1.0, v149\n"
"v_rcp_f32 v148, v148\n"
"v_rcp_f32 v149, v149\n"
"v_mul_f32 v60, v60, v148\n"
"v_mul_f32 v61, v61, v149\n"
"v_cvt_pk_bf16_f32 v154, v60, v61\n"
"v_fma_f32 v150, v30, v30, s75\n"
"v_fma_f32 v151, v31, v31, s75\n"
"v_mul_f32 v150, v150, v30\n"
"v_mul_f32 v151, v151, v31\n"
"v_exp_f32_e64 v150, -v150\n"
"v_exp_f32_e64 v151, -v151\n"
"v_mul_f32 v62, v30, v62\n"
"v_mul_f32 v63, v31, v63\n"
"v_add_f32 v150, 1.0, v150\n"
"v_add_f32 v151, 1.0, v151\n"
"v_rcp_f32 v150, v150\n"
"v_rcp_f32 v151, v151\n"
"v_mul_f32 v62, v62, v150\n"
"v_mul_f32 v63, v63, v151\n"
"v_cvt_pk_bf16_f32 v155, v62, v63\n"
"ds_write_b128 v156, v[152:155] offset:3072\n"
"buffer_store_dwordx4 v[152:155], v158, s[48:51], s17 offen offset:3072 sc1\n"
"s_lshl_b32 s16, s61, 12\n"
"s_lshl_b32 s17, s62, 3\n"
"s_add_u32 s16, s16, s17\n"
"s_add_u32 s16, s16, 65536\n"
"v_add_u32 v156, s16, v179\n"
"s_lshl_b32 s17, s62, 3\n"
"s_add_u32 s17, s17, s27\n"
"s_add_u32 s17, s17, 65536\n"
"v_fma_f32 v148, v64, v64, s75\n"
"v_fma_f32 v149, v65, v65, s75\n"
"v_mul_f32 v148, v148, v64\n"
"v_mul_f32 v149, v149, v65\n"
"v_exp_f32_e64 v148, -v148\n"
"v_exp_f32_e64 v149, -v149\n"
"v_mul_f32 v80, v64, v80\n"
"v_mul_f32 v81, v65, v81\n"
"v_add_f32 v148, 1.0, v148\n"
"v_add_f32 v149, 1.0, v149\n"
"v_rcp_f32 v148, v148\n"
"v_rcp_f32 v149, v149\n"
"v_mul_f32 v80, v80, v148\n"
"v_mul_f32 v81, v81, v149\n"
"v_cvt_pk_bf16_f32 v152, v80, v81\n"
"v_fma_f32 v150, v66, v66, s75\n"
"v_fma_f32 v151, v67, v67, s75\n"
"v_mul_f32 v150, v150, v66\n"
"v_mul_f32 v151, v151, v67\n"
"v_exp_f32_e64 v150, -v150\n"
"v_exp_f32_e64 v151, -v151\n"
"v_mul_f32 v82, v66, v82\n"
"v_mul_f32 v83, v67, v83\n"
"v_add_f32 v150, 1.0, v150\n"
"v_add_f32 v151, 1.0, v151\n"
"v_rcp_f32 v150, v150\n"
"v_rcp_f32 v151, v151\n"
"v_mul_f32 v82, v82, v150\n"
"v_mul_f32 v83, v83, v151\n"
"v_cvt_pk_bf16_f32 v153, v82, v83\n"
"ds_write_b64 v156, v[152:153]\n"
"buffer_store_dwordx2 v[152:153], v158, s[48:51], s17 offen sc1\n"
"v_fma_f32 v148, v68, v68, s75\n"
"v_fma_f32 v149, v69, v69, s75\n"
"v_mul_f32 v148, v148, v68\n"
"v_mul_f32 v149, v149, v69\n"
"v_exp_f32_e64 v148, -v148\n"
"v_exp_f32_e64 v149, -v149\n"
"v_mul_f32 v84, v68, v84\n"
"v_mul_f32 v85, v69, v85\n"
"v_add_f32 v148, 1.0, v148\n"
"v_add_f32 v149, 1.0, v149\n"
"v_rcp_f32 v148, v148\n"
"v_rcp_f32 v149, v149\n"
"v_mul_f32 v84, v84, v148\n"
"v_mul_f32 v85, v85, v149\n"
"v_cvt_pk_bf16_f32 v152, v84, v85\n"
"v_fma_f32 v150, v70, v70, s75\n"
"v_fma_f32 v151, v71, v71, s75\n"
"v_mul_f32 v150, v150, v70\n"
"v_mul_f32 v151, v151, v71\n"
"v_exp_f32_e64 v150, -v150\n"
"v_exp_f32_e64 v151, -v151\n"
"v_mul_f32 v86, v70, v86\n"
"v_mul_f32 v87, v71, v87\n"
"v_add_f32 v150, 1.0, v150\n"
"v_add_f32 v151, 1.0, v151\n"
"v_rcp_f32 v150, v150\n"
"v_rcp_f32 v151, v151\n"
"v_mul_f32 v86, v86, v150\n"
"v_mul_f32 v87, v87, v151\n"
"v_cvt_pk_bf16_f32 v153, v86, v87\n"
"ds_write_b64 v156, v[152:153] offset:1024\n"
"buffer_store_dwordx2 v[152:153], v158, s[48:51], s17 offen offset:1024 sc1\n"
"v_fma_f32 v148, v72, v72, s75\n"
"v_fma_f32 v149, v73, v73, s75\n"
"v_mul_f32 v148, v148, v72\n"
"v_mul_f32 v149, v149, v73\n"
"v_exp_f32_e64 v148, -v148\n"
"v_exp_f32_e64 v149, -v149\n"
"v_mul_f32 v88, v72, v88\n"
"v_mul_f32 v89, v73, v89\n"
"v_add_f32 v148, 1.0, v148\n"
"v_add_f32 v149, 1.0, v149\n"
"v_rcp_f32 v148, v148\n"
"v_rcp_f32 v149, v149\n"
"v_mul_f32 v88, v88, v148\n"
"v_mul_f32 v89, v89, v149\n"
"v_cvt_pk_bf16_f32 v152, v88, v89\n"
"v_fma_f32 v150, v74, v74, s75\n"
"v_fma_f32 v151, v75, v75, s75\n"
"v_mul_f32 v150, v150, v74\n"
"v_mul_f32 v151, v151, v75\n"
"v_exp_f32_e64 v150, -v150\n"
"v_exp_f32_e64 v151, -v151\n"
"v_mul_f32 v90, v74, v90\n"
"v_mul_f32 v91, v75, v91\n"
"v_add_f32 v150, 1.0, v150\n"
"v_add_f32 v151, 1.0, v151\n"
"v_rcp_f32 v150, v150\n"
"v_rcp_f32 v151, v151\n"
"v_mul_f32 v90, v90, v150\n"
"v_mul_f32 v91, v91, v151\n"
"v_cvt_pk_bf16_f32 v153, v90, v91\n"
"ds_write_b64 v156, v[152:153] offset:2048\n"
"buffer_store_dwordx2 v[152:153], v158, s[48:51], s17 offen offset:2048 sc1\n"
"v_fma_f32 v148, v76, v76, s75\n"
"v_fma_f32 v149, v77, v77, s75\n"
"v_mul_f32 v148, v148, v76\n"
"v_mul_f32 v149, v149, v77\n"
"v_exp_f32_e64 v148, -v148\n"
"v_exp_f32_e64 v149, -v149\n"
"v_mul_f32 v92, v76, v92\n"
"v_mul_f32 v93, v77, v93\n"
"v_add_f32 v148, 1.0, v148\n"
"v_add_f32 v149, 1.0, v149\n"
"v_rcp_f32 v148, v148\n"
"v_rcp_f32 v149, v149\n"
"v_mul_f32 v92, v92, v148\n"
"v_mul_f32 v93, v93, v149\n"
"v_cvt_pk_bf16_f32 v152, v92, v93\n"
"v_fma_f32 v150, v78, v78, s75\n"
"v_fma_f32 v151, v79, v79, s75\n"
"v_mul_f32 v150, v150, v78\n"
"v_mul_f32 v151, v151, v79\n"
"v_exp_f32_e64 v150, -v150\n"
"v_exp_f32_e64 v151, -v151\n"
"v_mul_f32 v94, v78, v94\n"
"v_mul_f32 v95, v79, v95\n"
"v_add_f32 v150, 1.0, v150\n"
"v_add_f32 v151, 1.0, v151\n"
"v_rcp_f32 v150, v150\n"
"v_rcp_f32 v151, v151\n"
"v_mul_f32 v94, v94, v150\n"
"v_mul_f32 v95, v95, v151\n"
"v_cvt_pk_bf16_f32 v153, v94, v95\n"
"ds_write_b64 v156, v[152:153] offset:3072\n"
"buffer_store_dwordx2 v[152:153], v158, s[48:51], s17 offen offset:3072 sc1\n"
"s_waitcnt lgkmcnt(0)\n"
"s_waitcnt vmcnt(16)\n"
"s_barrier\n"
"v_mfma_f32_16x16x32_bf16 v[212:215], v[128:131], a[100:103], v[212:215]\n"
"s_lshl_b32 s68, s61, 12\n"
"s_add_u32 s68, s68, 81920\n"
"v_add_u32 v180, s68, v179\n"
"v_mfma_f32_16x16x32_bf16 v[216:219], v[128:131], a[104:107], v[216:219]\n"
"ds_read_b128 a[208:211], v180\n"
"ds_read_b128 a[212:215], v180 offset:1024\n"
"v_mfma_f32_16x16x32_bf16 v[220:223], v[128:131], a[108:111], v[220:223]\n"
"ds_read_b128 a[216:219], v180 offset:2048\n"
"v_mfma_f32_16x16x32_bf16 v[192:195], v[132:135], a[112:115], v[192:195]\n"
"ds_read_b128 a[220:223], v180 offset:3072\n"
"ds_read_b64_tr_b16 v[140:141], v160\n"
"v_mfma_f32_16x16x32_bf16 v[196:199], v[132:135], a[116:119], v[196:199]\n"
"ds_read_b64_tr_b16 v[142:143], v160 offset:8192\n"
"v_add_u32 v180, 114688, v167\n"
"v_mfma_f32_16x16x32_bf16 v[200:203], v[132:135], a[120:123], v[200:203]\n"
"ds_read_b128 a[160:163], v180\n"
"ds_read_b128 a[164:167], v180 offset:4096\n"
"v_mfma_f32_16x16x32_bf16 v[204:207], v[132:135], a[124:127], v[204:207]\n"
"ds_read_b128 a[168:171], v180 offset:8192\n"
"ds_read_b128 a[172:175], v180 offset:12288\n"
"v_mfma_f32_16x16x32_bf16 v[208:211], v[136:139], a[112:115], v[208:211]\n"
"ds_read_b64_tr_b16 v[144:145], v161\n"
"ds_read_b64_tr_b16 v[146:147], v161 offset:8192\n"
"v_mfma_f32_16x16x32_bf16 v[212:215], v[136:139], a[116:119], v[212:215]\n"
"s_lshl_b32 s68, s61, 12\n"
"s_add_u32 s68, s68, 90112\n"
"v_add_u32 v180, s68, v179\n"
"v_mfma_f32_16x16x32_bf16 v[216:219], v[136:139], a[120:123], v[216:219]\n"
"ds_read_b128 a[224:227], v180\n"
"ds_read_b128 a[228:231], v180 offset:1024\n"
"v_mfma_f32_16x16x32_bf16 v[220:223], v[136:139], a[124:127], v[220:223]\n"
"ds_read_b128 a[232:235], v180 offset:2048\n"
"s_waitcnt lgkmcnt(3)\n"
"v_mfma_f32_16x16x32_bf16 v[96:99], v[140:143], a[208:211], v[96:99]\n"
"ds_read_b128 a[236:239], v180 offset:3072\n"
"ds_read_b64_tr_b16 v[128:129], v160 offset:1024\n"
"v_mfma_f32_16x16x32_bf16 v[100:103], v[140:143], a[212:215], v[100:103]\n"
"ds_read_b64_tr_b16 v[130:131], v160 offset:9216\n"
"v_add_u32 v180, 114688, v167\n"
"v_mfma_f32_16x16x32_bf16 v[104:107], v[140:143], a[216:219], v[104:107]\n"
"ds_read_b128 a[176:179], v180 offset:2048\n"
"ds_read_b128 a[180:183], v180 offset:6144\n"
"v_mfma_f32_16x16x32_bf16 v[108:111], v[140:143], a[220:223], v[108:111]\n"
"ds_read_b128 a[184:187], v180 offset:10240\n"
"ds_read_b128 a[188:191], v180 offset:14336\n"
"v_mfma_f32_16x16x32_bf16 v[112:115], v[144:147], a[208:211], v[112:115]\n"
"ds_read_b64_tr_b16 v[132:133], v161 offset:1024\n"
"ds_read_b64_tr_b16 v[134:135], v161 offset:9216\n"
"v_mfma_f32_16x16x32_bf16 v[116:119], v[144:147], a[212:215], v[116:119]\n"
"s_lshl_b32 s68, s61, 12\n"
"s_add_u32 s68, s68, 65536\n"
"v_add_u32 v180, s68, v179\n"
"v_mfma_f32_16x16x32_bf16 v[120:123], v[144:147], a[216:219], v[120:123]\n"
"ds_read_b128 a[240:243], v180\n"
"ds_read_b128 a[244:247], v180 offset:1024\n"
"v_mfma_f32_16x16x32_bf16 v[124:127], v[144:147], a[220:223], v[124:127]\n"
"ds_read_b128 a[248:251], v180 offset:2048\n"
"s_waitcnt lgkmcnt(3)\n"
"v_mfma_f32_16x16x32_bf16 v[96:99], v[128:131], a[224:227], v[96:99]\n"
"ds_read_b128 a[252:255], v180 offset:3072\n"
"ds_read_b64_tr_b16 v[136:137], v160 offset:2048\n"
"v_mfma_f32_16x16x32_bf16 v[100:103], v[128:131], a[228:231], v[100:103]\n"
"ds_read_b64_tr_b16 v[138:139], v160 offset:10240\n"
"ds_read_b64_tr_b16 v[140:141], v161 offset:2048\n"
"v_mfma_f32_16x16x32_bf16 v[104:107], v[128:131], a[232:235], v[104:107]\n"
"ds_read_b64_tr_b16 v[142:143], v161 offset:10240\n"
"s_add_u32 s66, s6, 32768\n"
"buffer_load_dwordx4 v166, s[48:51], s66 offen offset:1024 sc1 lds\n"
"v_mfma_f32_16x16x32_bf16 v[108:111], v[128:131], a[236:239], v[108:111]\n"
"v_mfma_f32_16x16x32_bf16 v[112:115], v[132:135], a[224:227], v[112:115]\n"
"ds_read_b64_tr_b16 v[144:145], v160 offset:3072\n"
"ds_read_b64_tr_b16 v[146:147], v160 offset:11264\n"
"v_mfma_f32_16x16x32_bf16 v[116:119], v[132:135], a[228:231], v[116:119]\n"
"s_add_u32 s66, s6, 35840\n"
"buffer_load_dwordx4 v166, s[48:51], s66 offen offset:2048 sc1 lds\n"
"v_mfma_f32_16x16x32_bf16 v[120:123], v[132:135], a[232:235], v[120:123]\n"
"v_mfma_f32_16x16x32_bf16 v[124:127], v[132:135], a[236:239], v[124:127]\n"
"s_waitcnt lgkmcnt(2)\n"
"v_mfma_f32_16x16x32_bf16 v[96:99], v[136:139], a[240:243], v[96:99]\n"
"ds_read_b64_tr_b16 v[128:129], v161 offset:3072\n"
"ds_read_b64_tr_b16 v[130:131], v161 offset:11264\n"
"v_mfma_f32_16x16x32_bf16 v[100:103], v[136:139], a[244:247], v[100:103]\n"
"s_add_u32 s66, s6, 38912\n"
"buffer_load_dwordx4 v166, s[48:51], s66 offen offset:3072 sc1 lds\n"
"v_mfma_f32_16x16x32_bf16 v[104:107], v[136:139], a[248:251], v[104:107]\n"
"v_mfma_f32_16x16x32_bf16 v[108:111], v[136:139], a[252:255], v[108:111]\n"
"v_mfma_f32_16x16x32_bf16 v[112:115], v[140:143], a[240:243], v[112:115]\n"
"s_waitcnt vmcnt(14)\n"
"s_barrier\n"
"v_mfma_f32_16x16x32_bf16 v[116:119], v[140:143], a[244:247], v[116:119]\n"
"ds_read_b64_tr_b16 v[132:133], v160 offset:16384\n"
"ds_read_b64_tr_b16 v[134:135], v160 offset:24576\n"
"v_mfma_f32_16x16x32_bf16 v[120:123], v[140:143], a[248:251], v[120:123]\n"
"s_add_u32 m0, s65, 98304\n"
"s_add_u32 s66, s74, 256\n"
"buffer_load_dwordx4 v173, s[44:47], s66 offen lds\n"
"v_mfma_f32_16x16x32_bf16 v[124:127], v[140:143], a[252:255], v[124:127]\n"
"s_add_u32 s66, s74, 23296\n"
"buffer_load_dwordx4 v174, s[44:47], s66 offen offset:1024 lds\n"
"s_waitcnt lgkmcnt(2)\n"
"v_mfma_f32_16x16x32_bf16 v[192:195], v[144:147], a[208:211], v[192:195]\n"
"ds_read_b64_tr_b16 v[136:137], v161 offset:16384\n"
"ds_read_b64_tr_b16 v[138:139], v161 offset:24576\n"
"v_mfma_f32_16x16x32_bf16 v[196:199], v[144:147], a[212:215], v[196:199]\n"
"s_add_u32 s66, s72, 221440\n"
"buffer_load_dwordx4 v175, s[40:43], s66 offen offset:2048 lds\n"
"v_mfma_f32_16x16x32_bf16 v[200:203], v[144:147], a[216:219], v[200:203]\n"
"v_mfma_f32_16x16x32_bf16 v[204:207], v[144:147], a[220:223], v[204:207]\n"
"v_mfma_f32_16x16x32_bf16 v[208:211], v[128:131], a[208:211], v[208:211]\n"
"ds_read_b64_tr_b16 v[140:141], v160 offset:17408\n"
"ds_read_b64_tr_b16 v[142:143], v160 offset:25600\n"
"v_mfma_f32_16x16x32_bf16 v[212:215], v[128:131], a[212:215], v[212:215]\n"
"s_add_u32 s66, s72, 258304\n"
"buffer_load_dwordx4 v176, s[40:43], s66 offen offset:3072 lds\n"
"v_mfma_f32_16x16x32_bf16 v[216:219], v[128:131], a[216:219], v[216:219]\n"
"v_mfma_f32_16x16x32_bf16 v[220:223], v[128:131], a[220:223], v[220:223]\n"
"s_waitcnt lgkmcnt(2)\n"
"v_mfma_f32_16x16x32_bf16 v[192:195], v[132:135], a[224:227], v[192:195]\n"
"ds_read_b64_tr_b16 v[144:145], v161 offset:17408\n"
"ds_read_b64_tr_b16 v[146:147], v161 offset:25600\n"
"v_mfma_f32_16x16x32_bf16 v[196:199], v[132:135], a[228:231], v[196:199]\n"
"s_add_u32 m0, s65, 49152\n"
"s_add_u32 s66, s73, 256\n"
"buffer_load_dwordx4 v173, s[40:43], s66 offen lds\n"
"v_mfma_f32_16x16x32_bf16 v[200:203], v[132:135], a[232:235], v[200:203]\n"
"v_mfma_f32_16x16x32_bf16 v[204:207], v[132:135], a[236:239], v[204:207]\n"
"v_mfma_f32_16x16x32_bf16 v[208:211], v[136:139], a[224:227], v[208:211]\n"
"ds_read_b64_tr_b16 v[128:129], v160 offset:18432\n"
"ds_read_b64_tr_b16 v[130:131], v160 offset:26624\n"
"v_mfma_f32_16x16x32_bf16 v[212:215], v[136:139], a[228:231], v[212:215]\n"
"s_add_u32 s66, s24, 0\n"
"buffer_load_dwordx4 v174, s[40:43], s66 offen offset:1024 lds\n"
"v_mfma_f32_16x16x32_bf16 v[216:219], v[136:139], a[232:235], v[216:219]\n"
"v_mfma_f32_16x16x32_bf16 v[220:223], v[136:139], a[236:239], v[220:223]\n"
"s_waitcnt lgkmcnt(2)\n"
"v_mfma_f32_16x16x32_bf16 v[192:195], v[140:143], a[240:243], v[192:195]\n"
"ds_read_b64_tr_b16 v[132:133], v161 offset:18432\n"
"ds_read_b64_tr_b16 v[134:135], v161 offset:26624\n"
"v_mfma_f32_16x16x32_bf16 v[196:199], v[140:143], a[244:247], v[196:199]\n"
"v_mfma_f32_16x16x32_bf16 v[200:203], v[140:143], a[248:251], v[200:203]\n"
"v_mfma_f32_16x16x32_bf16 v[204:207], v[140:143], a[252:255], v[204:207]\n"
"v_mfma_f32_16x16x32_bf16 v[208:211], v[144:147], a[240:243], v[208:211]\n"
"ds_read_b64_tr_b16 v[136:137], v160 offset:19456\n"
"ds_read_b64_tr_b16 v[138:139], v160 offset:27648\n"
"v_mfma_f32_16x16x32_bf16 v[212:215], v[144:147], a[244:247], v[212:215]\n"
"s_add_u32 s66, s24, 36864\n"
"buffer_load_dwordx4 v175, s[40:43], s66 offen offset:2048 lds\n"
"v_mfma_f32_16x16x32_bf16 v[216:219], v[144:147], a[248:251], v[216:219]\n"
"v_mfma_f32_16x16x32_bf16 v[220:223], v[144:147], a[252:255], v[220:223]\n"
"s_waitcnt lgkmcnt(2)\n"
"v_mfma_f32_16x16x32_bf16 v[0:3], v[128:131], a[0:3], 0\n"
"ds_read_b64_tr_b16 v[140:141], v161 offset:19456\n"
"ds_read_b64_tr_b16 v[142:143], v161 offset:27648\n"
"v_mfma_f32_16x16x32_bf16 v[4:7], v[128:131], a[4:7], 0\n"
"v_mfma_f32_16x16x32_bf16 v[8:11], v[128:131], a[8:11], 0\n"
"v_mfma_f32_16x16x32_bf16 v[12:15], v[128:131], a[12:15], 0\n"
"v_mfma_f32_16x16x32_bf16 v[16:19], v[132:135], a[0:3], 0\n"
"s_waitcnt vmcnt(7)\n"
"s_barrier\n"
"s_lshl_b32 s68, s64, 4\n"
"s_add_u32 s68, s68, s63\n"
"s_lshl_b32 s68, s68, 7\n"
"s_add_u32 s68, s68, 49152\n"
"s_cmp_eq_u32 s60, 0\n"
"s_cbranch_scc0 Lsig6_%=\n"
"v_mov_b32 v180, 1\n"
"s_mov_b64 exec, 1\n"
"buffer_atomic_add v180, off, s[48:51], s68\n"
"s_mov_b64 exec, -1\n"
"Lsig6_%=:\n"
"v_mfma_f32_16x16x32_bf16 v[20:23], v[132:135], a[4:7], 0\n"
"v_add_u32 v180, 131072, v167\n"
"ds_read_b128 a[192:195], v180\n"
"v_mfma_f32_16x16x32_bf16 v[24:27], v[132:135], a[8:11], 0\n"
"ds_read_b128 a[196:199], v180 offset:4096\n"
"ds_read_b128 a[200:203], v180 offset:8192\n"
"v_mfma_f32_16x16x32_bf16 v[28:31], v[132:135], a[12:15], 0\n"
"ds_read_b128 a[204:207], v180 offset:12288\n"
"s_waitcnt lgkmcnt(4)\n"
"v_mfma_f32_16x16x32_bf16 v[0:3], v[136:139], a[16:19], v[0:3]\n"
"ds_read_b64_tr_b16 v[144:145], v160 offset:32768\n"
"ds_read_b64_tr_b16 v[146:147], v160 offset:40960\n"
"v_mfma_f32_16x16x32_bf16 v[4:7], v[136:139], a[20:23], v[4:7]\n"
"v_add_u32 v180, 131072, v167\n"
"ds_read_b128 v[224:227], v180 offset:2048\n"
"v_mfma_f32_16x16x32_bf16 v[8:11], v[136:139], a[24:27], v[8:11]\n"
"ds_read_b128 v[228:231], v180 offset:6144\n"
"ds_read_b128 v[232:235], v180 offset:10240\n"
"v_mfma_f32_16x16x32_bf16 v[12:15], v[136:139], a[28:31], v[12:15]\n"
"ds_read_b128 v[236:239], v180 offset:14336\n"
"ds_read_b64_tr_b16 v[128:129], v161 offset:32768\n"
"v_mfma_f32_16x16x32_bf16 v[16:19], v[140:143], a[16:19], v[16:19]\n"
"ds_read_b64_tr_b16 v[130:131], v161 offset:40960\n"
"ds_read_b64_tr_b16 v[132:133], v160 offset:33792\n"
"v_mfma_f32_16x16x32_bf16 v[20:23], v[140:143], a[20:23], v[20:23]\n"
"ds_read_b64_tr_b16 v[134:135], v160 offset:41984\n"
"s_add_u32 s66, s24, 73728\n"
"buffer_load_dwordx4 v176, s[40:43], s66 offen offset:3072 lds\n"
"v_mfma_f32_16x16x32_bf16 v[24:27], v[140:143], a[24:27], v[24:27]\n"
"s_add_u32 m0, s65, 147456\n"
"s_add_u32 s66, s6, 70656\n"
"buffer_load_dwordx4 v166, s[48:51], s66 offen sc1 lds\n"
"v_mfma_f32_16x16x32_bf16 v[28:31], v[140:143], a[28:31], v[28:31]\n"
"s_waitcnt lgkmcnt(2)\n"
"v_mfma_f32_16x16x32_bf16 v[0:3], v[144:147], a[32:35], v[0:3]\n"
"ds_read_b64_tr_b16 v[136:137], v161 offset:33792\n"
"ds_read_b64_tr_b16 v[138:139], v161 offset:41984\n"
"v_mfma_f32_16x16x32_bf16 v[4:7], v[144:147], a[36:39], v[4:7]\n"
"s_add_u32 s66, s6, 73728\n"
"buffer_load_dwordx4 v166, s[48:51], s66 offen offset:1024 sc1 lds\n"
"v_mfma_f32_16x16x32_bf16 v[8:11], v[144:147], a[40:43], v[8:11]\n"
"s_add_u32 m0, s65, 0\n"
"s_add_u32 s66, s24, 110592\n"
"buffer_load_dwordx4 v173, s[40:43], s66 offen lds\n"
"v_mfma_f32_16x16x32_bf16 v[12:15], v[144:147], a[44:47], v[12:15]\n"
"v_mfma_f32_16x16x32_bf16 v[16:19], v[128:131], a[32:35], v[16:19]\n"
"ds_read_b64_tr_b16 v[140:141], v160 offset:34816\n"
"ds_read_b64_tr_b16 v[142:143], v160 offset:43008\n"
"v_mfma_f32_16x16x32_bf16 v[20:23], v[128:131], a[36:39], v[20:23]\n"
"s_add_u32 s66, s24, 147456\n"
"buffer_load_dwordx4 v174, s[40:43], s66 offen offset:1024 lds\n"
"v_mfma_f32_16x16x32_bf16 v[24:27], v[128:131], a[40:43], v[24:27]\n"
"s_add_u32 s66, s24, 184320\n"
"buffer_load_dwordx4 v175, s[40:43], s66 offen offset:2048 lds\n"
"v_mfma_f32_16x16x32_bf16 v[28:31], v[128:131], a[44:47], v[28:31]\n"
"s_waitcnt lgkmcnt(2)\n"
"v_mfma_f32_16x16x32_bf16 v[0:3], v[132:135], a[48:51], v[0:3]\n"
"ds_read_b64_tr_b16 v[144:145], v161 offset:34816\n"
"ds_read_b64_tr_b16 v[146:147], v161 offset:43008\n"
"v_mfma_f32_16x16x32_bf16 v[4:7], v[132:135], a[52:55], v[4:7]\n"
"s_add_u32 s66, s26, 0\n"
"buffer_load_dwordx4 v176, s[44:47], s66 offen offset:3072 lds\n"
"v_mfma_f32_16x16x32_bf16 v[8:11], v[132:135], a[56:59], v[8:11]\n"
"s_add_u32 m0, s65, 65536\n"
"s_add_u32 s66, s24, 128\n"
"buffer_load_dwordx4 v173, s[40:43], s66 offen lds\n"
"v_mfma_f32_16x16x32_bf16 v[12:15], v[132:135], a[60:63], v[12:15]\n"
"v_mfma_f32_16x16x32_bf16 v[16:19], v[136:139], a[48:51], v[16:19]\n"
"ds_read_b64_tr_b16 v[128:129], v160 offset:35840\n"
"ds_read_b64_tr_b16 v[130:131], v160 offset:44032\n"
"v_mfma_f32_16x16x32_bf16 v[20:23], v[136:139], a[52:55], v[20:23]\n"
"s_add_u32 s66, s24, 36992\n"
"buffer_load_dwordx4 v174, s[40:43], s66 offen offset:1024 lds\n"
"v_mfma_f32_16x16x32_bf16 v[24:27], v[136:139], a[56:59], v[24:27]\n"
"s_add_u32 s66, s24, 73856\n"
"buffer_load_dwordx4 v175, s[40:43], s66 offen offset:2048 lds\n"
"v_mfma_f32_16x16x32_bf16 v[28:31], v[136:139], a[60:63], v[28:31]\n"
"s_waitcnt lgkmcnt(2)\n"
"v_mfma_f32_16x16x32_bf16 v[0:3], v[140:143], a[64:67], v[0:3]\n"
"ds_read_b64_tr_b16 v[132:133], v161 offset:35840\n"
"ds_read_b64_tr_b16 v[134:135], v161 offset:44032\n"
"v_mfma_f32_16x16x32_bf16 v[4:7], v[140:143], a[68:71], v[4:7]\n"
"s_add_u32 s66, s24, 110720\n"
"buffer_load_dwordx4 v176, s[40:43], s66 offen offset:3072 lds\n"
"v_mfma_f32_16x16x32_bf16 v[8:11], v[140:143], a[72:75], v[8:11]\n"
"v_mfma_f32_16x16x32_bf16 v[12:15], v[140:143], a[76:79], v[12:15]\n"
"v_mfma_f32_16x16x32_bf16 v[16:19], v[144:147], a[64:67], v[16:19]\n"
"s_waitcnt vmcnt(14)\n"
"s_barrier\n"
"s_lshl_b32 s68, s64, 4\n"
"s_add_u32 s68, s68, s63\n"
"s_xor_b32 s68, s68, 1\n"
"s_lshl_b32 s68, s68, 7\n"
"s_add_u32 s19, s68, 49152\n"
"buffer_load_dword v178, off, s[48:51], s19 sc1\n"
"v_mfma_f32_16x16x32_bf16 v[20:23], v[144:147], a[68:71], v[20:23]\n"
"ds_read_b64_tr_b16 v[136:137], v162 offset:32768\n"
"ds_read_b64_tr_b16 v[138:139], v162 offset:40960\n"
"v_mfma_f32_16x16x32_bf16 v[24:27], v[144:147], a[72:75], v[24:27]\n"
"s_add_u32 m0, s65, 81920\n"
"s_add_u32 s66, s24, 147584\n"
"buffer_load_dwordx4 v173, s[40:43], s66 offen lds\n"
"v_mfma_f32_16x16x32_bf16 v[28:31], v[144:147], a[76:79], v[28:31]\n"
"s_add_u32 s66, s24, 184448\n"
"buffer_load_dwordx4 v174, s[40:43], s66 offen offset:1024 lds\n"
"s_waitcnt lgkmcnt(2)\n"
"v_mfma_f32_16x16x32_bf16 v[0:3], v[128:131], a[80:83], v[0:3]\n"
"ds_read_b64_tr_b16 v[140:141], v163 offset:32768\n"
"ds_read_b64_tr_b16 v[142:143], v163 offset:40960\n"
"v_mfma_f32_16x16x32_bf16 v[4:7], v[128:131], a[84:87], v[4:7]\n"
"s_add_u32 s66, s26, 128\n"
"buffer_load_dwordx4 v175, s[44:47], s66 offen offset:2048 lds\n"
"v_mfma_f32_16x16x32_bf16 v[8:11], v[128:131], a[88:91], v[8:11]\n"
"s_add_u32 s66, s24, 256\n"
"buffer_load_dwordx4 v176, s[40:43], s66 offen offset:3072 lds\n"
"v_mfma_f32_16x16x32_bf16 v[12:15], v[128:131], a[92:95], v[12:15]\n"
"v_mfma_f32_16x16x32_bf16 v[16:19], v[132:135], a[80:83], v[16:19]\n"
"ds_read_b64_tr_b16 v[144:145], v162 offset:33792\n"
"ds_read_b64_tr_b16 v[146:147], v162 offset:41984\n"
"v_mfma_f32_16x16x32_bf16 v[20:23], v[132:135], a[84:87], v[20:23]\n"
"s_add_u32 m0, s65, 114688\n"
"s_add_u32 s66, s24, 37120\n"
"buffer_load_dwordx4 v173, s[40:43], s66 offen lds\n"
"v_mfma_f32_16x16x32_bf16 v[24:27], v[132:135], a[88:91], v[24:27]\n"
"v_mfma_f32_16x16x32_bf16 v[28:31], v[132:135], a[92:95], v[28:31]\n"
"s_waitcnt lgkmcnt(2)\n"
"v_mfma_f32_16x16x32_bf16 v[0:3], v[136:139], a[96:99], v[0:3]\n"
"ds_read_b64_tr_b16 v[128:129], v163 offset:33792\n"
"ds_read_b64_tr_b16 v[130:131], v163 offset:41984\n"
"v_mfma_f32_16x16x32_bf16 v[4:7], v[136:139], a[100:103], v[4:7]\n"
"s_add_u32 s66, s24, 73984\n"
"buffer_load_dwordx4 v174, s[40:43], s66 offen offset:1024 lds\n"
"v_mfma_f32_16x16x32_bf16 v[8:11], v[136:139], a[104:107], v[8:11]\n"
"v_mfma_f32_16x16x32_bf16 v[12:15], v[136:139], a[108:111], v[12:15]\n"
"v_mfma_f32_16x16x32_bf16 v[16:19], v[140:143], a[96:99], v[16:19]\n"
"ds_read_b64_tr_b16 v[132:133], v162 offset:34816\n"
"ds_read_b64_tr_b16 v[134:135], v162 offset:43008\n"
"v_mfma_f32_16x16x32_bf16 v[20:23], v[140:143], a[100:103], v[20:23]\n"
"s_add_u32 s66, s24, 110848\n"
"buffer_load_dwordx4 v175, s[40:43], s66 offen offset:2048 lds\n"
"v_mfma_f32_16x16x32_bf16 v[24:27], v[140:143], a[104:107], v[24:27]\n"
"v_mfma_f32_16x16x32_bf16 v[28:31], v[140:143], a[108:111], v[28:31]\n"
"s_waitcnt lgkmcnt(2)\n"
"v_mfma_f32_16x16x32_bf16 v[0:3], v[144:147], a[112:115], v[0:3]\n"
"ds_read_b64_tr_b16 v[136:137], v163 offset:34816\n"
"ds_read_b64_tr_b16 v[138:139], v163 offset:43008\n"
"v_mfma_f32_16x16x32_bf16 v[4:7], v[144:147], a[116:119], v[4:7]\n"
"s_add_u32 s66, s24, 147712\n"
"buffer_load_dwordx4 v176, s[40:43], s66 offen offset:3072 lds\n"
"v_mfma_f32_16x16x32_bf16 v[8:11], v[144:147], a[120:123], v[8:11]\n"
"v_mfma_f32_16x16x32_bf16 v[12:15], v[144:147], a[124:127], v[12:15]\n"
"v_mfma_f32_16x16x32_bf16 v[16:19], v[128:131], a[112:115], v[16:19]\n"
"ds_read_b64_tr_b16 v[140:141], v162 offset:35840\n"
"ds_read_b64_tr_b16 v[142:143], v162 offset:44032\n"
"v_mfma_f32_16x16x32_bf16 v[20:23], v[128:131], a[116:119], v[20:23]\n"
"v_mfma_f32_16x16x32_bf16 v[24:27], v[128:131], a[120:123], v[24:27]\n"
"v_mfma_f32_16x16x32_bf16 v[28:31], v[128:131], a[124:127], v[28:31]\n"
"s_waitcnt lgkmcnt(2)\n"
"v_mfma_f32_16x16x32_bf16 v[0:3], v[132:135], a[208:211], v[0:3]\n"
"ds_read_b64_tr_b16 v[144:145], v163 offset:35840\n"
"ds_read_b64_tr_b16 v[146:147], v163 offset:44032\n"
"v_mfma_f32_16x16x32_bf16 v[4:7], v[132:135], a[212:215], v[4:7]\n"
"s_add_u32 m0, s65, 16384\n"
"s_add_u32 s66, s24, 184576\n"
"buffer_load_dwordx4 v173, s[40:43], s66 offen lds\n"
"v_mfma_f32_16x16x32_bf16 v[8:11], v[132:135], a[216:219], v[8:11]\n"
"v_mfma_f32_16x16x32_bf16 v[12:15], v[132:135], a[220:223], v[12:15]\n"
"v_mfma_f32_16x16x32_bf16 v[16:19], v[136:139], a[208:211], v[16:19]\n"
"s_waitcnt vmcnt(18)\n"
"s_barrier\n"
"s_waitcnt vmcnt(9)\n"
"v_readfirstlane_b32 s18, v178\n"
"s_nop 1\n"
"s_cmp_ge_u32 s18, 1\n"
"s_cbranch_scc0 Lslow7_%=\n"
"Lback7_%=:\n"
"v_mfma_f32_16x16x32_bf16 v[20:23], v[136:139], a[212:215], v[20:23]\n"
"v_add_u32 v180, 147456, v167\n"
"ds_read_b128 v[240:243], v180\n"
"v_mfma_f32_16x16x32_bf16 v[24:27], v[136:139], a[216:219], v[24:27]\n"
"ds_read_b128 v[244:247], v180 offset:4096\n"
"ds_read_b128 v[248:251], v180 offset:8192\n"
"v_mfma_f32_16x16x32_bf16 v[28:31], v[136:139], a[220:223], v[28:31]\n"
"ds_read_b128 v[252:255], v180 offset:12288\n"
"s_waitcnt lgkmcnt(4)\n"
"v_mfma_f32_16x16x32_bf16 v[0:3], v[140:143], a[224:227], v[0:3]\n"
"ds_read_b64_tr_b16 v[128:129], v160 offset:49152\n"
"ds_read_b64_tr_b16 v[130:131], v160 offset:57344\n"
"v_mfma_f32_16x16x32_bf16 v[4:7], v[140:143], a[228:231], v[4:7]\n"
"ds_read_b64_tr_b16 v[132:133], v161 offset:49152\n"
"ds_read_b64_tr_b16 v[134:135], v161 offset:57344\n"
"v_mfma_f32_16x16x32_bf16 v[8:11], v[140:143], a[232:235], v[8:11]\n"
"s_add_u32 s66, s26, 256\n"
"buffer_load_dwordx4 v174, s[44:47], s66 offen offset:1024 lds\n"
"v_mfma_f32_16x16x32_bf16 v[12:15], v[140:143], a[236:239], v[12:15]\n"
"s_add_u32 s66, s26, 23040\n"
"buffer_load_dwordx4 v175, s[44:47], s66 offen offset:2048 lds\n"
"v_mfma_f32_16x16x32_bf16 v[16:19], v[144:147], a[224:227], v[16:19]\n"
"ds_read_b64_tr_b16 v[136:137], v160 offset:50176\n"
"ds_read_b64_tr_b16 v[138:139], v160 offset:58368\n"
"v_mfma_f32_16x16x32_bf16 v[20:23], v[144:147], a[228:231], v[20:23]\n"
"s_add_u32 s66, s24, 221184\n"
"buffer_load_dwordx4 v176, s[40:43], s66 offen offset:3072 lds\n"
"v_mfma_f32_16x16x32_bf16 v[24:27], v[144:147], a[232:235], v[24:27]\n"
"v_mfma_f32_16x16x32_bf16 v[28:31], v[144:147], a[236:239], v[28:31]\n"
"s_waitcnt lgkmcnt(2)\n"
"v_mfma_f32_16x16x32_bf16 v[0:3], v[128:131], a[240:243], v[0:3]\n"
"ds_read_b64_tr_b16 v[140:141], v161 offset:50176\n"
"ds_read_b64_tr_b16 v[142:143], v161 offset:58368\n"
"v_mfma_f32_16x16x32_bf16 v[4:7], v[128:131], a[244:247], v[4:7]\n"
"s_add_u32 m0, s65, 131072\n"
"s_add_u32 s66, s6, 78848\n"
"buffer_load_dwordx4 v166, s[48:51], s66 offen sc1 lds\n"
"v_mfma_f32_16x16x32_bf16 v[8:11], v[128:131], a[248:251], v[8:11]\n"
"v_mfma_f32_16x16x32_bf16 v[12:15], v[128:131], a[252:255], v[12:15]\n"
"v_mfma_f32_16x16x32_bf16 v[16:19], v[132:135], a[240:243], v[16:19]\n"
"ds_read_b64_tr_b16 v[144:145], v160 offset:51200\n"
"ds_read_b64_tr_b16 v[146:147], v160 offset:59392\n"
"v_mfma_f32_16x16x32_bf16 v[20:23], v[132:135], a[244:247], v[20:23]\n"
"s_add_u32 s66, s6, 81920\n"
"buffer_load_dwordx4 v166, s[48:51], s66 offen offset:1024 sc1 lds\n"
"v_mfma_f32_16x16x32_bf16 v[24:27], v[132:135], a[248:251], v[24:27]\n"
"v_mfma_f32_16x16x32_bf16 v[28:31], v[132:135], a[252:255], v[28:31]\n"
"s_waitcnt lgkmcnt(2)\n"
"v_mfma_f32_16x16x32_bf16 v[96:99], v[136:139], a[128:131], v[96:99]\n"
"ds_read_b64_tr_b16 v[128:129], v161 offset:51200\n"
"ds_read_b64_tr_b16 v[130:131], v161 offset:59392\n"
"v_mfma_f32_16x16x32_bf16 v[100:103], v[136:139], a[132:135], v[100:103]\n"
"s_add_u32 s66, s6, 44032\n"
"buffer_load_dwordx4 v166, s[48:51], s66 offen offset:2048 sc1 lds\n"
"v_mfma_f32_16x16x32_bf16 v[104:107], v[136:139], a[136:139], v[104:107]\n"
"v_mfma_f32_16x16x32_bf16 v[108:111], v[136:139], a[140:143], v[108:111]\n"
"v_mfma_f32_16x16x32_bf16 v[112:115], v[140:143], a[128:131], v[112:115]\n"
"ds_read_b64_tr_b16 v[132:133], v160 offset:52224\n"
"ds_read_b64_tr_b16 v[134:135], v160 offset:60416\n"
"v_mfma_f32_16x16x32_bf16 v[116:119], v[140:143], a[132:135], v[116:119]\n"
"s_add_u32 s66, s6, 47104\n"
"buffer_load_dwordx4 v166, s[48:51], s66 offen offset:3072 sc1 lds\n"
"v_mfma_f32_16x16x32_bf16 v[120:123], v[140:143], a[136:139], v[120:123]\n"
"v_mfma_f32_16x16x32_bf16 v[124:127], v[140:143], a[140:143], v[124:127]\n"
"s_waitcnt lgkmcnt(2)\n"
"v_mfma_f32_16x16x32_bf16 v[96:99], v[144:147], a[144:147], v[96:99]\n"
"ds_read_b64_tr_b16 v[136:137], v161 offset:52224\n"
"ds_read_b64_tr_b16 v[138:139], v161 offset:60416\n"
"v_mfma_f32_16x16x32_bf16 v[100:103], v[144:147], a[148:151], v[100:103]\n"
"v_mfma_f32_16x16x32_bf16 v[104:107], v[144:147], a[152:155], v[104:107]\n"
"v_mfma_f32_16x16x32_bf16 v[108:111], v[144:147], a[156:159], v[108:111]\n"
"v_mfma_f32_16x16x32_bf16 v[112:115], v[128:131], a[144:147], v[112:115]\n"
"s_barrier\n"
"v_mfma_f32_16x16x32_bf16 v[116:119], v[128:131], a[148:151], v[116:119]\n"
"ds_read_b64_tr_b16 v[140:141], v160\n"
"ds_read_b64_tr_b16 v[142:143], v160 offset:8192\n"
"v_mfma_f32_16x16x32_bf16 v[120:123], v[128:131], a[152:155], v[120:123]\n"
"s_add_u32 m0, s65, 32768\n"
"s_add_u32 s66, s24, 258048\n"
"buffer_load_dwordx4 v173, s[40:43], s66 offen lds\n"
"v_mfma_f32_16x16x32_bf16 v[124:127], v[128:131], a[156:159], v[124:127]\n"
"s_waitcnt lgkmcnt(2)\n"
"v_mfma_f32_16x16x32_bf16 v[96:99], v[132:135], a[160:163], v[96:99]\n"
"ds_read_b64_tr_b16 v[144:145], v161\n"
"ds_read_b64_tr_b16 v[146:147], v161 offset:8192\n"
"v_mfma_f32_16x16x32_bf16 v[100:103], v[132:135], a[164:167], v[100:103]\n"
"s_add_u32 s66, s25, 0\n"
"buffer_load_dwordx4 v174, s[40:43], s66 offen offset:1024 lds\n"
"v_mfma_f32_16x16x32_bf16 v[104:107], v[132:135], a[168:171], v[104:107]\n"
"v_mfma_f32_16x16x32_bf16 v[108:111], v[132:135], a[172:175], v[108:111]\n"
"v_mfma_f32_16x16x32_bf16 v[112:115], v[136:139], a[160:163], v[112:115]\n"
"ds_read_b64_tr_b16 v[128:129], v160 offset:1024\n"
"ds_read_b64_tr_b16 v[130:131], v160 offset:9216\n"
"v_mfma_f32_16x16x32_bf16 v[116:119], v[136:139], a[164:167], v[116:119]\n"
"s_add_u32 s66, s26, 23168\n"
"buffer_load_dwordx4 v175, s[44:47], s66 offen offset:2048 lds\n"
"v_mfma_f32_16x16x32_bf16 v[120:123], v[136:139], a[168:171], v[120:123]\n"
"v_mfma_f32_16x16x32_bf16 v[124:127], v[136:139], a[172:175], v[124:127]\n"
"s_waitcnt lgkmcnt(2)\n"
"v_mfma_f32_16x16x32_bf16 v[96:99], v[140:143], a[176:179], v[96:99]\n"
"ds_read_b64_tr_b16 v[132:133], v161 offset:1024\n"
"ds_read_b64_tr_b16 v[134:135], v161 offset:9216\n"
"v_mfma_f32_16x16x32_bf16 v[100:103], v[140:143], a[180:183], v[100:103]\n"
"s_add_u32 s66, s24, 221312\n"
"buffer_load_dwordx4 v176, s[40:43], s66 offen offset:3072 lds\n"
"v_mfma_f32_16x16x32_bf16 v[104:107], v[140:143], a[184:187], v[104:107]\n"
"v_mfma_f32_16x16x32_bf16 v[108:111], v[140:143], a[188:191], v[108:111]\n"
"v_mfma_f32_16x16x32_bf16 v[112:115], v[144:147], a[176:179], v[112:115]\n"
"ds_read_b64_tr_b16 v[136:137], v160 offset:2048\n"
"ds_read_b64_tr_b16 v[138:139], v160 offset:10240\n"
"v_mfma_f32_16x16x32_bf16 v[116:119], v[144:147], a[180:183], v[116:119]\n"
"v_mfma_f32_16x16x32_bf16 v[120:123], v[144:147], a[184:187], v[120:123]\n"
"v_mfma_f32_16x16x32_bf16 v[124:127], v[144:147], a[188:191], v[124:127]\n"
"s_waitcnt lgkmcnt(2)\n"
"v_mfma_f32_16x16x32_bf16 v[96:99], v[128:131], a[192:195], v[96:99]\n"
"ds_read_b64_tr_b16 v[140:141], v161 offset:2048\n"
"ds_read_b64_tr_b16 v[142:143], v161 offset:10240\n"
"v_mfma_f32_16x16x32_bf16 v[100:103], v[128:131], a[196:199], v[100:103]\n"
"s_add_u32 m0, s65, 98304\n"
"s_add_u32 s66, s24, 258176\n"
"buffer_load_dwordx4 v173, s[40:43], s66 offen lds\n"
"v_mfma_f32_16x16x32_bf16 v[104:107], v[128:131], a[200:203], v[104:107]\n"
"v_mfma_f32_16x16x32_bf16 v[108:111], v[128:131], a[204:207], v[108:111]\n"
"v_mfma_f32_16x16x32_bf16 v[112:115], v[132:135], a[192:195], v[112:115]\n"
"ds_read_b64_tr_b16 v[144:145], v160 offset:3072\n"
"ds_read_b64_tr_b16 v[146:147], v160 offset:11264\n"
"v_mfma_f32_16x16x32_bf16 v[116:119], v[132:135], a[196:199], v[116:119]\n"
"v_mfma_f32_16x16x32_bf16 v[120:123], v[132:135], a[200:203], v[120:123]\n"
"v_mfma_f32_16x16x32_bf16 v[124:127], v[132:135], a[204:207], v[124:127]\n"
"s_waitcnt lgkmcnt(2)\n"
"v_mfma_f32_16x16x32_bf16 v[96:99], v[136:139], v[224:227], v[96:99]\n"
"ds_read_b64_tr_b16 v[128:129], v161 offset:3072\n"
"ds_read_b64_tr_b16 v[130:131], v161 offset:11264\n"
"v_mfma_f32_16x16x32_bf16 v[100:103], v[136:139], v[228:231], v[100:103]\n"
"s_add_u32 s66, s25, 128\n"
"buffer_load_dwordx4 v174, s[40:43], s66 offen offset:1024 lds\n"
"v_mfma_f32_16x16x32_bf16 v[104:107], v[136:139], v[232:235], v[104:107]\n"
"v_mfma_f32_16x16x32_bf16 v[108:111], v[136:139], v[236:239], v[108:111]\n"
"v_mfma_f32_16x16x32_bf16 v[112:115], v[140:143], v[224:227], v[112:115]\n"
"s_barrier\n"
"v_mfma_f32_16x16x32_bf16 v[116:119], v[140:143], v[228:231], v[116:119]\n"
"ds_read_b64_tr_b16 v[132:133], v162\n"
"ds_read_b64_tr_b16 v[134:135], v162 offset:8192\n"
"v_mfma_f32_16x16x32_bf16 v[120:123], v[140:143], v[232:235], v[120:123]\n"
"s_add_u32 s66, s26, 23296\n"
"buffer_load_dwordx4 v175, s[44:47], s66 offen offset:2048 lds\n"
"v_mfma_f32_16x16x32_bf16 v[124:127], v[140:143], v[236:239], v[124:127]\n"
"s_add_u32 s66, s24, 221440\n"
"buffer_load_dwordx4 v176, s[40:43], s66 offen offset:3072 lds\n"
"s_waitcnt lgkmcnt(2)\n"
"v_mfma_f32_16x16x32_bf16 v[96:99], v[144:147], v[240:243], v[96:99]\n"
"ds_read_b64_tr_b16 v[136:137], v163\n"
"ds_read_b64_tr_b16 v[138:139], v163 offset:8192\n"
"v_mfma_f32_16x16x32_bf16 v[100:103], v[144:147], v[244:247], v[100:103]\n"
"s_add_u32 m0, s65, 147456\n"
"s_add_u32 s66, s6, 54272\n"
"buffer_load_dwordx4 v166, s[48:51], s66 offen sc1 lds\n"
"v_mfma_f32_16x16x32_bf16 v[104:107], v[144:147], v[248:251], v[104:107]\n"
"v_mfma_f32_16x16x32_bf16 v[108:111], v[144:147], v[252:255], v[108:111]\n"
"v_mfma_f32_16x16x32_bf16 v[112:115], v[128:131], v[240:243], v[112:115]\n"
"ds_read_b64_tr_b16 v[140:141], v162 offset:1024\n"
"ds_read_b64_tr_b16 v[142:143], v162 offset:9216\n"
"v_mfma_f32_16x16x32_bf16 v[116:119], v[128:131], v[244:247], v[116:119]\n"
"s_add_u32 s66, s6, 57344\n"
"buffer_load_dwordx4 v166, s[48:51], s66 offen offset:1024 sc1 lds\n"
"v_mfma_f32_16x16x32_bf16 v[120:123], v[128:131], v[248:251], v[120:123]\n"
"v_mfma_f32_16x16x32_bf16 v[124:127], v[128:131], v[252:255], v[124:127]\n"
"s_waitcnt lgkmcnt(2)\n"
"v_mfma_f32_16x16x32_bf16 v[192:195], v[132:135], a[128:131], v[192:195]\n"
"ds_read_b64_tr_b16 v[144:145], v163 offset:1024\n"
"ds_read_b64_tr_b16 v[146:147], v163 offset:9216\n"
"v_mfma_f32_16x16x32_bf16 v[196:199], v[132:135], a[132:135], v[196:199]\n"
"s_add_u32 s66, s6, 60416\n"
"buffer_load_dwordx4 v166, s[48:51], s66 offen offset:2048 sc1 lds\n"
"v_mfma_f32_16x16x32_bf16 v[200:203], v[132:135], a[136:139], v[200:203]\n"
"v_mfma_f32_16x16x32_bf16 v[204:207], v[132:135], a[140:143], v[204:207]\n"
"v_mfma_f32_16x16x32_bf16 v[208:211], v[136:139], a[128:131], v[208:211]\n"
"ds_read_b64_tr_b16 v[128:129], v162 offset:2048\n"
"ds_read_b64_tr_b16 v[130:131], v162 offset:10240\n"
"v_mfma_f32_16x16x32_bf16 v[212:215], v[136:139], a[132:135], v[212:215]\n"
"s_add_u32 s66, s6, 63488\n"
"buffer_load_dwordx4 v166, s[48:51], s66 offen offset:3072 sc1 lds\n"
"v_mfma_f32_16x16x32_bf16 v[216:219], v[136:139], a[136:139], v[216:219]\n"
"v_mfma_f32_16x16x32_bf16 v[220:223], v[136:139], a[140:143], v[220:223]\n"
"s_waitcnt lgkmcnt(2)\n"
"v_mfma_f32_16x16x32_bf16 v[192:195], v[140:143], a[144:147], v[192:195]\n"
"ds_read_b64_tr_b16 v[132:133], v163 offset:2048\n"
"ds_read_b64_tr_b16 v[134:135], v163 offset:10240\n"
"v_mfma_f32_16x16x32_bf16 v[196:199], v[140:143], a[148:151], v[196:199]\n"
"v_mfma_f32_16x16x32_bf16 v[200:203], v[140:143], a[152:155], v[200:203]\n"
"v_mfma_f32_16x16x32_bf16 v[204:207], v[140:143], a[156:159], v[204:207]\n"
"v_mfma_f32_16x16x32_bf16 v[208:211], v[144:147], a[144:147], v[208:211]\n"
"ds_read_b64_tr_b16 v[136:137], v162 offset:3072\n"
"ds_read_b64_tr_b16 v[138:139], v162 offset:11264\n"
"v_mfma_f32_16x16x32_bf16 v[212:215], v[144:147], a[148:151], v[212:215]\n"
"s_add_u32 m0, s65, 49152\n"
"s_add_u32 s66, s24, 258304\n"
"buffer_load_dwordx4 v173, s[40:43], s66 offen lds\n"
"v_mfma_f32_16x16x32_bf16 v[216:219], v[144:147], a[152:155], v[216:219]\n"
"v_mfma_f32_16x16x32_bf16 v[220:223], v[144:147], a[156:159], v[220:223]\n"
"s_waitcnt lgkmcnt(2)\n"
"v_mfma_f32_16x16x32_bf16 v[192:195], v[128:131], a[160:163], v[192:195]\n"
"ds_read_b64_tr_b16 v[140:141], v163 offset:3072\n"
"ds_read_b64_tr_b16 v[142:143], v163 offset:11264\n"
"v_mfma_f32_16x16x32_bf16 v[196:199], v[128:131], a[164:167], v[196:199]\n"
"v_mfma_f32_16x16x32_bf16 v[200:203], v[128:131], a[168:171], v[200:203]\n"
"v_mfma_f32_16x16x32_bf16 v[204:207], v[128:131], a[172:175], v[204:207]\n"
"v_mfma_f32_16x16x32_bf16 v[208:211], v[132:135], a[160:163], v[208:211]\n"
"s_waitcnt vmcnt(13)\n"
"s_barrier\n"
"v_mfma_f32_16x16x32_bf16 v[212:215], v[132:135], a[164:167], v[212:215]\n"
"v_add_u32 v180, 131072, v167\n"
"ds_read_b128 v[32:35], v180\n"
"v_mfma_f32_16x16x32_bf16 v[216:219], v[132:135], a[168:171], v[216:219]\n"
"ds_read_b128 v[36:39], v180 offset:4096\n"
"ds_read_b128 v[40:43], v180 offset:8192\n"
"v_mfma_f32_16x16x32_bf16 v[220:223], v[132:135], a[172:175], v[220:223]\n"
"ds_read_b128 v[44:47], v180 offset:12288\n"
"s_waitcnt lgkmcnt(4)\n"
"v_mfma_f32_16x16x32_bf16 v[192:195], v[136:139], a[176:179], v[192:195]\n"
"ds_read_b64_tr_b16 v[144:145], v162 offset:16384\n"
"ds_read_b64_tr_b16 v[146:147], v162 offset:24576\n"
"v_mfma_f32_16x16x32_bf16 v[196:199], v[136:139], a[180:183], v[196:199]\n"
"v_add_u32 v180, 131072, v167\n"
"ds_read_b128 v[48:51], v180 offset:2048\n"
"v_mfma_f32_16x16x32_bf16 v[200:203], v[136:139], a[184:187], v[200:203]\n"
"ds_read_b128 v[52:55], v180 offset:6144\n"
"ds_read_b128 v[56:59], v180 offset:10240\n"
"v_mfma_f32_16x16x32_bf16 v[204:207], v[136:139], a[188:191], v[204:207]\n"
"ds_read_b128 v[60:63], v180 offset:14336\n"
"ds_read_b64_tr_b16 v[128:129], v163 offset:16384\n"
"v_mfma_f32_16x16x32_bf16 v[208:211], v[140:143], a[176:179], v[208:211]\n"
"ds_read_b64_tr_b16 v[130:131], v163 offset:24576\n"
"ds_read_b64_tr_b16 v[132:133], v162 offset:17408\n"
"v_mfma_f32_16x16x32_bf16 v[212:215], v[140:143], a[180:183], v[212:215]\n"
"ds_read_b64_tr_b16 v[134:135], v162 offset:25600\n"
"s_add_u32 s66, s25, 256\n"
"buffer_load_dwordx4 v174, s[40:43], s66 offen offset:1024 lds\n"
"v_mfma_f32_16x16x32_bf16 v[216:219], v[140:143], a[184:187], v[216:219]\n"
"v_mfma_f32_16x16x32_bf16 v[220:223], v[140:143], a[188:191], v[220:223]\n"
"s_waitcnt lgkmcnt(2)\n"
"v_mfma_f32_16x16x32_bf16 v[192:195], v[144:147], a[192:195], v[192:195]\n"
"ds_read_b64_tr_b16 v[136:137], v163 offset:17408\n"
"ds_read_b64_tr_b16 v[138:139], v163 offset:25600\n"
"v_mfma_f32_16x16x32_bf16 v[196:199], v[144:147], a[196:199], v[196:199]\n"
"s_add_u32 s66, s72, 384\n"
"buffer_load_dwordx4 v175, s[40:43], s66 offen offset:2048 lds\n"
"v_mfma_f32_16x16x32_bf16 v[200:203], v[144:147], a[200:203], v[200:203]\n"
"v_mfma_f32_16x16x32_bf16 v[204:207], v[144:147], a[204:207], v[204:207]\n"
"v_mfma_f32_16x16x32_bf16 v[208:211], v[128:131], a[192:195], v[208:211]\n"
"ds_read_b64_tr_b16 v[140:141], v162 offset:18432\n"
"ds_read_b64_tr_b16 v[142:143], v162 offset:26624\n"
"v_mfma_f32_16x16x32_bf16 v[212:215], v[128:131], a[196:199], v[212:215]\n"
"s_add_u32 s66, s72, 37248\n"
"buffer_load_dwordx4 v176, s[40:43], s66 offen offset:3072 lds\n"
"v_mfma_f32_16x16x32_bf16 v[216:219], v[128:131], a[200:203], v[216:219]\n"
"v_mfma_f32_16x16x32_bf16 v[220:223], v[128:131], a[204:207], v[220:223]\n"
"s_waitcnt lgkmcnt(2)\n"
"v_mfma_f32_16x16x32_bf16 v[192:195], v[132:135], v[224:227], v[192:195]\n"
"ds_read_b64_tr_b16 v[144:145], v163 offset:18432\n"
"ds_read_b64_tr_b16 v[146:147], v163 offset:26624\n"
"v_mfma_f32_16x16x32_bf16 v[196:199], v[132:135], v[228:231], v[196:199]\n"
"v_mfma_f32_16x16x32_bf16 v[200:203], v[132:135], v[232:235], v[200:203]\n"
"v_mfma_f32_16x16x32_bf16 v[204:207], v[132:135], v[236:239], v[204:207]\n"
"v_mfma_f32_16x16x32_bf16 v[208:211], v[136:139], v[224:227], v[208:211]\n"
"ds_read_b64_tr_b16 v[128:129], v162 offset:19456\n"
"ds_read_b64_tr_b16 v[130:131], v162 offset:27648\n"
"v_mfma_f32_16x16x32_bf16 v[212:215], v[136:139], v[228:231], v[212:215]\n"
"s_add_u32 m0, s65, 0\n"
"s_add_u32 s66, s72, 74112\n"
"buffer_load_dwordx4 v173, s[40:43], s66 offen lds\n"
"v_mfma_f32_16x16x32_bf16 v[216:219], v[136:139], v[232:235], v[216:219]\n"
"v_mfma_f32_16x16x32_bf16 v[220:223], v[136:139], v[236:239], v[220:223]\n"
"s_waitcnt lgkmcnt(2)\n"
"v_mfma_f32_16x16x32_bf16 v[192:195], v[140:143], v[240:243], v[192:195]\n"
"ds_read_b64_tr_b16 v[132:133], v163 offset:19456\n"
"ds_read_b64_tr_b16 v[134:135], v163 offset:27648\n"
"v_mfma_f32_16x16x32_bf16 v[196:199], v[140:143], v[244:247], v[196:199]\n"
"v_mfma_f32_16x16x32_bf16 v[200:203], v[140:143], v[248:251], v[200:203]\n"
"v_mfma_f32_16x16x32_bf16 v[204:207], v[140:143], v[252:255], v[204:207]\n"
"v_mfma_f32_16x16x32_bf16 v[208:211], v[144:147], v[240:243], v[208:211]\n"
"s_barrier\n"
"v_mfma_f32_16x16x32_bf16 v[212:215], v[144:147], v[244:247], v[212:215]\n"
"ds_read_b64_tr_b16 v[136:137], v162 offset:49152\n"
"ds_read_b64_tr_b16 v[138:139], v162 offset:57344\n"
"v_mfma_f32_16x16x32_bf16 v[216:219], v[144:147], v[248:251], v[216:219]\n"
"s_add_u32 s66, s72, 110976\n"
"buffer_load_dwordx4 v174, s[40:43], s66 offen offset:1024 lds\n"
"v_mfma_f32_16x16x32_bf16 v[220:223], v[144:147], v[252:255], v[220:223]\n"
"s_waitcnt lgkmcnt(2)\n"
"v_mfma_f32_16x16x32_bf16 v[0:3], v[128:131], a[128:131], v[0:3]\n"
"ds_read_b64_tr_b16 v[140:141], v163 offset:49152\n"
"ds_read_b64_tr_b16 v[142:143], v163 offset:57344\n"
"v_mfma_f32_16x16x32_bf16 v[4:7], v[128:131], a[132:135], v[4:7]\n"
"s_add_u32 s66, s72, 147840\n"
"buffer_load_dwordx4 v175, s[40:43], s66 offen offset:2048 lds\n"
"v_mfma_f32_16x16x32_bf16 v[8:11], v[128:131], a[136:139], v[8:11]\n"
"v_mfma_f32_16x16x32_bf16 v[12:15], v[128:131], a[140:143], v[12:15]\n"
"v_mfma_f32_16x16x32_bf16 v[16:19], v[132:135], a[128:131], v[16:19]\n"
"ds_read_b64_tr_b16 v[144:145], v162 offset:50176\n"
"ds_read_b64_tr_b16 v[146:147], v162 offset:58368\n"
"v_mfma_f32_16x16x32_bf16 v[20:23], v[132:135], a[132:135], v[20:23]\n"
"s_add_u32 s66, s72, 184704\n"
"buffer_load_dwordx4 v176, s[40:43], s66 offen offset:3072 lds\n"
"v_mfma_f32_16x16x32_bf16 v[24:27], v[132:135], a[136:139], v[24:27]\n"
"v_mfma_f32_16x16x32_bf16 v[28:31], v[132:135], a[140:143], v[28:31]\n"
"s_waitcnt lgkmcnt(2)\n"
"v_mfma_f32_16x16x32_bf16 v[0:3], v[136:139], a[144:147], v[0:3]\n"
"ds_read_b64_tr_b16 v[128:129], v163 offset:50176\n"
"ds_read_b64_tr_b16 v[130:131], v163 offset:58368\n"
"v_mfma_f32_16x16x32_bf16 v[4:7], v[136:139], a[148:151], v[4:7]\n"
"v_mfma_f32_16x16x32_bf16 v[8:11], v[136:139], a[152:155], v[8:11]\n"
"v_mfma_f32_16x16x32_bf16 v[12:15], v[136:139], a[156:159], v[12:15]\n"
"v_mfma_f32_16x16x32_bf16 v[16:19], v[140:143], a[144:147], v[16:19]\n"
"ds_read_b64_tr_b16 v[132:133], v162 offset:51200\n"
"ds_read_b64_tr_b16 v[134:135], v162 offset:59392\n"
"v_mfma_f32_16x16x32_bf16 v[20:23], v[140:143], a[148:151], v[20:23]\n"
"s_add_u32 m0, s65, 65536\n"
"s_add_u32 s66, s74, 384\n"
"buffer_load_dwordx4 v173, s[44:47], s66 offen lds\n"
"v_mfma_f32_16x16x32_bf16 v[24:27], v[140:143], a[152:155], v[24:27]\n"
"v_mfma_f32_16x16x32_bf16 v[28:31], v[140:143], a[156:159], v[28:31]\n"
"s_waitcnt lgkmcnt(2)\n"
"v_mfma_f32_16x16x32_bf16 v[0:3], v[144:147], a[160:163], v[0:3]\n"
"ds_read_b64_tr_b16 v[136:137], v163 offset:51200\n"
"ds_read_b64_tr_b16 v[138:139], v163 offset:59392\n"
"v_mfma_f32_16x16x32_bf16 v[4:7], v[144:147], a[164:167], v[4:7]\n"
"v_mfma_f32_16x16x32_bf16 v[8:11], v[144:147], a[168:171], v[8:11]\n"
"v_mfma_f32_16x16x32_bf16 v[12:15], v[144:147], a[172:175], v[12:15]\n"
"v_mfma_f32_16x16x32_bf16 v[16:19], v[128:131], a[160:163], v[16:19]\n"
"ds_read_b64_tr_b16 v[140:141], v162 offset:52224\n"
"ds_read_b64_tr_b16 v[142:143], v162 offset:60416\n"
"v_mfma_f32_16x16x32_bf16 v[20:23], v[128:131], a[164:167], v[20:23]\n"
"s_add_u32 s66, s74, 23424\n"
"buffer_load_dwordx4 v174, s[44:47], s66 offen offset:1024 lds\n"
"v_mfma_f32_16x16x32_bf16 v[24:27], v[128:131], a[168:171], v[24:27]\n"
"v_mfma_f32_16x16x32_bf16 v[28:31], v[128:131], a[172:175], v[28:31]\n"
"s_waitcnt lgkmcnt(2)\n"
"v_mfma_f32_16x16x32_bf16 v[0:3], v[132:135], a[176:179], v[0:3]\n"
"ds_read_b64_tr_b16 v[144:145], v163 offset:52224\n"
"ds_read_b64_tr_b16 v[146:147], v163 offset:60416\n"
"v_mfma_f32_16x16x32_bf16 v[4:7], v[132:135], a[180:183], v[4:7]\n"
"v_mfma_f32_16x16x32_bf16 v[8:11], v[132:135], a[184:187], v[8:11]\n"
"v_mfma_f32_16x16x32_bf16 v[12:15], v[132:135], a[188:191], v[12:15]\n"
"v_mfma_f32_16x16x32_bf16 v[16:19], v[136:139], a[176:179], v[16:19]\n"
"s_waitcnt vmcnt(10)\n"
"s_barrier\n"
"v_mfma_f32_16x16x32_bf16 v[20:23], v[136:139], a[180:183], v[20:23]\n"
"v_add_u32 v180, 147456, v167\n"
"ds_read_b128 v[64:67], v180\n"
"v_mfma_f32_16x16x32_bf16 v[24:27], v[136:139], a[184:187], v[24:27]\n"
"ds_read_b128 v[68:71], v180 offset:4096\n"
"ds_read_b128 v[72:75], v180 offset:8192\n"
"v_mfma_f32_16x16x32_bf16 v[28:31], v[136:139], a[188:191], v[28:31]\n"
"ds_read_b128 v[76:79], v180 offset:12288\n"
"s_waitcnt lgkmcnt(4)\n"
"v_mfma_f32_16x16x32_bf16 v[0:3], v[140:143], a[192:195], v[0:3]\n"
"ds_read_b64_tr_b16 v[128:129], v160 offset:16384\n"
"ds_read_b64_tr_b16 v[130:131], v160 offset:24576\n"
"v_mfma_f32_16x16x32_bf16 v[4:7], v[140:143], a[196:199], v[4:7]\n"
"v_add_u32 v180, 147456, v167\n"
"ds_read_b128 v[80:83], v180 offset:2048\n"
"v_mfma_f32_16x16x32_bf16 v[8:11], v[140:143], a[200:203], v[8:11]\n"
"ds_read_b128 v[84:87], v180 offset:6144\n"
"ds_read_b128 v[88:91], v180 offset:10240\n"
"v_mfma_f32_16x16x32_bf16 v[12:15], v[140:143], a[204:207], v[12:15]\n"
"ds_read_b128 v[92:95], v180 offset:14336\n"
"ds_read_b64_tr_b16 v[132:133], v161 offset:16384\n"
"v_mfma_f32_16x16x32_bf16 v[16:19], v[144:147], a[192:195], v[16:19]\n"
"ds_read_b64_tr_b16 v[134:135], v161 offset:24576\n"
"ds_read_b64_tr_b16 v[136:137], v160 offset:17408\n"
"v_mfma_f32_16x16x32_bf16 v[20:23], v[144:147], a[196:199], v[20:23]\n"
"ds_read_b64_tr_b16 v[138:139], v160 offset:25600\n"
"s_add_u32 s66, s72, 221568\n"
"buffer_load_dwordx4 v175, s[40:43], s66 offen offset:2048 lds\n"
"v_mfma_f32_16x16x32_bf16 v[24:27], v[144:147], a[200:203], v[24:27]\n"
"s_add_u32 s66, s72, 258432\n"
"buffer_load_dwordx4 v176, s[40:43], s66 offen offset:3072 lds\n"
"v_mfma_f32_16x16x32_bf16 v[28:31], v[144:147], a[204:207], v[28:31]\n"
"s_waitcnt lgkmcnt(2)\n"
"v_mfma_f32_16x16x32_bf16 v[0:3], v[128:131], v[224:227], v[0:3]\n"
"ds_read_b64_tr_b16 v[140:141], v161 offset:17408\n"
"ds_read_b64_tr_b16 v[142:143], v161 offset:25600\n"
"v_mfma_f32_16x16x32_bf16 v[4:7], v[128:131], v[228:231], v[4:7]\n"
"s_add_u32 m0, s65, 81920\n"
"s_add_u32 s66, s73, 384\n"
"buffer_load_dwordx4 v173, s[40:43], s66 offen lds\n"
"v_mfma_f32_16x16x32_bf16 v[8:11], v[128:131], v[232:235], v[8:11]\n"
"v_mfma_f32_16x16x32_bf16 v[12:15], v[128:131], v[236:239], v[12:15]\n"
"v_mfma_f32_16x16x32_bf16 v[16:19], v[132:135], v[224:227], v[16:19]\n"
"ds_read_b64_tr_b16 v[144:145], v160 offset:18432\n"
"ds_read_b64_tr_b16 v[146:147], v160 offset:26624\n"
"v_mfma_f32_16x16x32_bf16 v[20:23], v[132:135], v[228:231], v[20:23]\n"
"s_add_u32 s66, s24, 384\n"
"buffer_load_dwordx4 v174, s[40:43], s66 offen offset:1024 lds\n"
"v_mfma_f32_16x16x32_bf16 v[24:27], v[132:135], v[232:235], v[24:27]\n"
"v_mfma_f32_16x16x32_bf16 v[28:31], v[132:135], v[236:239], v[28:31]\n"
"s_waitcnt lgkmcnt(2)\n"
"v_mfma_f32_16x16x32_bf16 v[0:3], v[136:139], v[240:243], v[0:3]\n"
"ds_read_b64_tr_b16 v[128:129], v161 offset:18432\n"
"ds_read_b64_tr_b16 v[130:131], v161 offset:26624\n"
"v_mfma_f32_16x16x32_bf16 v[4:7], v[136:139], v[244:247], v[4:7]\n"
"s_add_u32 s66, s24, 37248\n"
"buffer_load_dwordx4 v175, s[40:43], s66 offen offset:2048 lds\n"
"v_mfma_f32_16x16x32_bf16 v[8:11], v[136:139], v[248:251], v[8:11]\n"
"v_mfma_f32_16x16x32_bf16 v[12:15], v[136:139], v[252:255], v[12:15]\n"
"v_mfma_f32_16x16x32_bf16 v[16:19], v[140:143], v[240:243], v[16:19]\n"
"ds_read_b64_tr_b16 v[132:133], v160 offset:19456\n"
"ds_read_b64_tr_b16 v[134:135], v160 offset:27648\n"
"v_mfma_f32_16x16x32_bf16 v[20:23], v[140:143], v[244:247], v[20:23]\n"
"s_add_u32 s66, s24, 74112\n"
"buffer_load_dwordx4 v176, s[40:43], s66 offen offset:3072 lds\n"
"v_mfma_f32_16x16x32_bf16 v[24:27], v[140:143], v[248:251], v[24:27]\n"
"v_mfma_f32_16x16x32_bf16 v[28:31], v[140:143], v[252:255], v[28:31]\n"
"s_waitcnt lgkmcnt(2)\n"
"v_mfma_f32_16x16x32_bf16 v[96:99], v[144:147], v[32:35], v[96:99]\n"
"ds_read_b64_tr_b16 v[136:137], v161 offset:19456\n"
"ds_read_b64_tr_b16 v[138:139], v161 offset:27648\n"
"v_mfma_f32_16x16x32_bf16 v[100:103], v[144:147], v[36:39], v[100:103]\n"
"v_mfma_f32_16x16x32_bf16 v[104:107], v[144:147], v[40:43], v[104:107]\n"
"v_mfma_f32_16x16x32_bf16 v[108:111], v[144:147], v[44:47], v[108:111]\n"
"v_mfma_f32_16x16x32_bf16 v[112:115], v[128:131], v[32:35], v[112:115]\n"
"s_barrier\n"
"v_mfma_f32_16x16x32_bf16 v[116:119], v[128:131], v[36:39], v[116:119]\n"
"ds_read_b64_tr_b16 v[140:141], v160 offset:32768\n"
"ds_read_b64_tr_b16 v[142:143], v160 offset:40960\n"
"v_mfma_f32_16x16x32_bf16 v[120:123], v[128:131], v[40:43], v[120:123]\n"
"s_add_u32 m0, s65, 131072\n"
"s_add_u32 s66, s24, 110976\n"
"buffer_load_dwordx4 v173, s[40:43], s66 offen lds\n"
"v_mfma_f32_16x16x32_bf16 v[124:127], v[128:131], v[44:47], v[124:127]\n"
"s_waitcnt lgkmcnt(2)\n"
"v_mfma_f32_16x16x32_bf16 v[96:99], v[132:135], v[48:51], v[96:99]\n"
"ds_read_b64_tr_b16 v[144:145], v161 offset:32768\n"
"ds_read_b64_tr_b16 v[146:147], v161 offset:40960\n"
"v_mfma_f32_16x16x32_bf16 v[100:103], v[132:135], v[52:55], v[100:103]\n"
"s_add_u32 s66, s24, 147840\n"
"buffer_load_dwordx4 v174, s[40:43], s66 offen offset:1024 lds\n"
"v_mfma_f32_16x16x32_bf16 v[104:107], v[132:135], v[56:59], v[104:107]\n"
"v_mfma_f32_16x16x32_bf16 v[108:111], v[132:135], v[60:63], v[108:111]\n"
"v_mfma_f32_16x16x32_bf16 v[112:115], v[136:139], v[48:51], v[112:115]\n"
"ds_read_b64_tr_b16 v[128:129], v160 offset:33792\n"
"ds_read_b64_tr_b16 v[130:131], v160 offset:41984\n"
"v_mfma_f32_16x16x32_bf16 v[116:119], v[136:139], v[52:55], v[116:119]\n"
"s_add_u32 s66, s24, 184704\n"
"buffer_load_dwordx4 v175, s[40:43], s66 offen offset:2048 lds\n"
"v_mfma_f32_16x16x32_bf16 v[120:123], v[136:139], v[56:59], v[120:123]\n"
"v_mfma_f32_16x16x32_bf16 v[124:127], v[136:139], v[60:63], v[124:127]\n"
"s_waitcnt lgkmcnt(2)\n"
"v_mfma_f32_16x16x32_bf16 v[96:99], v[140:143], v[64:67], v[96:99]\n"
"ds_read_b64_tr_b16 v[132:133], v161 offset:33792\n"
"ds_read_b64_tr_b16 v[134:135], v161 offset:41984\n"
"v_mfma_f32_16x16x32_bf16 v[100:103], v[140:143], v[68:71], v[100:103]\n"
"s_add_u32 s66, s26, 384\n"
"buffer_load_dwordx4 v176, s[44:47], s66 offen offset:3072 lds\n"
"v_mfma_f32_16x16x32_bf16 v[104:107], v[140:143], v[72:75], v[104:107]\n"
"v_mfma_f32_16x16x32_bf16 v[108:111], v[140:143], v[76:79], v[108:111]\n"
"v_mfma_f32_16x16x32_bf16 v[112:115], v[144:147], v[64:67], v[112:115]\n"
"ds_read_b64_tr_b16 v[136:137], v160 offset:34816\n"
"ds_read_b64_tr_b16 v[138:139], v160 offset:43008\n"
"v_mfma_f32_16x16x32_bf16 v[116:119], v[144:147], v[68:71], v[116:119]\n"
"v_mfma_f32_16x16x32_bf16 v[120:123], v[144:147], v[72:75], v[120:123]\n"
"v_mfma_f32_16x16x32_bf16 v[124:127], v[144:147], v[76:79], v[124:127]\n"
"s_waitcnt lgkmcnt(2)\n"
"v_mfma_f32_16x16x32_bf16 v[96:99], v[128:131], v[80:83], v[96:99]\n"
"ds_read_b64_tr_b16 v[140:141], v161 offset:34816\n"
"ds_read_b64_tr_b16 v[142:143], v161 offset:43008\n"
"v_mfma_f32_16x16x32_bf16 v[100:103], v[128:131], v[84:87], v[100:103]\n"
"s_add_u32 m0, s65, 114688\n"
"s_add_u32 s66, s26, 23424\n"
"buffer_load_dwordx4 v173, s[44:47], s66 offen lds\n"
"v_mfma_f32_16x16x32_bf16 v[104:107], v[128:131], v[88:91], v[104:107]\n"
"v_mfma_f32_16x16x32_bf16 v[108:111], v[128:131], v[92:95], v[108:111]\n"
"v_mfma_f32_16x16x32_bf16 v[112:115], v[132:135], v[80:83], v[112:115]\n"
"ds_read_b64_tr_b16 v[144:145], v160 offset:35840\n"
"ds_read_b64_tr_b16 v[146:147], v160 offset:44032\n"
"v_mfma_f32_16x16x32_bf16 v[116:119], v[132:135], v[84:87], v[116:119]\n"
"v_mfma_f32_16x16x32_bf16 v[120:123], v[132:135], v[88:91], v[120:123]\n"
"v_mfma_f32_16x16x32_bf16 v[124:127], v[132:135], v[92:95], v[124:127]\n"
"s_waitcnt lgkmcnt(2)\n"
"v_mfma_f32_16x16x32_bf16 v[192:195], v[136:139], v[32:35], v[192:195]\n"
"ds_read_b64_tr_b16 v[128:129], v161 offset:35840\n"
"ds_read_b64_tr_b16 v[130:131], v161 offset:44032\n"
"v_mfma_f32_16x16x32_bf16 v[196:199], v[136:139], v[36:39], v[196:199]\n"
"s_add_u32 s66, s24, 221568\n"
"buffer_load_dwordx4 v174, s[40:43], s66 offen offset:1024 lds\n"
"v_mfma_f32_16x16x32_bf16 v[200:203], v[136:139], v[40:43], v[200:203]\n"
"v_mfma_f32_16x16x32_bf16 v[204:207], v[136:139], v[44:47], v[204:207]\n"
"v_mfma_f32_16x16x32_bf16 v[208:211], v[140:143], v[32:35], v[208:211]\n"
"s_barrier\n"
"v_mfma_f32_16x16x32_bf16 v[212:215], v[140:143], v[36:39], v[212:215]\n"
"ds_read_b64_tr_b16 v[132:133], v162 offset:32768\n"
"ds_read_b64_tr_b16 v[134:135], v162 offset:40960\n"
"v_mfma_f32_16x16x32_bf16 v[216:219], v[140:143], v[40:43], v[216:219]\n"
"s_add_u32 s66, s24, 258432\n"
"buffer_load_dwordx4 v175, s[40:43], s66 offen offset:2048 lds\n"
"v_mfma_f32_16x16x32_bf16 v[220:223], v[140:143], v[44:47], v[220:223]\n"
"s_waitcnt lgkmcnt(2)\n"
"v_mfma_f32_16x16x32_bf16 v[192:195], v[144:147], v[48:51], v[192:195]\n"
"ds_read_b64_tr_b16 v[136:137], v163 offset:32768\n"
"ds_read_b64_tr_b16 v[138:139], v163 offset:40960\n"
"v_mfma_f32_16x16x32_bf16 v[196:199], v[144:147], v[52:55], v[196:199]\n"
"s_add_u32 s66, s25, 384\n"
"buffer_load_dwordx4 v176, s[40:43], s66 offen offset:3072 lds\n"
"v_mfma_f32_16x16x32_bf16 v[200:203], v[144:147], v[56:59], v[200:203]\n"
"s_lshl_b32 s16, s62, 13\n"
"s_lshl_b32 s17, s61, 12\n"
"s_add_u32 s16, s16, s17\n"
"s_add_u32 s16, s16, s95\n"
"s_add_u32 s17, s16, 0\n"
"v_mfma_f32_16x16x32_bf16 v[204:207], v[144:147], v[60:63], v[204:207]\n"
"v_cvt_pk_bf16_f32 v152, v96, v97\n"
"v_cvt_pk_bf16_f32 v153, v98, v99\n"
"v_mfma_f32_16x16x32_bf16 v[208:211], v[128:131], v[48:51], v[208:211]\n"
"ds_read_b64_tr_b16 v[140:141], v162 offset:33792\n"
"ds_read_b64_tr_b16 v[142:143], v162 offset:41984\n"
"v_mfma_f32_16x16x32_bf16 v[212:215], v[128:131], v[52:55], v[212:215]\n"
"v_cvt_pk_bf16_f32 v154, v112, v113\n"
"v_cvt_pk_bf16_f32 v155, v114, v115\n"
"v_mfma_f32_16x16x32_bf16 v[216:219], v[128:131], v[56:59], v[216:219]\n"
"buffer_store_dwordx4 v[152:155], v158, s[48:51], s17 offen sc1\n"
"s_nop 1\n"
"v_cvt_pk_bf16_f32 v152, v100, v101\n"
"v_mfma_f32_16x16x32_bf16 v[220:223], v[128:131], v[60:63], v[220:223]\n"
"v_cvt_pk_bf16_f32 v153, v102, v103\n"
"s_waitcnt lgkmcnt(2)\n"
"v_mfma_f32_16x16x32_bf16 v[192:195], v[132:135], v[64:67], v[192:195]\n"
"ds_read_b64_tr_b16 v[144:145], v163 offset:33792\n"
"ds_read_b64_tr_b16 v[146:147], v163 offset:41984\n"
"v_mfma_f32_16x16x32_bf16 v[196:199], v[132:135], v[68:71], v[196:199]\n"
"s_add_u32 m0, s65, 16384\n"
"s_add_u32 s66, s71, 3072\n"
"buffer_load_dwordx4 v166, s[20:23], s66 offen sc1 lds\n"
"v_mfma_f32_16x16x32_bf16 v[200:203], v[132:135], v[72:75], v[200:203]\n"
"v_cvt_pk_bf16_f32 v154, v116, v117\n"
"v_cvt_pk_bf16_f32 v155, v118, v119\n"
"v_mfma_f32_16x16x32_bf16 v[204:207], v[132:135], v[76:79], v[204:207]\n"
"buffer_store_dwordx4 v[152:155], v158, s[48:51], s17 offen offset:1024 sc1\n"
"s_nop 1\n"
"v_cvt_pk_bf16_f32 v152, v104, v105\n"
"v_mfma_f32_16x16x32_bf16 v[208:211], v[136:139], v[64:67], v[208:211]\n"
"ds_read_b64_tr_b16 v[128:129], v162 offset:34816\n"
"ds_read_b64_tr_b16 v[130:131], v162 offset:43008\n"
"v_mfma_f32_16x16x32_bf16 v[212:215], v[136:139], v[68:71], v[212:215]\n"
"v_cvt_pk_bf16_f32 v153, v106, v107\n"
"v_cvt_pk_bf16_f32 v154, v120, v121\n"
"v_mfma_f32_16x16x32_bf16 v[216:219], v[136:139], v[72:75], v[216:219]\n"
"v_cvt_pk_bf16_f32 v155, v122, v123\n"
"buffer_store_dwordx4 v[152:155], v158, s[48:51], s17 offen offset:2048 sc1\n"
"v_mfma_f32_16x16x32_bf16 v[220:223], v[136:139], v[76:79], v[220:223]\n"
"s_nop 0\n"
"v_cvt_pk_bf16_f32 v152, v108, v109\n"
"s_waitcnt lgkmcnt(2)\n"
"v_mfma_f32_16x16x32_bf16 v[192:195], v[140:143], v[80:83], v[192:195]\n"
"ds_read_b64_tr_b16 v[132:133], v163 offset:34816\n"
"ds_read_b64_tr_b16 v[134:135], v163 offset:43008\n"
"v_mfma_f32_16x16x32_bf16 v[196:199], v[140:143], v[84:87], v[196:199]\n"
"s_add_u32 s66, s71, 6144\n"
"buffer_load_dwordx4 v166, s[20:23], s66 offen offset:1024 sc1 lds\n"
"v_mfma_f32_16x16x32_bf16 v[200:203], v[140:143], v[88:91], v[200:203]\n"
"v_cvt_pk_bf16_f32 v153, v110, v111\n"
"v_cvt_pk_bf16_f32 v154, v124, v125\n"
"v_mfma_f32_16x16x32_bf16 v[204:207], v[140:143], v[92:95], v[204:207]\n"
"v_cvt_pk_bf16_f32 v155, v126, v127\n"
"buffer_store_dwordx4 v[152:155], v158, s[48:51], s17 offen offset:3072 sc1\n"
"v_mfma_f32_16x16x32_bf16 v[208:211], v[144:147], v[80:83], v[208:211]\n"
"ds_read_b64_tr_b16 v[136:137], v162 offset:35840\n"
"ds_read_b64_tr_b16 v[138:139], v162 offset:44032\n"
"v_mfma_f32_16x16x32_bf16 v[212:215], v[144:147], v[84:87], v[212:215]\n"
"v_mfma_f32_16x16x32_bf16 v[216:219], v[144:147], v[88:91], v[216:219]\n"
"v_mfma_f32_16x16x32_bf16 v[220:223], v[144:147], v[92:95], v[220:223]\n"
"s_waitcnt lgkmcnt(2)\n"
"v_mfma_f32_16x16x32_bf16 v[0:3], v[128:131], v[32:35], v[0:3]\n"
"ds_read_b64_tr_b16 v[140:141], v163 offset:35840\n"
"ds_read_b64_tr_b16 v[142:143], v163 offset:44032\n"
"v_mfma_f32_16x16x32_bf16 v[4:7], v[128:131], v[36:39], v[4:7]\n"
"s_add_u32 s66, s71, 9216\n"
"buffer_load_dwordx4 v166, s[20:23], s66 offen offset:2048 sc1 lds\n"
"v_mfma_f32_16x16x32_bf16 v[8:11], v[128:131], v[40:43], v[8:11]\n"
"v_mfma_f32_16x16x32_bf16 v[12:15], v[128:131], v[44:47], v[12:15]\n"
"v_mfma_f32_16x16x32_bf16 v[16:19], v[132:135], v[32:35], v[16:19]\n"
"s_waitcnt vmcnt(27)\n"
"s_barrier\n"
"v_mfma_f32_16x16x32_bf16 v[20:23], v[132:135], v[36:39], v[20:23]\n"
"ds_read_b64_tr_b16 v[144:145], v160 offset:49152\n"
"ds_read_b64_tr_b16 v[146:147], v160 offset:57344\n"
"v_mfma_f32_16x16x32_bf16 v[24:27], v[132:135], v[40:43], v[24:27]\n"
"s_add_u32 s66, s71, 12288\n"
"buffer_load_dwordx4 v166, s[20:23], s66 offen offset:3072 sc1 lds\n"
"v_mfma_f32_16x16x32_bf16 v[28:31], v[132:135], v[44:47], v[28:31]\n"
"s_waitcnt lgkmcnt(2)\n"
"v_mfma_f32_16x16x32_bf16 v[0:3], v[136:139], v[48:51], v[0:3]\n"
"ds_read_b64_tr_b16 v[128:129], v161 offset:49152\n"
"ds_read_b64_tr_b16 v[130:131], v161 offset:57344\n"
"v_mfma_f32_16x16x32_bf16 v[4:7], v[136:139], v[52:55], v[4:7]\n"
"s_lshl_b32 s16, s62, 13\n"
"s_lshl_b32 s17, s61, 12\n"
"s_add_u32 s16, s16, s17\n"
"s_add_u32 s16, s16, s95\n"
"s_add_u32 s17, s16, 16384\n"
"v_mfma_f32_16x16x32_bf16 v[8:11], v[136:139], v[56:59], v[8:11]\n"
"v_cvt_pk_bf16_f32 v152, v192, v193\n"
"v_cvt_pk_bf16_f32 v153, v194, v195\n"
"v_mfma_f32_16x16x32_bf16 v[12:15], v[136:139], v[60:63], v[12:15]\n"
"v_cvt_pk_bf16_f32 v154, v208, v209\n"
"v_cvt_pk_bf16_f32 v155, v210, v211\n"
"v_mfma_f32_16x16x32_bf16 v[16:19], v[140:143], v[48:51], v[16:19]\n"
"ds_read_b64_tr_b16 v[132:133], v160 offset:50176\n"
"ds_read_b64_tr_b16 v[134:135], v160 offset:58368\n"
"v_mfma_f32_16x16x32_bf16 v[20:23], v[140:143], v[52:55], v[20:23]\n"
"s_add_u32 m0, s65, 32768\n"
"s_add_u32 s66, s71, 19456\n"
"buffer_load_dwordx4 v166, s[20:23], s66 offen sc1 lds\n"
"v_mfma_f32_16x16x32_bf16 v[24:27], v[140:143], v[56:59], v[24:27]\n"
"buffer_store_dwordx4 v[152:155], v158, s[48:51], s17 offen sc1\n"
"s_nop 1\n"
"v_cvt_pk_bf16_f32 v152, v196, v197\n"
"v_mfma_f32_16x16x32_bf16 v[28:31], v[140:143], v[60:63], v[28:31]\n"
"v_cvt_pk_bf16_f32 v153, v198, v199\n"
"s_waitcnt lgkmcnt(2)\n"
"v_mfma_f32_16x16x32_bf16 v[0:3], v[144:147], v[64:67], v[0:3]\n"
"ds_read_b64_tr_b16 v[136:137], v161 offset:50176\n"
"ds_read_b64_tr_b16 v[138:139], v161 offset:58368\n"
"v_mfma_f32_16x16x32_bf16 v[4:7], v[144:147], v[68:71], v[4:7]\n"
"v_cvt_pk_bf16_f32 v154, v212, v213\n"
"v_cvt_pk_bf16_f32 v155, v214, v215\n"
"v_mfma_f32_16x16x32_bf16 v[8:11], v[144:147], v[72:75], v[8:11]\n"
"buffer_store_dwordx4 v[152:155], v158, s[48:51], s17 offen offset:1024 sc1\n"
"s_nop 1\n"
"v_cvt_pk_bf16_f32 v152, v200, v201\n"
"v_mfma_f32_16x16x32_bf16 v[12:15], v[144:147], v[76:79], v[12:15]\n"
"v_cvt_pk_bf16_f32 v153, v202, v203\n"
"v_cvt_pk_bf16_f32 v154, v216, v217\n"
"v_mfma_f32_16x16x32_bf16 v[16:19], v[128:131], v[64:67], v[16:19]\n"
"ds_read_b64_tr_b16 v[140:141], v160 offset:51200\n"
"ds_read_b64_tr_b16 v[142:143], v160 offset:59392\n"
"v_mfma_f32_16x16x32_bf16 v[20:23], v[128:131], v[68:71], v[20:23]\n"
"s_add_u32 s66, s71, 22528\n"
"buffer_load_dwordx4 v166, s[20:23], s66 offen offset:1024 sc1 lds\n"
"v_mfma_f32_16x16x32_bf16 v[24:27], v[128:131], v[72:75], v[24:27]\n"
"v_cvt_pk_bf16_f32 v155, v218, v219\n"
"buffer_store_dwordx4 v[152:155], v158, s[48:51], s17 offen offset:2048 sc1\n"
"v_mfma_f32_16x16x32_bf16 v[28:31], v[128:131], v[76:79], v[28:31]\n"
"s_nop 0\n"
"v_cvt_pk_bf16_f32 v152, v204, v205\n"
"s_waitcnt lgkmcnt(2)\n"
"v_mfma_f32_16x16x32_bf16 v[0:3], v[132:135], v[80:83], v[0:3]\n"
"ds_read_b64_tr_b16 v[144:145], v161 offset:51200\n"
"ds_read_b64_tr_b16 v[146:147], v161 offset:59392\n"
"v_mfma_f32_16x16x32_bf16 v[4:7], v[132:135], v[84:87], v[4:7]\n"
"v_cvt_pk_bf16_f32 v153, v206, v207\n"
"v_cvt_pk_bf16_f32 v154, v220, v221\n"
"v_mfma_f32_16x16x32_bf16 v[8:11], v[132:135], v[88:91], v[8:11]\n"
"v_cvt_pk_bf16_f32 v155, v222, v223\n"
"buffer_store_dwordx4 v[152:155], v158, s[48:51], s17 offen offset:3072 sc1\n"
"v_mfma_f32_16x16x32_bf16 v[12:15], v[132:135], v[92:95], v[12:15]\n"
"v_mfma_f32_16x16x32_bf16 v[16:19], v[136:139], v[80:83], v[16:19]\n"
"ds_read_b64_tr_b16 v[128:129], v160 offset:52224\n"
"ds_read_b64_tr_b16 v[130:131], v160 offset:60416\n"
"v_mfma_f32_16x16x32_bf16 v[20:23], v[136:139], v[84:87], v[20:23]\n"
"s_add_u32 s66, s71, 25600\n"
"buffer_load_dwordx4 v166, s[20:23], s66 offen offset:2048 sc1 lds\n"
"v_mfma_f32_16x16x32_bf16 v[24:27], v[136:139], v[88:91], v[24:27]\n"
"v_mfma_f32_16x16x32_bf16 v[28:31], v[136:139], v[92:95], v[28:31]\n"
"s_waitcnt lgkmcnt(2)\n"
"v_mfma_f32_16x16x32_bf16 v[96:99], v[140:143], a[0:3], 0\n"
"ds_read_b64_tr_b16 v[132:133], v161 offset:52224\n"
"ds_read_b64_tr_b16 v[134:135], v161 offset:60416\n"
"v_mfma_f32_16x16x32_bf16 v[100:103], v[140:143], a[4:7], 0\n"
"v_mfma_f32_16x16x32_bf16 v[104:107], v[140:143], a[8:11], 0\n"
"v_mfma_f32_16x16x32_bf16 v[108:111], v[140:143], a[12:15], 0\n"
"v_mfma_f32_16x16x32_bf16 v[112:115], v[144:147], a[0:3], 0\n"
"s_waitcnt vmcnt(31)\n"
"s_barrier\n"
"v_mfma_f32_16x16x32_bf16 v[116:119], v[144:147], a[4:7], 0\n"
"ds_read_b64_tr_b16 v[136:137], v160\n"
"ds_read_b64_tr_b16 v[138:139], v160 offset:8192\n"
"v_mfma_f32_16x16x32_bf16 v[120:123], v[144:147], a[8:11], 0\n"
"s_add_u32 s66, s71, 28672\n"
"buffer_load_dwordx4 v166, s[20:23], s66 offen offset:3072 sc1 lds\n"
"v_mfma_f32_16x16x32_bf16 v[124:127], v[144:147], a[12:15], 0\n"
"s_waitcnt lgkmcnt(2)\n"
"v_mfma_f32_16x16x32_bf16 v[96:99], v[128:131], a[16:19], v[96:99]\n"
"ds_read_b64_tr_b16 v[140:141], v161\n"
"ds_read_b64_tr_b16 v[142:143], v161 offset:8192\n"
"v_mfma_f32_16x16x32_bf16 v[100:103], v[128:131], a[20:23], v[100:103]\n"
"s_lshl_b32 s16, s62, 13\n"
"s_lshl_b32 s17, s61, 12\n"
"s_add_u32 s16, s16, s17\n"
"s_add_u32 s16, s16, s95\n"
"s_add_u32 s17, s16, 32768\n"
"v_mfma_f32_16x16x32_bf16 v[104:107], v[128:131], a[24:27], v[104:107]\n"
"v_cvt_pk_bf16_f32 v152, v0, v1\n"
"v_cvt_pk_bf16_f32 v153, v2, v3\n"
"v_mfma_f32_16x16x32_bf16 v[108:111], v[128:131], a[28:31], v[108:111]\n"
"v_cvt_pk_bf16_f32 v154, v16, v17\n"
"v_cvt_pk_bf16_f32 v155, v18, v19\n"
"v_mfma_f32_16x16x32_bf16 v[112:115], v[132:135], a[16:19], v[112:115]\n"
"ds_read_b64_tr_b16 v[144:145], v160 offset:1024\n"
"ds_read_b64_tr_b16 v[146:147], v160 offset:9216\n"
"v_mfma_f32_16x16x32_bf16 v[116:119], v[132:135], a[20:23], v[116:119]\n"
"s_add_u32 m0, s65, 98304\n"
"s_add_u32 s66, s71, 35840\n"
"buffer_load_dwordx4 v166, s[20:23], s66 offen sc1 lds\n"
"v_mfma_f32_16x16x32_bf16 v[120:123], v[132:135], a[24:27], v[120:123]\n"
"buffer_store_dwordx4 v[152:155], v158, s[48:51], s17 offen sc1\n"
"s_nop 1\n"
"v_cvt_pk_bf16_f32 v152, v4, v5\n"
"v_mfma_f32_16x16x32_bf16 v[124:127], v[132:135], a[28:31], v[124:127]\n"
"v_cvt_pk_bf16_f32 v153, v6, v7\n"
"s_waitcnt lgkmcnt(2)\n"
"v_mfma_f32_16x16x32_bf16 v[96:99], v[136:139], a[32:35], v[96:99]\n"
"ds_read_b64_tr_b16 v[128:129], v161 offset:1024\n"
"ds_read_b64_tr_b16 v[130:131], v161 offset:9216\n"
"v_mfma_f32_16x16x32_bf16 v[100:103], v[136:139], a[36:39], v[100:103]\n"
"v_cvt_pk_bf16_f32 v154, v20, v21\n"
"v_cvt_pk_bf16_f32 v155, v22, v23\n"
"v_mfma_f32_16x16x32_bf16 v[104:107], v[136:139], a[40:43], v[104:107]\n"
"buffer_store_dwordx4 v[152:155], v158, s[48:51], s17 offen offset:1024 sc1\n"
"s_nop 1\n"
"v_cvt_pk_bf16_f32 v152, v8, v9\n"
"v_mfma_f32_16x16x32_bf16 v[108:111], v[136:139], a[44:47], v[108:111]\n"
"v_cvt_pk_bf16_f32 v153, v10, v11\n"
"v_cvt_pk_bf16_f32 v154, v24, v25\n"
"v_mfma_f32_16x16x32_bf16 v[112:115], v[140:143], a[32:35], v[112:115]\n"
"ds_read_b64_tr_b16 v[132:133], v160 offset:2048\n"
"ds_read_b64_tr_b16 v[134:135], v160 offset:10240\n"
"v_mfma_f32_16x16x32_bf16 v[116:119], v[140:143], a[36:39], v[116:119]\n"
"s_add_u32 s66, s71, 38912\n"
"buffer_load_dwordx4 v166, s[20:23], s66 offen offset:1024 sc1 lds\n"
"v_mfma_f32_16x16x32_bf16 v[120:123], v[140:143], a[40:43], v[120:123]\n"
"v_cvt_pk_bf16_f32 v155, v26, v27\n"
"buffer_store_dwordx4 v[152:155], v158, s[48:51], s17 offen offset:2048 sc1\n"
"v_mfma_f32_16x16x32_bf16 v[124:127], v[140:143], a[44:47], v[124:127]\n"
"s_nop 0\n"
"v_cvt_pk_bf16_f32 v152, v12, v13\n"
"s_waitcnt lgkmcnt(2)\n"
"v_mfma_f32_16x16x32_bf16 v[96:99], v[144:147], a[48:51], v[96:99]\n"
"ds_read_b64_tr_b16 v[136:137], v161 offset:2048\n"
"ds_read_b64_tr_b16 v[138:139], v161 offset:10240\n"
"v_mfma_f32_16x16x32_bf16 v[100:103], v[144:147], a[52:55], v[100:103]\n"
"v_cvt_pk_bf16_f32 v153, v14, v15\n"
"v_cvt_pk_bf16_f32 v154, v28, v29\n"
"v_mfma_f32_16x16x32_bf16 v[104:107], v[144:147], a[56:59], v[104:107]\n"
"v_cvt_pk_bf16_f32 v155, v30, v31\n"
"buffer_store_dwordx4 v[152:155], v158, s[48:51], s17 offen offset:3072 sc1\n"
"v_mfma_f32_16x16x32_bf16 v[108:111], v[144:147], a[60:63], v[108:111]\n"
"v_mfma_f32_16x16x32_bf16 v[112:115], v[128:131], a[48:51], v[112:115]\n"
"ds_read_b64_tr_b16 v[140:141], v160 offset:3072\n"
"ds_read_b64_tr_b16 v[142:143], v160 offset:11264\n"
"v_mfma_f32_16x16x32_bf16 v[116:119], v[128:131], a[52:55], v[116:119]\n"
"s_add_u32 s66, s71, 41984\n"
"buffer_load_dwordx4 v166, s[20:23], s66 offen offset:2048 sc1 lds\n"
"v_mfma_f32_16x16x32_bf16 v[120:123], v[128:131], a[56:59], v[120:123]\n"
"v_mfma_f32_16x16x32_bf16 v[124:127], v[128:131], a[60:63], v[124:127]\n"
"s_waitcnt lgkmcnt(2)\n"
"v_mfma_f32_16x16x32_bf16 v[96:99], v[132:135], a[64:67], v[96:99]\n"
"ds_read_b64_tr_b16 v[144:145], v161 offset:3072\n"
"ds_read_b64_tr_b16 v[146:147], v161 offset:11264\n"
"v_mfma_f32_16x16x32_bf16 v[100:103], v[132:135], a[68:71], v[100:103]\n"
"v_mfma_f32_16x16x32_bf16 v[104:107], v[132:135], a[72:75], v[104:107]\n"
"v_mfma_f32_16x16x32_bf16 v[108:111], v[132:135], a[76:79], v[108:111]\n"
"v_mfma_f32_16x16x32_bf16 v[112:115], v[136:139], a[64:67], v[112:115]\n"
"s_waitcnt vmcnt(35)\n"
"s_barrier\n"
"v_mfma_f32_16x16x32_bf16 v[116:119], v[136:139], a[68:71], v[116:119]\n"
"ds_read_b64_tr_b16 v[128:129], v162\n"
"ds_read_b64_tr_b16 v[130:131], v162 offset:8192\n"
"v_mfma_f32_16x16x32_bf16 v[120:123], v[136:139], a[72:75], v[120:123]\n"
"v_mfma_f32_16x16x32_bf16 v[124:127], v[136:139], a[76:79], v[124:127]\n"
"s_waitcnt lgkmcnt(2)\n"
"v_mfma_f32_16x16x32_bf16 v[96:99], v[140:143], a[80:83], v[96:99]\n"
"ds_read_b64_tr_b16 v[132:133], v163\n"
"ds_read_b64_tr_b16 v[134:135], v163 offset:8192\n"
"v_mfma_f32_16x16x32_bf16 v[100:103], v[140:143], a[84:87], v[100:103]\n"
"v_mfma_f32_16x16x32_bf16 v[104:107], v[140:143], a[88:91], v[104:107]\n"
"v_mfma_f32_16x16x32_bf16 v[108:111], v[140:143], a[92:95], v[108:111]\n"
"v_mfma_f32_16x16x32_bf16 v[112:115], v[144:147], a[80:83], v[112:115]\n"
"ds_read_b64_tr_b16 v[136:137], v162 offset:1024\n"
"ds_read_b64_tr_b16 v[138:139], v162 offset:9216\n"
"v_mfma_f32_16x16x32_bf16 v[116:119], v[144:147], a[84:87], v[116:119]\n"
"v_mfma_f32_16x16x32_bf16 v[120:123], v[144:147], a[88:91], v[120:123]\n"
"v_mfma_f32_16x16x32_bf16 v[124:127], v[144:147], a[92:95], v[124:127]\n"
"s_waitcnt lgkmcnt(2)\n"
"v_mfma_f32_16x16x32_bf16 v[96:99], v[128:131], a[96:99], v[96:99]\n"
"ds_read_b64_tr_b16 v[140:141], v163 offset:1024\n"
"ds_read_b64_tr_b16 v[142:143], v163 offset:9216\n"
"v_mfma_f32_16x16x32_bf16 v[100:103], v[128:131], a[100:103], v[100:103]\n"
"v_mfma_f32_16x16x32_bf16 v[104:107], v[128:131], a[104:107], v[104:107]\n"
"v_mfma_f32_16x16x32_bf16 v[108:111], v[128:131], a[108:111], v[108:111]\n"
"v_mfma_f32_16x16x32_bf16 v[112:115], v[132:135], a[96:99], v[112:115]\n"
"ds_read_b64_tr_b16 v[144:145], v162 offset:2048\n"
"ds_read_b64_tr_b16 v[146:147], v162 offset:10240\n"
"v_mfma_f32_16x16x32_bf16 v[116:119], v[132:135], a[100:103], v[116:119]\n"
"v_mfma_f32_16x16x32_bf16 v[120:123], v[132:135], a[104:107], v[120:123]\n"
"v_mfma_f32_16x16x32_bf16 v[124:127], v[132:135], a[108:111], v[124:127]\n"
"s_waitcnt lgkmcnt(2)\n"
"v_mfma_f32_16x16x32_bf16 v[96:99], v[136:139], a[112:115], v[96:99]\n"
"ds_read_b64_tr_b16 v[128:129], v163 offset:2048\n"
"ds_read_b64_tr_b16 v[130:131], v163 offset:10240\n"
"v_mfma_f32_16x16x32_bf16 v[100:103], v[136:139], a[116:119], v[100:103]\n"
"v_mfma_f32_16x16x32_bf16 v[104:107], v[136:139], a[120:123], v[104:107]\n"
"v_mfma_f32_16x16x32_bf16 v[108:111], v[136:139], a[124:127], v[108:111]\n"
"v_mfma_f32_16x16x32_bf16 v[112:115], v[140:143], a[112:115], v[112:115]\n"
"ds_read_b64_tr_b16 v[132:133], v162 offset:3072\n"
"ds_read_b64_tr_b16 v[134:135], v162 offset:11264\n"
"v_mfma_f32_16x16x32_bf16 v[116:119], v[140:143], a[116:119], v[116:119]\n"
"v_mfma_f32_16x16x32_bf16 v[120:123], v[140:143], a[120:123], v[120:123]\n"
"v_mfma_f32_16x16x32_bf16 v[124:127], v[140:143], a[124:127], v[124:127]\n"
"s_waitcnt lgkmcnt(2)\n"
"v_mfma_f32_16x16x32_bf16 v[96:99], v[144:147], a[208:211], v[96:99]\n"
"ds_read_b64_tr_b16 v[136:137], v163 offset:3072\n"
"ds_read_b64_tr_b16 v[138:139], v163 offset:11264\n"
"v_mfma_f32_16x16x32_bf16 v[100:103], v[144:147], a[212:215], v[100:103]\n"
"v_mfma_f32_16x16x32_bf16 v[104:107], v[144:147], a[216:219], v[104:107]\n"
"v_mfma_f32_16x16x32_bf16 v[108:111], v[144:147], a[220:223], v[108:111]\n"
"v_mfma_f32_16x16x32_bf16 v[112:115], v[128:131], a[208:211], v[112:115]\n"
"s_waitcnt vmcnt(31)\n"
"s_barrier\n"
"v_mfma_f32_16x16x32_bf16 v[116:119], v[128:131], a[212:215], v[116:119]\n"
"ds_read_b64_tr_b16 v[140:141], v162 offset:16384\n"
"ds_read_b64_tr_b16 v[142:143], v162 offset:24576\n"
"v_mfma_f32_16x16x32_bf16 v[120:123], v[128:131], a[216:219], v[120:123]\n"
"v_mfma_f32_16x16x32_bf16 v[124:127], v[128:131], a[220:223], v[124:127]\n"
"s_waitcnt lgkmcnt(2)\n"
"v_mfma_f32_16x16x32_bf16 v[96:99], v[132:135], a[224:227], v[96:99]\n"
"ds_read_b64_tr_b16 v[144:145], v163 offset:16384\n"
"ds_read_b64_tr_b16 v[146:147], v163 offset:24576\n"
"v_mfma_f32_16x16x32_bf16 v[100:103], v[132:135], a[228:231], v[100:103]\n"
"v_mfma_f32_16x16x32_bf16 v[104:107], v[132:135], a[232:235], v[104:107]\n"
"v_mfma_f32_16x16x32_bf16 v[108:111], v[132:135], a[236:239], v[108:111]\n"
"v_mfma_f32_16x16x32_bf16 v[112:115], v[136:139], a[224:227], v[112:115]\n"
"ds_read_b64_tr_b16 v[128:129], v162 offset:17408\n"
"ds_read_b64_tr_b16 v[130:131], v162 offset:25600\n"
"v_mfma_f32_16x16x32_bf16 v[116:119], v[136:139], a[228:231], v[116:119]\n"
"v_mfma_f32_16x16x32_bf16 v[120:123], v[136:139], a[232:235], v[120:123]\n"
"v_mfma_f32_16x16x32_bf16 v[124:127], v[136:139], a[236:239], v[124:127]\n"
"s_waitcnt lgkmcnt(2)\n"
"v_mfma_f32_16x16x32_bf16 v[96:99], v[140:143], a[240:243], v[96:99]\n"
"ds_read_b64_tr_b16 v[132:133], v163 offset:17408\n"
"ds_read_b64_tr_b16 v[134:135], v163 offset:25600\n"
"v_mfma_f32_16x16x32_bf16 v[100:103], v[140:143], a[244:247], v[100:103]\n"
"v_mfma_f32_16x16x32_bf16 v[104:107], v[140:143], a[248:251], v[104:107]\n"
"v_mfma_f32_16x16x32_bf16 v[108:111], v[140:143], a[252:255], v[108:111]\n"
"v_mfma_f32_16x16x32_bf16 v[112:115], v[144:147], a[240:243], v[112:115]\n"
"ds_read_b64_tr_b16 v[136:137], v162 offset:18432\n"
"ds_read_b64_tr_b16 v[138:139], v162 offset:26624\n"
"v_mfma_f32_16x16x32_bf16 v[116:119], v[144:147], a[244:247], v[116:119]\n"
"v_mfma_f32_16x16x32_bf16 v[120:123], v[144:147], a[248:251], v[120:123]\n"
"v_mfma_f32_16x16x32_bf16 v[124:127], v[144:147], a[252:255], v[124:127]\n"
"s_waitcnt lgkmcnt(2)\n"
"v_mfma_f32_16x16x32_bf16 v[96:99], v[128:131], a[128:131], v[96:99]\n"
"ds_read_b64_tr_b16 v[140:141], v163 offset:18432\n"
"ds_read_b64_tr_b16 v[142:143], v163 offset:26624\n"
"v_mfma_f32_16x16x32_bf16 v[100:103], v[128:131], a[132:135], v[100:103]\n"
"v_mfma_f32_16x16x32_bf16 v[104:107], v[128:131], a[136:139], v[104:107]\n"
"v_mfma_f32_16x16x32_bf16 v[108:111], v[128:131], a[140:143], v[108:111]\n"
"v_mfma_f32_16x16x32_bf16 v[112:115], v[132:135], a[128:131], v[112:115]\n"
"ds_read_b64_tr_b16 v[144:145], v162 offset:19456\n"
"ds_read_b64_tr_b16 v[146:147], v162 offset:27648\n"
"v_mfma_f32_16x16x32_bf16 v[116:119], v[132:135], a[132:135], v[116:119]\n"
"v_mfma_f32_16x16x32_bf16 v[120:123], v[132:135], a[136:139], v[120:123]\n"
"v_mfma_f32_16x16x32_bf16 v[124:127], v[132:135], a[140:143], v[124:127]\n"
"s_waitcnt lgkmcnt(2)\n"
"v_mfma_f32_16x16x32_bf16 v[96:99], v[136:139], a[144:147], v[96:99]\n"
"ds_read_b64_tr_b16 v[128:129], v163 offset:19456\n"
"ds_read_b64_tr_b16 v[130:131], v163 offset:27648\n"
"v_mfma_f32_16x16x32_bf16 v[100:103], v[136:139], a[148:151], v[100:103]\n"
"v_mfma_f32_16x16x32_bf16 v[104:107], v[136:139], a[152:155], v[104:107]\n"
"v_mfma_f32_16x16x32_bf16 v[108:111], v[136:139], a[156:159], v[108:111]\n"
"v_mfma_f32_16x16x32_bf16 v[112:115], v[140:143], a[144:147], v[112:115]\n"
"s_waitcnt vmcnt(27)\n"
"s_barrier\n"
"v_mfma_f32_16x16x32_bf16 v[116:119], v[140:143], a[148:151], v[116:119]\n"
"ds_read_b64_tr_b16 v[132:133], v164\n"
"ds_read_b64_tr_b16 v[134:135], v164 offset:8192\n"
"v_mfma_f32_16x16x32_bf16 v[120:123], v[140:143], a[152:155], v[120:123]\n"
"v_mfma_f32_16x16x32_bf16 v[124:127], v[140:143], a[156:159], v[124:127]\n"
"s_waitcnt lgkmcnt(2)\n"
"v_mfma_f32_16x16x32_bf16 v[96:99], v[144:147], a[160:163], v[96:99]\n"
"ds_read_b64_tr_b16 v[136:137], v165\n"
"ds_read_b64_tr_b16 v[138:139], v165 offset:8192\n"
"v_mfma_f32_16x16x32_bf16 v[100:103], v[144:147], a[164:167], v[100:103]\n"
"v_mfma_f32_16x16x32_bf16 v[104:107], v[144:147], a[168:171], v[104:107]\n"
"v_mfma_f32_16x16x32_bf16 v[108:111], v[144:147], a[172:175], v[108:111]\n"
"v_mfma_f32_16x16x32_bf16 v[112:115], v[128:131], a[160:163], v[112:115]\n"
"ds_read_b64_tr_b16 v[140:141], v164 offset:1024\n"
"ds_read_b64_tr_b16 v[142:143], v164 offset:9216\n"
"v_mfma_f32_16x16x32_bf16 v[116:119], v[128:131], a[164:167], v[116:119]\n"
"v_mfma_f32_16x16x32_bf16 v[120:123], v[128:131], a[168:171], v[120:123]\n"
"v_mfma_f32_16x16x32_bf16 v[124:127], v[128:131], a[172:175], v[124:127]\n"
"s_waitcnt lgkmcnt(2)\n"
"v_mfma_f32_16x16x32_bf16 v[96:99], v[132:135], a[176:179], v[96:99]\n"
"ds_read_b64_tr_b16 v[144:145], v165 offset:1024\n"
"ds_read_b64_tr_b16 v[146:147], v165 offset:9216\n"
"v_mfma_f32_16x16x32_bf16 v[100:103], v[132:135], a[180:183], v[100:103]\n"
"v_mfma_f32_16x16x32_bf16 v[104:107], v[132:135], a[184:187], v[104:107]\n"
"v_mfma_f32_16x16x32_bf16 v[108:111], v[132:135], a[188:191], v[108:111]\n"
"v_mfma_f32_16x16x32_bf16 v[112:115], v[136:139], a[176:179], v[112:115]\n"
"ds_read_b64_tr_b16 v[128:129], v164 offset:2048\n"
"ds_read_b64_tr_b16 v[130:131], v164 offset:10240\n"
"v_mfma_f32_16x16x32_bf16 v[116:119], v[136:139], a[180:183], v[116:119]\n"
"v_mfma_f32_16x16x32_bf16 v[120:123], v[136:139], a[184:187], v[120:123]\n"
"v_mfma_f32_16x16x32_bf16 v[124:127], v[136:139], a[188:191], v[124:127]\n"
"s_waitcnt lgkmcnt(2)\n"
"v_mfma_f32_16x16x32_bf16 v[96:99], v[140:143], a[192:195], v[96:99]\n"
"ds_read_b64_tr_b16 v[132:133], v165 offset:2048\n"
"ds_read_b64_tr_b16 v[134:135], v165 offset:10240\n"
"v_mfma_f32_16x16x32_bf16 v[100:103], v[140:143], a[196:199], v[100:103]\n"
"v_mfma_f32_16x16x32_bf16 v[104:107], v[140:143], a[200:203], v[104:107]\n"
"v_mfma_f32_16x16x32_bf16 v[108:111], v[140:143], a[204:207], v[108:111]\n"
"v_mfma_f32_16x16x32_bf16 v[112:115], v[144:147], a[192:195], v[112:115]\n"
"ds_read_b64_tr_b16 v[136:137], v164 offset:3072\n"
"ds_read_b64_tr_b16 v[138:139], v164 offset:11264\n"
"v_mfma_f32_16x16x32_bf16 v[116:119], v[144:147], a[196:199], v[116:119]\n"
"v_mfma_f32_16x16x32_bf16 v[120:123], v[144:147], a[200:203], v[120:123]\n"
"v_mfma_f32_16x16x32_bf16 v[124:127], v[144:147], a[204:207], v[124:127]\n"
"s_waitcnt lgkmcnt(2)\n"
"v_mfma_f32_16x16x32_bf16 v[96:99], v[128:131], v[224:227], v[96:99]\n"
"ds_read_b64_tr_b16 v[140:141], v165 offset:3072\n"
"ds_read_b64_tr_b16 v[142:143], v165 offset:11264\n"
"v_mfma_f32_16x16x32_bf16 v[100:103], v[128:131], v[228:231], v[100:103]\n"
"v_mfma_f32_16x16x32_bf16 v[104:107], v[128:131], v[232:235], v[104:107]\n"
"v_mfma_f32_16x16x32_bf16 v[108:111], v[128:131], v[236:239], v[108:111]\n"
"v_mfma_f32_16x16x32_bf16 v[112:115], v[132:135], v[224:227], v[112:115]\n"
"s_waitcnt vmcnt(23)\n"
"s_barrier\n"
"v_mfma_f32_16x16x32_bf16 v[116:119], v[132:135], v[228:231], v[116:119]\n"
"ds_read_b64_tr_b16 v[144:145], v162 offset:49152\n"
"ds_read_b64_tr_b16 v[146:147], v162 offset:57344\n"
"v_mfma_f32_16x16x32_bf16 v[120:123], v[132:135], v[232:235], v[120:123]\n"
"v_mfma_f32_16x16x32_bf16 v[124:127], v[132:135], v[236:239], v[124:127]\n"
"s_waitcnt lgkmcnt(2)\n"
"v_mfma_f32_16x16x32_bf16 v[96:99], v[136:139], v[240:243], v[96:99]\n"
"ds_read_b64_tr_b16 v[128:129], v163 offset:49152\n"
"ds_read_b64_tr_b16 v[130:131], v163 offset:57344\n"
"v_mfma_f32_16x16x32_bf16 v[100:103], v[136:139], v[244:247], v[100:103]\n"
"v_mfma_f32_16x16x32_bf16 v[104:107], v[136:139], v[248:251], v[104:107]\n"
"v_mfma_f32_16x16x32_bf16 v[108:111], v[136:139], v[252:255], v[108:111]\n"
"v_mfma_f32_16x16x32_bf16 v[112:115], v[140:143], v[240:243], v[112:115]\n"
"ds_read_b64_tr_b16 v[132:133], v162 offset:50176\n"
"ds_read_b64_tr_b16 v[134:135], v162 offset:58368\n"
"v_mfma_f32_16x16x32_bf16 v[116:119], v[140:143], v[244:247], v[116:119]\n"
"v_mfma_f32_16x16x32_bf16 v[120:123], v[140:143], v[248:251], v[120:123]\n"
"v_mfma_f32_16x16x32_bf16 v[124:127], v[140:143], v[252:255], v[124:127]\n"
"s_waitcnt lgkmcnt(2)\n"
"v_mfma_f32_16x16x32_bf16 v[96:99], v[144:147], v[32:35], v[96:99]\n"
"ds_read_b64_tr_b16 v[136:137], v163 offset:50176\n"
"ds_read_b64_tr_b16 v[138:139], v163 offset:58368\n"
"v_mfma_f32_16x16x32_bf16 v[100:103], v[144:147], v[36:39], v[100:103]\n"
"v_mfma_f32_16x16x32_bf16 v[104:107], v[144:147], v[40:43], v[104:107]\n"
"v_mfma_f32_16x16x32_bf16 v[108:111], v[144:147], v[44:47], v[108:111]\n"
"v_mfma_f32_16x16x32_bf16 v[112:115], v[128:131], v[32:35], v[112:115]\n"
"ds_read_b64_tr_b16 v[140:141], v162 offset:51200\n"
"ds_read_b64_tr_b16 v[142:143], v162 offset:59392\n"
"v_mfma_f32_16x16x32_bf16 v[116:119], v[128:131], v[36:39], v[116:119]\n"
"v_mfma_f32_16x16x32_bf16 v[120:123], v[128:131], v[40:43], v[120:123]\n"
"v_mfma_f32_16x16x32_bf16 v[124:127], v[128:131], v[44:47], v[124:127]\n"
"s_waitcnt lgkmcnt(2)\n"
"v_mfma_f32_16x16x32_bf16 v[96:99], v[132:135], v[48:51], v[96:99]\n"
"ds_read_b64_tr_b16 v[144:145], v163 offset:51200\n"
"ds_read_b64_tr_b16 v[146:147], v163 offset:59392\n"
"v_mfma_f32_16x16x32_bf16 v[100:103], v[132:135], v[52:55], v[100:103]\n"
"v_mfma_f32_16x16x32_bf16 v[104:107], v[132:135], v[56:59], v[104:107]\n"
"v_mfma_f32_16x16x32_bf16 v[108:111], v[132:135], v[60:63], v[108:111]\n"
"v_mfma_f32_16x16x32_bf16 v[112:115], v[136:139], v[48:51], v[112:115]\n"
"ds_read_b64_tr_b16 v[128:129], v162 offset:52224\n"
"ds_read_b64_tr_b16 v[130:131], v162 offset:60416\n"
"v_mfma_f32_16x16x32_bf16 v[116:119], v[136:139], v[52:55], v[116:119]\n"
"v_mfma_f32_16x16x32_bf16 v[120:123], v[136:139], v[56:59], v[120:123]\n"
"v_mfma_f32_16x16x32_bf16 v[124:127], v[136:139], v[60:63], v[124:127]\n"
"s_waitcnt lgkmcnt(2)\n"
"v_mfma_f32_16x16x32_bf16 v[96:99], v[140:143], v[64:67], v[96:99]\n"
"ds_read_b64_tr_b16 v[132:133], v163 offset:52224\n"
"ds_read_b64_tr_b16 v[134:135], v163 offset:60416\n"
"v_mfma_f32_16x16x32_bf16 v[100:103], v[140:143], v[68:71], v[100:103]\n"
"v_mfma_f32_16x16x32_bf16 v[104:107], v[140:143], v[72:75], v[104:107]\n"
"v_mfma_f32_16x16x32_bf16 v[108:111], v[140:143], v[76:79], v[108:111]\n"
"v_mfma_f32_16x16x32_bf16 v[112:115], v[144:147], v[64:67], v[112:115]\n"
"s_waitcnt vmcnt(15)\n"
"s_barrier\n"
"v_mfma_f32_16x16x32_bf16 v[116:119], v[144:147], v[68:71], v[116:119]\n"
"ds_read_b64_tr_b16 v[136:137], v160 offset:16384\n"
"ds_read_b64_tr_b16 v[138:139], v160 offset:24576\n"
"v_mfma_f32_16x16x32_bf16 v[120:123], v[144:147], v[72:75], v[120:123]\n"
"v_mfma_f32_16x16x32_bf16 v[124:127], v[144:147], v[76:79], v[124:127]\n"
"s_waitcnt lgkmcnt(2)\n"
"v_mfma_f32_16x16x32_bf16 v[96:99], v[128:131], v[80:83], v[96:99]\n"
"ds_read_b64_tr_b16 v[140:141], v161 offset:16384\n"
"ds_read_b64_tr_b16 v[142:143], v161 offset:24576\n"
"v_mfma_f32_16x16x32_bf16 v[100:103], v[128:131], v[84:87], v[100:103]\n"
"v_mfma_f32_16x16x32_bf16 v[104:107], v[128:131], v[88:91], v[104:107]\n"
"v_mfma_f32_16x16x32_bf16 v[108:111], v[128:131], v[92:95], v[108:111]\n"
"v_mfma_f32_16x16x32_bf16 v[112:115], v[132:135], v[80:83], v[112:115]\n"
"ds_read_b64_tr_b16 v[144:145], v160 offset:17408\n"
"ds_read_b64_tr_b16 v[146:147], v160 offset:25600\n"
"v_mfma_f32_16x16x32_bf16 v[116:119], v[132:135], v[84:87], v[116:119]\n"
"v_mfma_f32_16x16x32_bf16 v[120:123], v[132:135], v[88:91], v[120:123]\n"
"v_mfma_f32_16x16x32_bf16 v[124:127], v[132:135], v[92:95], v[124:127]\n"
"s_waitcnt lgkmcnt(2)\n"
"v_mfma_f32_16x16x32_bf16 v[192:195], v[136:139], a[0:3], 0\n"
"ds_read_b64_tr_b16 v[128:129], v161 offset:17408\n"
"ds_read_b64_tr_b16 v[130:131], v161 offset:25600\n"
"v_mfma_f32_16x16x32_bf16 v[196:199], v[136:139], a[4:7], 0\n"
"v_mfma_f32_16x16x32_bf16 v[200:203], v[136:139], a[8:11], 0\n"
"v_mfma_f32_16x16x32_bf16 v[204:207], v[136:139], a[12:15], 0\n"
"v_mfma_f32_16x16x32_bf16 v[192:195], v[140:143], a[16:19], v[192:195]\n"
"ds_read_b64_tr_b16 v[132:133], v160 offset:18432\n"
"ds_read_b64_tr_b16 v[134:135], v160 offset:26624\n"
"v_mfma_f32_16x16x32_bf16 v[196:199], v[140:143], a[20:23], v[196:199]\n"
"s_lshl_b32 s16, s62, 13\n"
"s_lshl_b32 s17, s61, 12\n"
"s_add_u32 s16, s16, s17\n"
"s_add_u32 s16, s16, s95\n"
"s_add_u32 s17, s16, 49152\n"
"v_mfma_f32_16x16x32_bf16 v[200:203], v[140:143], a[24:27], v[200:203]\n"
"v_cvt_pk_bf16_f32 v152, v96, v97\n"
"v_cvt_pk_bf16_f32 v153, v98, v99\n"
"v_mfma_f32_16x16x32_bf16 v[204:207], v[140:143], a[28:31], v[204:207]\n"
"v_cvt_pk_bf16_f32 v154, v112, v113\n"
"s_waitcnt lgkmcnt(2)\n"
"v_mfma_f32_16x16x32_bf16 v[192:195], v[144:147], a[32:35], v[192:195]\n"
"ds_read_b64_tr_b16 v[136:137], v161 offset:18432\n"
"ds_read_b64_tr_b16 v[138:139], v161 offset:26624\n"
"v_mfma_f32_16x16x32_bf16 v[196:199], v[144:147], a[36:39], v[196:199]\n"
"v_cvt_pk_bf16_f32 v155, v114, v115\n"
"buffer_store_dwordx4 v[152:155], v158, s[48:51], s17 offen sc1\n"
"v_mfma_f32_16x16x32_bf16 v[200:203], v[144:147], a[40:43], v[200:203]\n"
"s_nop 0\n"
"v_cvt_pk_bf16_f32 v152, v100, v101\n"
"v_cvt_pk_bf16_f32 v153, v102, v103\n"
"v_mfma_f32_16x16x32_bf16 v[204:207], v[144:147], a[44:47], v[204:207]\n"
"v_cvt_pk_bf16_f32 v154, v116, v117\n"
"v_cvt_pk_bf16_f32 v155, v118, v119\n"
"v_mfma_f32_16x16x32_bf16 v[192:195], v[128:131], a[48:51], v[192:195]\n"
"ds_read_b64_tr_b16 v[140:141], v160 offset:19456\n"
"ds_read_b64_tr_b16 v[142:143], v160 offset:27648\n"
"v_mfma_f32_16x16x32_bf16 v[196:199], v[128:131], a[52:55], v[196:199]\n"
"buffer_store_dwordx4 v[152:155], v158, s[48:51], s17 offen offset:1024 sc1\n"
"s_nop 1\n"
"v_cvt_pk_bf16_f32 v152, v104, v105\n"
"v_mfma_f32_16x16x32_bf16 v[200:203], v[128:131], a[56:59], v[200:203]\n"
"v_cvt_pk_bf16_f32 v153, v106, v107\n"
"v_cvt_pk_bf16_f32 v154, v120, v121\n"
"v_mfma_f32_16x16x32_bf16 v[204:207], v[128:131], a[60:63], v[204:207]\n"
"v_cvt_pk_bf16_f32 v155, v122, v123\n"
"s_waitcnt lgkmcnt(2)\n"
"v_mfma_f32_16x16x32_bf16 v[192:195], v[132:135], a[64:67], v[192:195]\n"
"ds_read_b64_tr_b16 v[144:145], v161 offset:19456\n"
"ds_read_b64_tr_b16 v[146:147], v161 offset:27648\n"
"v_mfma_f32_16x16x32_bf16 v[196:199], v[132:135], a[68:71], v[196:199]\n"
"buffer_store_dwordx4 v[152:155], v158, s[48:51], s17 offen offset:2048 sc1\n"
"s_nop 1\n"
"v_cvt_pk_bf16_f32 v152, v108, v109\n"
"v_mfma_f32_16x16x32_bf16 v[200:203], v[132:135], a[72:75], v[200:203]\n"
"v_cvt_pk_bf16_f32 v153, v110, v111\n"
"v_cvt_pk_bf16_f32 v154, v124, v125\n"
"v_mfma_f32_16x16x32_bf16 v[204:207], v[132:135], a[76:79], v[204:207]\n"
"v_cvt_pk_bf16_f32 v155, v126, v127\n"
"buffer_store_dwordx4 v[152:155], v158, s[48:51], s17 offen offset:3072 sc1\n"
"v_mfma_f32_16x16x32_bf16 v[192:195], v[136:139], a[80:83], v[192:195]\n"
"s_waitcnt vmcnt(11)\n"
"s_barrier\n"
"v_mfma_f32_16x16x32_bf16 v[196:199], v[136:139], a[84:87], v[196:199]\n"
"ds_read_b64_tr_b16 v[128:129], v160 offset:32768\n"
"ds_read_b64_tr_b16 v[130:131], v160 offset:40960\n"
"v_mfma_f32_16x16x32_bf16 v[200:203], v[136:139], a[88:91], v[200:203]\n"
"v_mfma_f32_16x16x32_bf16 v[204:207], v[136:139], a[92:95], v[204:207]\n"
"s_waitcnt lgkmcnt(2)\n"
"v_mfma_f32_16x16x32_bf16 v[192:195], v[140:143], a[96:99], v[192:195]\n"
"ds_read_b64_tr_b16 v[132:133], v161 offset:32768\n"
"ds_read_b64_tr_b16 v[134:135], v161 offset:40960\n"
"v_mfma_f32_16x16x32_bf16 v[196:199], v[140:143], a[100:103], v[196:199]\n"
"v_mfma_f32_16x16x32_bf16 v[200:203], v[140:143], a[104:107], v[200:203]\n"
"v_mfma_f32_16x16x32_bf16 v[204:207], v[140:143], a[108:111], v[204:207]\n"
"v_mfma_f32_16x16x32_bf16 v[192:195], v[144:147], a[112:115], v[192:195]\n"
"ds_read_b64_tr_b16 v[136:137], v160 offset:33792\n"
"ds_read_b64_tr_b16 v[138:139], v160 offset:41984\n"
"v_mfma_f32_16x16x32_bf16 v[196:199], v[144:147], a[116:119], v[196:199]\n"
"v_mfma_f32_16x16x32_bf16 v[200:203], v[144:147], a[120:123], v[200:203]\n"
"v_mfma_f32_16x16x32_bf16 v[204:207], v[144:147], a[124:127], v[204:207]\n"
"s_waitcnt lgkmcnt(2)\n"
"v_mfma_f32_16x16x32_bf16 v[192:195], v[128:131], a[208:211], v[192:195]\n"
"ds_read_b64_tr_b16 v[140:141], v161 offset:33792\n"
"ds_read_b64_tr_b16 v[142:143], v161 offset:41984\n"
"v_mfma_f32_16x16x32_bf16 v[196:199], v[128:131], a[212:215], v[196:199]\n"
"v_mfma_f32_16x16x32_bf16 v[200:203], v[128:131], a[216:219], v[200:203]\n"
"v_mfma_f32_16x16x32_bf16 v[204:207], v[128:131], a[220:223], v[204:207]\n"
"v_mfma_f32_16x16x32_bf16 v[192:195], v[132:135], a[224:227], v[192:195]\n"
"ds_read_b64_tr_b16 v[144:145], v160 offset:34816\n"
"ds_read_b64_tr_b16 v[146:147], v160 offset:43008\n"
"v_mfma_f32_16x16x32_bf16 v[196:199], v[132:135], a[228:231], v[196:199]\n"
"v_mfma_f32_16x16x32_bf16 v[200:203], v[132:135], a[232:235], v[200:203]\n"
"v_mfma_f32_16x16x32_bf16 v[204:207], v[132:135], a[236:239], v[204:207]\n"
"s_waitcnt lgkmcnt(2)\n"
"v_mfma_f32_16x16x32_bf16 v[192:195], v[136:139], a[240:243], v[192:195]\n"
"ds_read_b64_tr_b16 v[128:129], v161 offset:34816\n"
"ds_read_b64_tr_b16 v[130:131], v161 offset:43008\n"
"v_mfma_f32_16x16x32_bf16 v[196:199], v[136:139], a[244:247], v[196:199]\n"
"v_mfma_f32_16x16x32_bf16 v[200:203], v[136:139], a[248:251], v[200:203]\n"
"v_mfma_f32_16x16x32_bf16 v[204:207], v[136:139], a[252:255], v[204:207]\n"
"v_mfma_f32_16x16x32_bf16 v[192:195], v[140:143], a[128:131], v[192:195]\n"
"ds_read_b64_tr_b16 v[132:133], v160 offset:35840\n"
"ds_read_b64_tr_b16 v[134:135], v160 offset:44032\n"
"v_mfma_f32_16x16x32_bf16 v[196:199], v[140:143], a[132:135], v[196:199]\n"
"v_mfma_f32_16x16x32_bf16 v[200:203], v[140:143], a[136:139], v[200:203]\n"
"v_mfma_f32_16x16x32_bf16 v[204:207], v[140:143], a[140:143], v[204:207]\n"
"s_waitcnt lgkmcnt(2)\n"
"v_mfma_f32_16x16x32_bf16 v[192:195], v[144:147], a[144:147], v[192:195]\n"
"ds_read_b64_tr_b16 v[136:137], v161 offset:35840\n"
"ds_read_b64_tr_b16 v[138:139], v161 offset:44032\n"
"v_mfma_f32_16x16x32_bf16 v[196:199], v[144:147], a[148:151], v[196:199]\n"
"v_mfma_f32_16x16x32_bf16 v[200:203], v[144:147], a[152:155], v[200:203]\n"
"v_mfma_f32_16x16x32_bf16 v[204:207], v[144:147], a[156:159], v[204:207]\n"
"v_mfma_f32_16x16x32_bf16 v[192:195], v[128:131], a[160:163], v[192:195]\n"
"s_waitcnt vmcnt(4)\n"
"s_barrier\n"
"v_mfma_f32_16x16x32_bf16 v[196:199], v[128:131], a[164:167], v[196:199]\n"
"ds_read_b64_tr_b16 v[140:141], v162 offset:32768\n"
"ds_read_b64_tr_b16 v[142:143], v162 offset:40960\n"
"v_mfma_f32_16x16x32_bf16 v[200:203], v[128:131], a[168:171], v[200:203]\n"
"v_mfma_f32_16x16x32_bf16 v[204:207], v[128:131], a[172:175], v[204:207]\n"
"s_waitcnt lgkmcnt(2)\n"
"v_mfma_f32_16x16x32_bf16 v[192:195], v[132:135], a[176:179], v[192:195]\n"
"ds_read_b64_tr_b16 v[144:145], v163 offset:32768\n"
"ds_read_b64_tr_b16 v[146:147], v163 offset:40960\n"
"v_mfma_f32_16x16x32_bf16 v[196:199], v[132:135], a[180:183], v[196:199]\n"
"v_mfma_f32_16x16x32_bf16 v[200:203], v[132:135], a[184:187], v[200:203]\n"
"v_mfma_f32_16x16x32_bf16 v[204:207], v[132:135], a[188:191], v[204:207]\n"
"v_mfma_f32_16x16x32_bf16 v[192:195], v[136:139], a[192:195], v[192:195]\n"
"ds_read_b64_tr_b16 v[128:129], v162 offset:33792\n"
"ds_read_b64_tr_b16 v[130:131], v162 offset:41984\n"
"v_mfma_f32_16x16x32_bf16 v[196:199], v[136:139], a[196:199], v[196:199]\n"
"v_mfma_f32_16x16x32_bf16 v[200:203], v[136:139], a[200:203], v[200:203]\n"
"v_mfma_f32_16x16x32_bf16 v[204:207], v[136:139], a[204:207], v[204:207]\n"
"s_waitcnt lgkmcnt(2)\n"
"v_mfma_f32_16x16x32_bf16 v[192:195], v[140:143], v[224:227], v[192:195]\n"
"ds_read_b64_tr_b16 v[132:133], v163 offset:33792\n"
"ds_read_b64_tr_b16 v[134:135], v163 offset:41984\n"
"v_mfma_f32_16x16x32_bf16 v[196:199], v[140:143], v[228:231], v[196:199]\n"
"v_mfma_f32_16x16x32_bf16 v[200:203], v[140:143], v[232:235], v[200:203]\n"
"v_mfma_f32_16x16x32_bf16 v[204:207], v[140:143], v[236:239], v[204:207]\n"
"v_mfma_f32_16x16x32_bf16 v[192:195], v[144:147], v[240:243], v[192:195]\n"
"ds_read_b64_tr_b16 v[136:137], v162 offset:34816\n"
"ds_read_b64_tr_b16 v[138:139], v162 offset:43008\n"
"v_mfma_f32_16x16x32_bf16 v[196:199], v[144:147], v[244:247], v[196:199]\n"
"v_mfma_f32_16x16x32_bf16 v[200:203], v[144:147], v[248:251], v[200:203]\n"
"v_mfma_f32_16x16x32_bf16 v[204:207], v[144:147], v[252:255], v[204:207]\n"
"s_waitcnt lgkmcnt(2)\n"
"v_mfma_f32_16x16x32_bf16 v[192:195], v[128:131], v[32:35], v[192:195]\n"
"ds_read_b64_tr_b16 v[140:141], v163 offset:34816\n"
"ds_read_b64_tr_b16 v[142:143], v163 offset:43008\n"
"v_mfma_f32_16x16x32_bf16 v[196:199], v[128:131], v[36:39], v[196:199]\n"
"v_mfma_f32_16x16x32_bf16 v[200:203], v[128:131], v[40:43], v[200:203]\n"
"v_mfma_f32_16x16x32_bf16 v[204:207], v[128:131], v[44:47], v[204:207]\n"
"v_mfma_f32_16x16x32_bf16 v[192:195], v[132:135], v[48:51], v[192:195]\n"
"v_mfma_f32_16x16x32_bf16 v[196:199], v[132:135], v[52:55], v[196:199]\n"
"v_mfma_f32_16x16x32_bf16 v[200:203], v[132:135], v[56:59], v[200:203]\n"
"v_mfma_f32_16x16x32_bf16 v[204:207], v[132:135], v[60:63], v[204:207]\n"
"s_waitcnt lgkmcnt(0)\n"
"v_mfma_f32_16x16x32_bf16 v[192:195], v[136:139], v[64:67], v[192:195]\n"
"v_mfma_f32_16x16x32_bf16 v[196:199], v[136:139], v[68:71], v[196:199]\n"
"v_mfma_f32_16x16x32_bf16 v[200:203], v[136:139], v[72:75], v[200:203]\n"
"v_mfma_f32_16x16x32_bf16 v[204:207], v[136:139], v[76:79], v[204:207]\n"
"v_mfma_f32_16x16x32_bf16 v[192:195], v[140:143], v[80:83], v[192:195]\n"
"v_mfma_f32_16x16x32_bf16 v[196:199], v[140:143], v[84:87], v[196:199]\n"
"v_mfma_f32_16x16x32_bf16 v[200:203], v[140:143], v[88:91], v[200:203]\n"
"v_mfma_f32_16x16x32_bf16 v[204:207], v[140:143], v[92:95], v[204:207]\n"
"s_lshl_b32 s16, s62, 12\n"
"s_lshl_b32 s17, s61, 11\n"
"s_add_u32 s16, s16, s17\n"
"s_add_u32 s16, s16, s95\n"
"s_add_u32 s17, s16, 65536\n"
"v_lshrrev_b32 v156, 1, v158\n"
"v_cvt_pk_bf16_f32 v152, v192, v193\n"
"v_cvt_pk_bf16_f32 v153, v194, v195\n"
"buffer_store_dwordx2 v[152:153], v156, s[48:51], s17 offen sc1\n"
"v_cvt_pk_bf16_f32 v152, v196, v197\n"
"v_cvt_pk_bf16_f32 v153, v198, v199\n"
"buffer_store_dwordx2 v[152:153], v156, s[48:51], s17 offen offset:512 sc1\n"
"v_cvt_pk_bf16_f32 v152, v200, v201\n"
"v_cvt_pk_bf16_f32 v153, v202, v203\n"
"buffer_store_dwordx2 v[152:153], v156, s[48:51], s17 offen offset:1024 sc1\n"
"v_cvt_pk_bf16_f32 v152, v204, v205\n"
"v_cvt_pk_bf16_f32 v153, v206, v207\n"
"buffer_store_dwordx2 v[152:153], v156, s[48:51], s17 offen offset:1536 sc1\n"
"s_waitcnt vmcnt(0) lgkmcnt(0)\n"
"s_barrier\n"
"s_and_b32 s68, s63, 1\n"
"s_lshl_b32 s10, s64, 1\n"
"s_add_u32 s68, s68, s10\n"
"s_lshl_b32 s68, s68, 8\n"
"s_add_u32 s68, s68, 8192\n"
"s_cmp_eq_u32 s60, 0\n"
"s_cbranch_scc0 Lsig8_%=\n"
"v_mov_b32 v180, 1\n"
"s_mov_b64 exec, 1\n"
"buffer_atomic_add v180, off, s[48:51], s68\n"
"s_mov_b64 exec, -1\n"
"Lsig8_%=:\n"
"v_lshrrev_b32 v200, 4, v158\n"
"v_and_b32 v201, 15, v200\n"
"v_lshrrev_b32 v202, 4, v200\n"
"v_lshrrev_b32 v206, 1, v158\n"
"s_lshl_b32 s66, s64, 7\n"
"s_lshr_b32 s67, s63, 1\n"
"s_lshl_b32 s67, s67, 4\n"
"s_add_u32 s66, s66, s67\n"
"v_add_u32 v203, s66, v201\n"
"s_movk_i32 s66, 1152\n"
"v_mul_lo_u32 v203, s66, v203\n"
"s_and_b32 s66, s63, 1\n"
"s_lshl_b32 s66, s66, 6\n"
"s_lshl_b32 s67, s62, 5\n"
"s_add_u32 s66, s66, s67\n"
"s_add_u32 s66, s66, 512\n"
"v_lshlrev_b32 v205, 3, v202\n"
"v_add_u32 v204, v203, v205\n"
"v_add_u32 v204, s66, v204\n"
"v_and_b32 v205, 1, v202\n"
"v_lshlrev_b32 v205, 5, v205\n"
"v_add_u32 v203, v203, v205\n"
"v_lshrrev_b32 v205, 1, v202\n"
"v_lshlrev_b32 v205, 4, v205\n"
"v_add_u32 v203, v203, v205\n"
"s_lshl_b32 s66, s60, 7\n"
"s_add_u32 s8, s8, s66\n"
"s_and_b32 s66, s63, 1\n"
"s_lshl_b32 s67, s64, 1\n"
"s_add_u32 s66, s66, s67\n"
"s_mul_i32 s66, s66, 589824\n"
"s_add_u32 s66, s66, 32505856\n"
"s_lshr_b32 s67, s63, 1\n"
"s_lshl_b32 s68, s67, 9\n"
"s_add_u32 s95, s66, s68\n"
"s_lshl_b32 s67, s67, 10\n"
"s_add_u32 s66, s66, s67\n"
"s_lshl_b32 s67, s60, 14\n"
"s_add_u32 s94, s66, s67\n"
"s_lshl_b32 s67, s62, 12\n"
"s_add_u32 s95, s95, s67\n"
"s_add_u32 s95, s95, 65536\n"
"s_mov_b32 s96, 16256\n"
"s_mov_b32 s97, 1065353216\n"
"s_mov_b32 s98, 0x4091afa4\n"
"s_add_u32 s68, s8, 0\n"
"buffer_load_dwordx4 v[208:211], v203, s[52:55], s68 offen\n"
"s_add_u32 s68, s8, 64\n"
"buffer_load_dwordx4 v[212:215], v203, s[52:55], s68 offen\n"
"buffer_load_dwordx2 v[216:217], v204, s[52:55], 0 offen\n"
"s_and_b32 s68, s63, 1\n"
"s_lshl_b32 s10, s64, 1\n"
"s_add_u32 s68, s68, s10\n"
"s_lshl_b32 s68, s68, 8\n"
"s_add_u32 s19, s68, 8192\n"
"buffer_load_dword v178, off, s[48:51], s19 sc1\n"
"s_waitcnt vmcnt(0)\n"
"v_readfirstlane_b32 s18, v178\n"
"s_nop 1\n"
"s_cmp_ge_u32 s18, 8\n"
"s_cbranch_scc0 Lslow9_%=\n"
"Lback9_%=:\n"
"s_add_u32 s67, s94, 0\n"
"buffer_load_dwordx4 v[0:3], v158, s[48:51], s67 offen sc1\n"
"s_add_u32 s67, s67, 73728\n"
"buffer_load_dwordx4 v[4:7], v158, s[48:51], s67 offen sc1\n"
"s_add_u32 s67, s67, 73728\n"
"buffer_load_dwordx4 v[8:11], v158, s[48:51], s67 offen sc1\n"
"s_add_u32 s67, s67, 73728\n"
"buffer_load_dwordx4 v[12:15], v158, s[48:51], s67 offen sc1\n"
"s_add_u32 s67, s67, 73728\n"
"buffer_load_dwordx4 v[16:19], v158, s[48:51], s67 offen sc1\n"
"s_add_u32 s67, s67, 73728\n"
"buffer_load_dwordx4 v[20:23], v158, s[48:51], s67 offen sc1\n"
"s_add_u32 s67, s67, 73728\n"
"buffer_load_dwordx4 v[24:27], v158, s[48:51], s67 offen sc1\n"
"s_add_u32 s67, s67, 73728\n"
"buffer_load_dwordx4 v[28:31], v158, s[48:51], s67 offen sc1\n"
"s_add_u32 s67, s94, 8192\n"
"buffer_load_dwordx4 v[32:35], v158, s[48:51], s67 offen sc1\n"
"s_add_u32 s67, s67, 73728\n"
"buffer_load_dwordx4 v[36:39], v158, s[48:51], s67 offen sc1\n"
"s_add_u32 s67, s67, 73728\n"
"buffer_load_dwordx4 v[40:43], v158, s[48:51], s67 offen sc1\n"
"s_add_u32 s67, s67, 73728\n"
"buffer_load_dwordx4 v[44:47], v158, s[48:51], s67 offen sc1\n"
"s_add_u32 s67, s67, 73728\n"
"buffer_load_dwordx4 v[48:51], v158, s[48:51], s67 offen sc1\n"
"s_add_u32 s67, s67, 73728\n"
"buffer_load_dwordx4 v[52:55], v158, s[48:51], s67 offen sc1\n"
"s_add_u32 s67, s67, 73728\n"
"buffer_load_dwordx4 v[56:59], v158, s[48:51], s67 offen sc1\n"
"s_add_u32 s67, s67, 73728\n"
"buffer_load_dwordx4 v[60:63], v158, s[48:51], s67 offen sc1\n"
"s_mov_b32 s67, s95\n"
"buffer_load_dwordx2 v[64:65], v206, s[48:51], s67 offen sc1\n"
"s_add_u32 s67, s67, 73728\n"
"buffer_load_dwordx2 v[66:67], v206, s[48:51], s67 offen sc1\n"
"s_add_u32 s67, s67, 73728\n"
"buffer_load_dwordx2 v[68:69], v206, s[48:51], s67 offen sc1\n"
"s_add_u32 s67, s67, 73728\n"
"buffer_load_dwordx2 v[70:71], v206, s[48:51], s67 offen sc1\n"
"s_add_u32 s67, s67, 73728\n"
"buffer_load_dwordx2 v[72:73], v206, s[48:51], s67 offen sc1\n"
"s_add_u32 s67, s67, 73728\n"
"buffer_load_dwordx2 v[74:75], v206, s[48:51], s67 offen sc1\n"
"s_add_u32 s67, s67, 73728\n"
"buffer_load_dwordx2 v[76:77], v206, s[48:51], s67 offen sc1\n"
"s_add_u32 s67, s67, 73728\n"
"buffer_load_dwordx2 v[78:79], v206, s[48:51], s67 offen sc1\n"
"v_mov_b32 v220, 0\n"
"v_mov_b32 v221, 0\n"
"v_mov_b32 v222, 0\n"
"v_mov_b32 v223, 0\n"
"v_mov_b32 v224, 0\n"
"v_mov_b32 v225, 0\n"
"v_mov_b32 v226, 0\n"
"v_mov_b32 v227, 0\n"
"s_waitcnt vmcnt(23)\n"
"v_dot2_f32_bf16 v220, v0, s96, v220\n"
"v_dot2_f32_bf16 v221, v0, s97, v221\n"
"v_dot2_f32_bf16 v222, v1, s96, v222\n"
"v_dot2_f32_bf16 v223, v1, s97, v223\n"
"v_dot2_f32_bf16 v224, v2, s96, v224\n"
"v_dot2_f32_bf16 v225, v2, s97, v225\n"
"v_dot2_f32_bf16 v226, v3, s96, v226\n"
"v_dot2_f32_bf16 v227, v3, s97, v227\n"
"s_waitcnt vmcnt(22)\n"
"v_dot2_f32_bf16 v220, v4, s96, v220\n"
"v_dot2_f32_bf16 v221, v4, s97, v221\n"
"v_dot2_f32_bf16 v222, v5, s96, v222\n"
"v_dot2_f32_bf16 v223, v5, s97, v223\n"
"v_dot2_f32_bf16 v224, v6, s96, v224\n"
"v_dot2_f32_bf16 v225, v6, s97, v225\n"
"v_dot2_f32_bf16 v226, v7, s96, v226\n"
"v_dot2_f32_bf16 v227, v7, s97, v227\n"
"s_waitcnt vmcnt(21)\n"
"v_dot2_f32_bf16 v220, v8, s96, v220\n"
"v_dot2_f32_bf16 v221, v8, s97, v221\n"
"v_dot2_f32_bf16 v222, v9, s96, v222\n"
"v_dot2_f32_bf16 v223, v9, s97, v223\n"
"v_dot2_f32_bf16 v224, v10, s96, v224\n"
"v_dot2_f32_bf16 v225, v10, s97, v225\n"
"v_dot2_f32_bf16 v226, v11, s96, v226\n"
"v_dot2_f32_bf16 v227, v11, s97, v227\n"
"s_waitcnt vmcnt(20)\n"
"v_dot2_f32_bf16 v220, v12, s96, v220\n"
"v_dot2_f32_bf16 v221, v12, s97, v221\n"
"v_dot2_f32_bf16 v222, v13, s96, v222\n"
"v_dot2_f32_bf16 v223, v13, s97, v223\n"
"v_dot2_f32_bf16 v224, v14, s96, v224\n"
"v_dot2_f32_bf16 v225, v14, s97, v225\n"
"v_dot2_f32_bf16 v226, v15, s96, v226\n"
"v_dot2_f32_bf16 v227, v15, s97, v227\n"
"s_waitcnt vmcnt(19)\n"
"v_dot2_f32_bf16 v220, v16, s96, v220\n"
"v_dot2_f32_bf16 v221, v16, s97, v221\n"
"v_dot2_f32_bf16 v222, v17, s96, v222\n"
"v_dot2_f32_bf16 v223, v17, s97, v223\n"
"v_dot2_f32_bf16 v224, v18, s96, v224\n"
"v_dot2_f32_bf16 v225, v18, s97, v225\n"
"v_dot2_f32_bf16 v226, v19, s96, v226\n"
"v_dot2_f32_bf16 v227, v19, s97, v227\n"
"s_waitcnt vmcnt(18)\n"
"v_dot2_f32_bf16 v220, v20, s96, v220\n"
"v_dot2_f32_bf16 v221, v20, s97, v221\n"
"v_dot2_f32_bf16 v222, v21, s96, v222\n"
"v_dot2_f32_bf16 v223, v21, s97, v223\n"
"v_dot2_f32_bf16 v224, v22, s96, v224\n"
"v_dot2_f32_bf16 v225, v22, s97, v225\n"
"v_dot2_f32_bf16 v226, v23, s96, v226\n"
"v_dot2_f32_bf16 v227, v23, s97, v227\n"
"s_waitcnt vmcnt(17)\n"
"v_dot2_f32_bf16 v220, v24, s96, v220\n"
"v_dot2_f32_bf16 v221, v24, s97, v221\n"
"v_dot2_f32_bf16 v222, v25, s96, v222\n"
"v_dot2_f32_bf16 v223, v25, s97, v223\n"
"v_dot2_f32_bf16 v224, v26, s96, v224\n"
"v_dot2_f32_bf16 v225, v26, s97, v225\n"
"v_dot2_f32_bf16 v226, v27, s96, v226\n"
"v_dot2_f32_bf16 v227, v27, s97, v227\n"
"s_waitcnt vmcnt(16)\n"
"v_dot2_f32_bf16 v220, v28, s96, v220\n"
"v_dot2_f32_bf16 v221, v28, s97, v221\n"
"v_dot2_f32_bf16 v222, v29, s96, v222\n"
"v_dot2_f32_bf16 v223, v29, s97, v223\n"
"v_dot2_f32_bf16 v224, v30, s96, v224\n"
"v_dot2_f32_bf16 v225, v30, s97, v225\n"
"v_dot2_f32_bf16 v226, v31, s96, v226\n"
"v_dot2_f32_bf16 v227, v31, s97, v227\n"
"v_permlane16_swap_b32 v220, v224\n"
"v_permlane16_swap_b32 v221, v225\n"
"v_permlane16_swap_b32 v222, v226\n"
"v_permlane16_swap_b32 v223, v227\n"
"v_lshlrev_b32 v228, 16, v208\n"
"v_and_b32 v229, 4294901760, v208\n"
"v_fma_f32 v220, v220, s98, v228\n"
"v_fma_f32 v221, v221, s98, v229\n"
"v_cvt_pk_bf16_f32 v232, v220, v221\n"
"v_lshlrev_b32 v228, 16, v209\n"
"v_and_b32 v229, 4294901760, v209\n"
"v_fma_f32 v222, v222, s98, v228\n"
"v_fma_f32 v223, v223, s98, v229\n"
"v_cvt_pk_bf16_f32 v233, v222, v223\n"
"v_lshlrev_b32 v228, 16, v210\n"
"v_and_b32 v229, 4294901760, v210\n"
"v_fma_f32 v224, v224, s98, v228\n"
"v_fma_f32 v225, v225, s98, v229\n"
"v_cvt_pk_bf16_f32 v234, v224, v225\n"
"v_lshlrev_b32 v228, 16, v211\n"
"v_and_b32 v229, 4294901760, v211\n"
"v_fma_f32 v226, v226, s98, v228\n"
"v_fma_f32 v227, v227, s98, v229\n"
"v_cvt_pk_bf16_f32 v235, v226, v227\n"
"s_add_u32 s68, s8, 0\n"
"buffer_store_dwordx4 v[232:235], v203, s[56:59], s68 offen\n"
"v_mov_b32 v220, 0\n"
"v_mov_b32 v221, 0\n"
"v_mov_b32 v222, 0\n"
"v_mov_b32 v223, 0\n"
"v_mov_b32 v224, 0\n"
"v_mov_b32 v225, 0\n"
"v_mov_b32 v226, 0\n"
"v_mov_b32 v227, 0\n"
"s_waitcnt vmcnt(16)\n"
"v_dot2_f32_bf16 v220, v32, s96, v220\n"
"v_dot2_f32_bf16 v221, v32, s97, v221\n"
"v_dot2_f32_bf16 v222, v33, s96, v222\n"
"v_dot2_f32_bf16 v223, v33, s97, v223\n"
"v_dot2_f32_bf16 v224, v34, s96, v224\n"
"v_dot2_f32_bf16 v225, v34, s97, v225\n"
"v_dot2_f32_bf16 v226, v35, s96, v226\n"
"v_dot2_f32_bf16 v227, v35, s97, v227\n"
"s_waitcnt vmcnt(15)\n"
"v_dot2_f32_bf16 v220, v36, s96, v220\n"
"v_dot2_f32_bf16 v221, v36, s97, v221\n"
"v_dot2_f32_bf16 v222, v37, s96, v222\n"
"v_dot2_f32_bf16 v223, v37, s97, v223\n"
"v_dot2_f32_bf16 v224, v38, s96, v224\n"
"v_dot2_f32_bf16 v225, v38, s97, v225\n"
"v_dot2_f32_bf16 v226, v39, s96, v226\n"
"v_dot2_f32_bf16 v227, v39, s97, v227\n"
"s_waitcnt vmcnt(14)\n"
"v_dot2_f32_bf16 v220, v40, s96, v220\n"
"v_dot2_f32_bf16 v221, v40, s97, v221\n"
"v_dot2_f32_bf16 v222, v41, s96, v222\n"
"v_dot2_f32_bf16 v223, v41, s97, v223\n"
"v_dot2_f32_bf16 v224, v42, s96, v224\n"
"v_dot2_f32_bf16 v225, v42, s97, v225\n"
"v_dot2_f32_bf16 v226, v43, s96, v226\n"
"v_dot2_f32_bf16 v227, v43, s97, v227\n"
"s_waitcnt vmcnt(13)\n"
"v_dot2_f32_bf16 v220, v44, s96, v220\n"
"v_dot2_f32_bf16 v221, v44, s97, v221\n"
"v_dot2_f32_bf16 v222, v45, s96, v222\n"
"v_dot2_f32_bf16 v223, v45, s97, v223\n"
"v_dot2_f32_bf16 v224, v46, s96, v224\n"
"v_dot2_f32_bf16 v225, v46, s97, v225\n"
"v_dot2_f32_bf16 v226, v47, s96, v226\n"
"v_dot2_f32_bf16 v227, v47, s97, v227\n"
"s_waitcnt vmcnt(12)\n"
"v_dot2_f32_bf16 v220, v48, s96, v220\n"
"v_dot2_f32_bf16 v221, v48, s97, v221\n"
"v_dot2_f32_bf16 v222, v49, s96, v222\n"
"v_dot2_f32_bf16 v223, v49, s97, v223\n"
"v_dot2_f32_bf16 v224, v50, s96, v224\n"
"v_dot2_f32_bf16 v225, v50, s97, v225\n"
"v_dot2_f32_bf16 v226, v51, s96, v226\n"
"v_dot2_f32_bf16 v227, v51, s97, v227\n"
"s_waitcnt vmcnt(11)\n"
"v_dot2_f32_bf16 v220, v52, s96, v220\n"
"v_dot2_f32_bf16 v221, v52, s97, v221\n"
"v_dot2_f32_bf16 v222, v53, s96, v222\n"
"v_dot2_f32_bf16 v223, v53, s97, v223\n"
"v_dot2_f32_bf16 v224, v54, s96, v224\n"
"v_dot2_f32_bf16 v225, v54, s97, v225\n"
"v_dot2_f32_bf16 v226, v55, s96, v226\n"
"v_dot2_f32_bf16 v227, v55, s97, v227\n"
"s_waitcnt vmcnt(10)\n"
"v_dot2_f32_bf16 v220, v56, s96, v220\n"
"v_dot2_f32_bf16 v221, v56, s97, v221\n"
"v_dot2_f32_bf16 v222, v57, s96, v222\n"
"v_dot2_f32_bf16 v223, v57, s97, v223\n"
"v_dot2_f32_bf16 v224, v58, s96, v224\n"
"v_dot2_f32_bf16 v225, v58, s97, v225\n"
"v_dot2_f32_bf16 v226, v59, s96, v226\n"
"v_dot2_f32_bf16 v227, v59, s97, v227\n"
"s_waitcnt vmcnt(9)\n"
"v_dot2_f32_bf16 v220, v60, s96, v220\n"
"v_dot2_f32_bf16 v221, v60, s97, v221\n"
"v_dot2_f32_bf16 v222, v61, s96, v222\n"
"v_dot2_f32_bf16 v223, v61, s97, v223\n"
"v_dot2_f32_bf16 v224, v62, s96, v224\n"
"v_dot2_f32_bf16 v225, v62, s97, v225\n"
"v_dot2_f32_bf16 v226, v63, s96, v226\n"
"v_dot2_f32_bf16 v227, v63, s97, v227\n"
"v_permlane16_swap_b32 v220, v224\n"
"v_permlane16_swap_b32 v221, v225\n"
"v_permlane16_swap_b32 v222, v226\n"
"v_permlane16_swap_b32 v223, v227\n"
"v_lshlrev_b32 v228, 16, v212\n"
"v_and_b32 v229, 4294901760, v212\n"
"v_fma_f32 v220, v220, s98, v228\n"
"v_fma_f32 v221, v221, s98, v229\n"
"v_cvt_pk_bf16_f32 v232, v220, v221\n"
"v_lshlrev_b32 v228, 16, v213\n"
"v_and_b32 v229, 4294901760, v213\n"
"v_fma_f32 v222, v222, s98, v228\n"
"v_fma_f32 v223, v223, s98, v229\n"
"v_cvt_pk_bf16_f32 v233, v222, v223\n"
"v_lshlrev_b32 v228, 16, v214\n"
"v_and_b32 v229, 4294901760, v214\n"
"v_fma_f32 v224, v224, s98, v228\n"
"v_fma_f32 v225, v225, s98, v229\n"
"v_cvt_pk_bf16_f32 v234, v224, v225\n"
"v_lshlrev_b32 v228, 16, v215\n"
"v_and_b32 v229, 4294901760, v215\n"
"v_fma_f32 v226, v226, s98, v228\n"
"v_fma_f32 v227, v227, s98, v229\n"
"v_cvt_pk_bf16_f32 v235, v226, v227\n"
"s_add_u32 s68, s8, 64\n"
"buffer_store_dwordx4 v[232:235], v203, s[56:59], s68 offen\n"
"v_mov_b32 v220, 0\n"
"v_mov_b32 v221, 0\n"
"v_mov_b32 v222, 0\n"
"v_mov_b32 v223, 0\n"
"s_waitcnt vmcnt(9)\n"
"v_dot2_f32_bf16 v220, v64, s96, v220\n"
"v_dot2_f32_bf16 v221, v64, s97, v221\n"
"v_dot2_f32_bf16 v222, v65, s96, v222\n"
"v_dot2_f32_bf16 v223, v65, s97, v223\n"
"s_waitcnt vmcnt(8)\n"
"v_dot2_f32_bf16 v220, v66, s96, v220\n"
"v_dot2_f32_bf16 v221, v66, s97, v221\n"
"v_dot2_f32_bf16 v222, v67, s96, v222\n"
"v_dot2_f32_bf16 v223, v67, s97, v223\n"
"s_waitcnt vmcnt(7)\n"
"v_dot2_f32_bf16 v220, v68, s96, v220\n"
"v_dot2_f32_bf16 v221, v68, s97, v221\n"
"v_dot2_f32_bf16 v222, v69, s96, v222\n"
"v_dot2_f32_bf16 v223, v69, s97, v223\n"
"s_waitcnt vmcnt(6)\n"
"v_dot2_f32_bf16 v220, v70, s96, v220\n"
"v_dot2_f32_bf16 v221, v70, s97, v221\n"
"v_dot2_f32_bf16 v222, v71, s96, v222\n"
"v_dot2_f32_bf16 v223, v71, s97, v223\n"
"s_waitcnt vmcnt(5)\n"
"v_dot2_f32_bf16 v220, v72, s96, v220\n"
"v_dot2_f32_bf16 v221, v72, s97, v221\n"
"v_dot2_f32_bf16 v222, v73, s96, v222\n"
"v_dot2_f32_bf16 v223, v73, s97, v223\n"
"s_waitcnt vmcnt(4)\n"
"v_dot2_f32_bf16 v220, v74, s96, v220\n"
"v_dot2_f32_bf16 v221, v74, s97, v221\n"
"v_dot2_f32_bf16 v222, v75, s96, v222\n"
"v_dot2_f32_bf16 v223, v75, s97, v223\n"
"s_waitcnt vmcnt(3)\n"
"v_dot2_f32_bf16 v220, v76, s96, v220\n"
"v_dot2_f32_bf16 v221, v76, s97, v221\n"
"v_dot2_f32_bf16 v222, v77, s96, v222\n"
"v_dot2_f32_bf16 v223, v77, s97, v223\n"
"s_waitcnt vmcnt(2)\n"
"v_dot2_f32_bf16 v220, v78, s96, v220\n"
"v_dot2_f32_bf16 v221, v78, s97, v221\n"
"v_dot2_f32_bf16 v222, v79, s96, v222\n"
"v_dot2_f32_bf16 v223, v79, s97, v223\n"
"v_lshlrev_b32 v228, 16, v216\n"
"v_and_b32 v229, 4294901760, v216\n"
"v_fma_f32 v220, v220, s98, v228\n"
"v_fma_f32 v221, v221, s98, v229\n"
"v_cvt_pk_bf16_f32 v236, v220, v221\n"
"v_lshlrev_b32 v228, 16, v217\n"
"v_and_b32 v229, 4294901760, v217\n"
"v_fma_f32 v222, v222, s98, v228\n"
"v_fma_f32 v223, v223, s98, v229\n"
"v_cvt_pk_bf16_f32 v237, v222, v223\n"
"buffer_store_dwordx2 v[236:237], v204, s[56:59], 0 offen\n"
"s_waitcnt vmcnt(0) lgkmcnt(0)\n"
"s_branch Lexit_%=\n"
"Lslow1_%=:\n"
"s_mov_b32 s79, 0\n"
"Lspin1_%=:\n"
"s_sleep 1\n"
"buffer_load_dword v178, off, s[48:51], s19 sc1\n"
"s_waitcnt vmcnt(0)\n"
"v_readfirstlane_b32 s18, v178\n"
"s_nop 3\n"
"s_cmp_ge_u32 s18, 16\n"
"s_cbranch_scc1 Lback1_%=\n"
"s_add_u32 s79, s79, 1\n"
"s_cmp_lt_u32 s79, 0x40000\n"
"s_cbranch_scc1 Lspin1_%=\n"
"s_branch Lback1_%=\n"
"Lslow3_%=:\n"
"s_mov_b32 s79, 0\n"
"Lspin3_%=:\n"
"s_sleep 1\n"
"buffer_load_dword v178, off, s[48:51], s19 sc1\n"
"s_waitcnt vmcnt(0)\n"
"v_readfirstlane_b32 s18, v178\n"
"s_nop 3\n"
"s_cmp_ge_u32 s18, 16\n"
"s_cbranch_scc1 Lback3_%=\n"
"s_add_u32 s79, s79, 1\n"
"s_cmp_lt_u32 s79, 0x40000\n"
"s_cbranch_scc1 Lspin3_%=\n"
"s_branch Lback3_%=\n"
"Lslow5_%=:\n"
"s_mov_b32 s79, 0\n"
"Lspin5_%=:\n"
"s_sleep 1\n"
"buffer_load_dword v178, off, s[48:51], s19 sc1\n"
"s_waitcnt vmcnt(0)\n"
"v_readfirstlane_b32 s18, v178\n"
"s_nop 3\n"
"s_cmp_ge_u32 s18, 1\n"
"s_cbranch_scc1 Lback5_%=\n"
"s_add_u32 s79, s79, 1\n"
"s_cmp_lt_u32 s79, 0x40000\n"
"s_cbranch_scc1 Lspin5_%=\n"
"s_branch Lback5_%=\n"
"Lslow7_%=:\n"
"s_mov_b32 s79, 0\n"
"Lspin7_%=:\n"
"s_sleep 1\n"
"buffer_load_dword v178, off, s[48:51], s19 sc1\n"
"s_waitcnt vmcnt(0)\n"
"v_readfirstlane_b32 s18, v178\n"
"s_nop 3\n"
"s_cmp_ge_u32 s18, 1\n"
"s_cbranch_scc1 Lback7_%=\n"
"s_add_u32 s79, s79, 1\n"
"s_cmp_lt_u32 s79, 0x40000\n"
"s_cbranch_scc1 Lspin7_%=\n"
"s_branch Lback7_%=\n"
"Lslow9_%=:\n"
"s_mov_b32 s79, 0\n"
"Lspin9_%=:\n"
"s_sleep 1\n"
"buffer_load_dword v178, off, s[48:51], s19 sc1\n"
"s_waitcnt vmcnt(0)\n"
"v_readfirstlane_b32 s18, v178\n"
"s_nop 3\n"
"s_cmp_ge_u32 s18, 8\n"
"s_cbranch_scc1 Lback9_%=\n"
"s_add_u32 s79, s79, 1\n"
"s_cmp_lt_u32 s79, 0x40000\n"
"s_cbranch_scc1 Lspin9_%=\n"
"s_branch Lback9_%=\n"
"Lexit_%=:\n"
      :
      : [karg] "s"(karg), [wave] "s"(wave), [bid] "s"(bid), [lds] "s"(lds)
      : "v0","v1","v2","v3","v4","v5","v6","v7","v8","v9","v10","v11","v12","v13","v14","v15","v16","v17","v18","v19","v20","v21","v22","v23","v24","v25","v26","v27","v28","v29","v30","v31","v32","v33","v34","v35","v36","v37","v38","v39","v40","v41","v42","v43","v44","v45","v46","v47","v48","v49","v50","v51","v52","v53","v54","v55","v56","v57","v58","v59","v60","v61","v62","v63","v64","v65","v66","v67","v68","v69","v70","v71","v72","v73","v74","v75","v76","v77","v78","v79","v80","v81","v82","v83","v84","v85","v86","v87","v88","v89","v90","v91","v92","v93","v94","v95","v96","v97","v98","v99","v100","v101","v102","v103","v104","v105","v106","v107","v108","v109","v110","v111","v112","v113","v114","v115","v116","v117","v118","v119","v120","v121","v122","v123","v124","v125","v126","v127","v128","v129","v130","v131","v132","v133","v134","v135","v136","v137","v138","v139","v140","v141","v142","v143","v144","v145","v146","v147","v148","v149","v150","v151","v152","v153","v154","v155","v156","v157","v158","v159","v160","v161","v162","v163","v164","v165","v166","v167","v168","v169","v170","v171","v172","v173","v174","v175","v176","v177","v178","v179","v180","v181","v182","v183","v184","v185","v186","v187","v188","v189","v190","v191","v192","v193","v194","v195","v196","v197","v198","v199","v200","v201","v202","v203","v204","v205","v206","v207","v208","v209","v210","v211","v212","v213","v214","v215","v216","v217","v218","v219","v220","v221","v222","v223","v224","v225","v226","v227","v228","v229","v230","v231","v232","v233","v234","v235","v236","v237","v238","v239","v240","v241","v242","v243","v244","v245","v246","v247","v248","v249","v250","v251","v252","v253","v254","v255","a0","a1","a2","a3","a4","a5","a6","a7","a8","a9","a10","a11","a12","a13","a14","a15","a16","a17","a18","a19","a20","a21","a22","a23","a24","a25","a26","a27","a28","a29","a30","a31","a32","a33","a34","a35","a36","a37","a38","a39","a40","a41","a42","a43","a44","a45","a46","a47","a48","a49","a50","a51","a52","a53","a54","a55","a56","a57","a58","a59","a60","a61","a62","a63","a64","a65","a66","a67","a68","a69","a70","a71","a72","a73","a74","a75","a76","a77","a78","a79","a80","a81","a82","a83","a84","a85","a86","a87","a88","a89","a90","a91","a92","a93","a94","a95","a96","a97","a98","a99","a100","a101","a102","a103","a104","a105","a106","a107","a108","a109","a110","a111","a112","a113","a114","a115","a116","a117","a118","a119","a120","a121","a122","a123","a124","a125","a126","a127","a128","a129","a130","a131","a132","a133","a134","a135","a136","a137","a138","a139","a140","a141","a142","a143","a144","a145","a146","a147","a148","a149","a150","a151","a152","a153","a154","a155","a156","a157","a158","a159","a160","a161","a162","a163","a164","a165","a166","a167","a168","a169","a170","a171","a172","a173","a174","a175","a176","a177","a178","a179","a180","a181","a182","a183","a184","a185","a186","a187","a188","a189","a190","a191","a192","a193","a194","a195","a196","a197","a198","a199","a200","a201","a202","a203","a204","a205","a206","a207","a208","a209","a210","a211","a212","a213","a214","a215","a216","a217","a218","a219","a220","a221","a222","a223","a224","a225","a226","a227","a228","a229","a230","a231","a232","a233","a234","a235","a236","a237","a238","a239","a240","a241","a242","a243","a244","a245","a246","a247","a248","a249","a250","a251","a252","a253","a254","a255","s5","s6","s7","s8","s9","s10","s11","s12","s13","s14","s15","s16","s17","s18","s19","s20","s21","s22","s23","s24","s25","s26","s27","s28","s29","s30","s31","s36","s37","s38","s39","s40","s41","s42","s43","s44","s45","s46","s47","s48","s49","s50","s51","s52","s53","s54","s55","s56","s57","s58","s59","s60","s61","s62","s63","s64","s65","s66","s67","s68","s69","s70","s71","s72","s73","s74","s75","s76","s77","s78","s79","s80","s81","s82","s83","s84","s85","s86","s87","s88","s89","s90","s91","s92","s93","s94","s95","s96","s97","s98","s99","vcc","scc","memory");
}

extern "C" void kernel_launch(void* const* d_in, const int* in_sizes, int n_in, void* d_out, int out_size, void* d_ws,
                              size_t ws_size, hipStream_t stream) {
  fused_kernel<<<256, 256, 0, stream>>>(d_in[0], d_in[1], d_in[2], d_in[4], d_in[5], d_in[6], d_out, d_ws);
}
```
